```python
import torch

def _softmax(x, axis=-1):
    return torch.softmax(x, dim=axis)
def _gelu(x):
    return torch.nn.functional.gelu(x, approximate="tanh")
def _silu(x):
    return torch.nn.functional.silu(x)
def _sigmoid(x):
    return torch.sigmoid(x)
def _layernorm(x, axis=-1, eps=1e-5):
    m = x.mean(dim=axis, keepdim=True)
    v = x.var(dim=axis, keepdim=True, unbiased=False)
    return (x - m) / torch.sqrt(v + eps)
def _scatter_add(tgt, idx, src, axis=0):
    return tgt.clone().index_add_(axis, idx, src)
def _segment_sum(src, seg, n):
    out = torch.zeros((n,) + src.shape[1:], dtype=src.dtype)
    return out.index_add_(0, seg, src)
def _pad(x, pw):
    flat = tuple(v for pair in reversed(pw) for v in pair)
    return torch.nn.functional.pad(x, flat)

kernel_name = 'interleave_layers__sliding_local_dilated__swiglu_ffn__prompt'

def setup_inputs():

    _w = (lambda a: (lambda t: t.to(torch.bfloat16) if t.is_floating_point() else t)(torch.as_tensor(a)))
    def _A_setup():
        B, S, D = 1, 2048, 576
        return {
            "x": _w((lambda *s: torch.randn(*s, dtype=torch.bfloat16) * 0.05)(B, S, D)),
            "wqkv": _w((lambda *s: torch.randn(*s, dtype=torch.bfloat16) * 0.05)(D, 3 * D)),
            "wo": _w((lambda *s: torch.randn(*s, dtype=torch.bfloat16) * 0.05)(D, D)),
        }
    _A_in = _A_setup()

    def _B_setup():
        B, S, D, F = 1, 2048, 576, 4608
        return {
            "x": _w((lambda *s: torch.randn(*s, dtype=torch.bfloat16) * 0.05)(B, S, D)),
            "w_gate": _w((lambda *s: torch.randn(*s, dtype=torch.bfloat16) * 0.05)(D, F)),
            "w_up": _w((lambda *s: torch.randn(*s, dtype=torch.bfloat16) * 0.05)(D, F)),
            "w_down": _w((lambda *s: torch.randn(*s, dtype=torch.bfloat16) * 0.05)(F, D)),
        }
    _B_in = _B_setup()

    return {
        "A_x": _A_in["x"],
        "A_wqkv": _A_in["wqkv"],
        "A_wo": _A_in["wo"],
        "B_x": _B_in["x"],
        "B_w_gate": _B_in["w_gate"],
        "B_w_up": _B_in["w_up"],
        "B_w_down": _B_in["w_down"],
    }

def reference(A_x, A_wqkv, A_wo, B_x, B_w_gate, B_w_up, B_w_down):
    def _A_ref(x, wqkv, wo):
        B, S, D = x.shape
        H = 32; Dh = D // H
        W, dil = 8, 2
        qkv = (lambda x, *d: x.permute(*d))((x @ wqkv).reshape(B, S, 3, H, Dh), 2, 0, 3, 1, 4)
        q, k, v = qkv[0], qkv[1], qkv[2]
        scale = Dh ** -0.5
        pos = torch.arange(S)
        off = torch.arange(W) * dil
        idx = pos[:, None] - off[None, :]
        mask = idx >= 0
        idx_c = torch.where(mask, idx, 0).reshape(-1)
        kg = k[:, :, idx_c, :].reshape(B, H, S, W, Dh)
        vg = v[:, :, idx_c, :].reshape(B, H, S, W, Dh)
        logits = torch.einsum("bhsd,bhswd->bhsw", q, kg) * scale
        logits = torch.where(mask[None, None, :, :], logits, -1e9)
        attn = _softmax(logits, axis=-1)
        o = torch.einsum("bhsw,bhswd->bhsd", attn, vg)
        return (lambda x, *d: x.permute(*d))(o, 0, 2, 1, 3).reshape(B, S, D) @ wo

    def _B_ref(x, w_gate, w_up, w_down):
        g = _silu(x @ w_gate)
        u = x @ w_up
        return (g * u) @ w_down

    x = A_x
    x = x + _A_ref(_layernorm(x, axis=-1), A_wqkv, A_wo)
    x = x + _B_ref(_layernorm(x, axis=-1), B_w_gate, B_w_up, B_w_down)
    x = x + _A_ref(_layernorm(x, axis=-1), A_wqkv, A_wo)
    x = x + _B_ref(_layernorm(x, axis=-1), B_w_gate, B_w_up, B_w_down)
    return x
```

```cpp
#include <hip/hip_runtime.h>
#include <hip/hip_bf16.h>
#include <stdint.h>

typedef unsigned short bf16_t;
typedef short bf16x8 __attribute__((ext_vector_type(8)));
typedef short bf16x4 __attribute__((ext_vector_type(4)));
typedef float f32x4 __attribute__((ext_vector_type(4)));
typedef float f32x2 __attribute__((ext_vector_type(2)));
typedef unsigned u32x4 __attribute__((ext_vector_type(4)));
typedef unsigned u32x2 __attribute__((ext_vector_type(2)));
#define LAS __attribute__((address_space(3)))
#define DEVI __device__ __forceinline__

constexpr int S_ = 2048, D_ = 576, F_ = 4608, NH_ = 32, DH_ = 18;
constexpr float LN_EPS = 1e-5f;

DEVI float bf2f(unsigned b) { return __uint_as_float(b << 16); }
DEVI float bflo(unsigned w) { return __uint_as_float(w << 16); }
DEVI float bfhi(unsigned w) { return __uint_as_float(w & 0xffff0000u); }
DEVI unsigned pk2(float lo, float hi) {
  typedef __bf16 bf2v __attribute__((ext_vector_type(2)));
  bf2v v; v.x = (__bf16)lo; v.y = (__bf16)hi;
  return __builtin_bit_cast(unsigned, v);
}
DEVI float rbf(float x) { return (float)((__bf16)x); }

#define WAIT_V(n) asm volatile("s_waitcnt vmcnt(" #n ")" ::: "memory")
#define WAIT_L0() asm volatile("s_waitcnt lgkmcnt(0)" ::: "memory")
#define BAR() do { asm volatile("" ::: "memory"); __builtin_amdgcn_s_barrier(); asm volatile("" ::: "memory"); } while (0)

DEVI void glds16(const char* sbase, unsigned voff, unsigned ldsaddr) {
  asm volatile("s_mov_b32 m0, %2\n\ts_nop 0\n\tglobal_load_lds_dwordx4 %0, %1" ::"v"(voff), "s"(sbase), "s"(ldsaddr) : "memory");
}
DEVI unsigned ldsa(const LAS char* p) { return (unsigned)(size_t)p; }
DEVI bf16x8 lds_b128(const LAS char* p) { return *(const LAS bf16x8*)p; }
DEVI bf16x8 lds_tr2(const LAS char* p0, const LAS char* p1) {
  bf16x4 a = __builtin_amdgcn_ds_read_tr16_b64_v4i16((LAS bf16x4*)p0);
  bf16x4 b = __builtin_amdgcn_ds_read_tr16_b64_v4i16((LAS bf16x4*)p1);
  return __builtin_shufflevector(a, b, 0, 1, 2, 3, 4, 5, 6, 7);
}
DEVI f32x4 mfma16(bf16x8 a, bf16x8 b, f32x4 c) { return __builtin_amdgcn_mfma_f32_16x16x32_bf16(a, b, c, 0, 0, 0); }

__global__ __launch_bounds__(256) void k0_colsum(const bf16_t* __restrict__ Wg, const bf16_t* __restrict__ Wu, float* __restrict__ csum) {
  __shared__ float red[32][64 + 1];
  const int b = blockIdx.x;
  const bf16_t* W = (b < 72) ? Wg : Wu;
  const int col0 = (b % 72) * 64;
  const int ch = threadIdx.x & 7, rg = threadIdx.x >> 3;
  float s[8] = {0, 0, 0, 0, 0, 0, 0, 0};
#pragma unroll
  for (int i = 0; i < 18; ++i) {
    const u32x4 v = *(const u32x4*)(W + (size_t)(rg * 18 + i) * F_ + col0 + ch * 8);
    s[0] += bflo(v.x); s[1] += bfhi(v.x); s[2] += bflo(v.y); s[3] += bfhi(v.y);
    s[4] += bflo(v.z); s[5] += bfhi(v.z); s[6] += bflo(v.w); s[7] += bfhi(v.w);
  }
#pragma unroll
  for (int j = 0; j < 8; ++j) red[rg][ch * 8 + j] = s[j];
  __syncthreads();
  if (threadIdx.x < 64) {
    float t = 0.f;
#pragma unroll
    for (int r = 0; r < 32; ++r) t += red[r][threadIdx.x];
    csum[(b < 72 ? 0 : F_) + col0 + threadIdx.x] = t;
  }
}

constexpr int K1_ROWS = 80;
constexpr int K1_XBYTES = 9 * K1_ROWS * 128;
constexpr int K1_WSTG = 8 * 14 * 256;
constexpr int K1_LDS = K1_XBYTES + 2 * K1_WSTG;
constexpr int K1_QS = 220;

__global__ __launch_bounds__(512, 2) void k1_qkv_attn(const bf16_t* __restrict__ X, const bf16_t* __restrict__ Wqkv, bf16_t* __restrict__ O) {
  __shared__ __attribute__((aligned(16))) char lds_[K1_LDS];
  LAS char* lds = (LAS char*)lds_;
  const int tid = threadIdx.x, lane = tid & 63, wid = __builtin_amdgcn_readfirstlane(tid >> 6);
  const int bid = blockIdx.x, xcd = bid & 7, idx = bid >> 3;
  const int tt = (xcd >> 1) * 8 + (idx & 7), hg = (xcd & 1) * 4 + (idx >> 3);
  const int t0 = tt * 64 - 16;

  unsigned woff[4];
#pragma unroll
  for (int i = 0; i < 4; ++i) {
    int ci = i * 64 + lane; if (ci > 223) ci = 223;
    const int f = ci >> 4, rp = (ci >> 1) & 7, c = ci & 1;
    const int r = rp ^ ((wid & 1) * 4);
    int ch = f * 2 + c; if (ch > 26) ch = 26;
    const int seg = ch / 9, cc = ch - seg * 9;
    const int gcol = (seg == 2 ? 0 : (seg + 1) * D_) + hg * 72 + cc * 8;
    woff[i] = (unsigned)(((wid * 8 + r) * (3 * D_) + gcol) * 2);
  }
  auto stageW = [&](int kt, int buf) {
    const char* base = (const char*)Wqkv + (size_t)kt * 64 * (3 * D_) * 2;
    LAS char* dst = lds + K1_XBYTES + buf * K1_WSTG + wid * (14 * 256);
#pragma unroll
    for (int i = 0; i < 3; ++i) glds16(base, woff[i], ldsa(dst + i * 1024));
    if (lane < 32) glds16(base, woff[3], ldsa(dst + 3 * 1024));
  };
  stageW(0, 0);

  {
    const int sub = lane & 7;
#pragma unroll
    for (int pass = 0; pass < 2; ++pass) {
      const int row = pass * 64 + wid * 8 + (lane >> 3);
      if (pass == 0 || wid < 2) {
        int grow = t0 + row; if (grow < 0) grow = 0;
        const bf16_t* xr = X + (size_t)grow * D_;
        u32x4 v[9];
#pragma unroll
        for (int c = 0; c < 9; ++c) v[c] = *(const u32x4*)(xr + c * 64 + sub * 8);
        float s = 0.f;
#pragma unroll
        for (int c = 0; c < 9; ++c)
          s += (bflo(v[c].x) + bfhi(v[c].x)) + (bflo(v[c].y) + bfhi(v[c].y)) + (bflo(v[c].z) + bfhi(v[c].z)) + (bflo(v[c].w) + bfhi(v[c].w));
        s += __shfl_xor(s, 1); s += __shfl_xor(s, 2); s += __shfl_xor(s, 4);
        const float mean = s * (1.0f / D_);
        float q = 0.f;
#pragma unroll
        for (int c = 0; c < 9; ++c) {
          float d;
          d = bflo(v[c].x) - mean; q += d * d; d = bfhi(v[c].x) - mean; q += d * d;
          d = bflo(v[c].y) - mean; q += d * d; d = bfhi(v[c].y) - mean; q += d * d;
          d = bflo(v[c].z) - mean; q += d * d; d = bfhi(v[c].z) - mean; q += d * d;
          d = bflo(v[c].w) - mean; q += d * d; d = bfhi(v[c].w) - mean; q += d * d;
        }
        q += __shfl_xor(q, 1); q += __shfl_xor(q, 2); q += __shfl_xor(q, 4);
        const float rstd = rsqrtf(q * (1.0f / D_) + LN_EPS);
        LAS char* dst = lds + row * 128 + ((sub ^ ((row >> 1) & 7)) * 16);
#pragma unroll
        for (int c = 0; c < 9; ++c) {
          u32x4 w;
          w.x = pk2((bflo(v[c].x) - mean) * rstd, (bfhi(v[c].x) - mean) * rstd);
          w.y = pk2((bflo(v[c].y) - mean) * rstd, (bfhi(v[c].y) - mean) * rstd);
          w.z = pk2((bflo(v[c].z) - mean) * rstd, (bfhi(v[c].z) - mean) * rstd);
          w.w = pk2((bflo(v[c].w) - mean) * rstd, (bfhi(v[c].w) - mean) * rstd);
          *(LAS u32x4*)(dst + c * (K1_ROWS * 128)) = w;
        }
      }
    }
  }

  const int g = lane >> 4, m = lane & 15, q4 = (lane >> 2) & 3, p4 = lane & 3;
  const int xa0 = m * 128 + ((g ^ (m >> 1)) * 16);
  const int wa0 = (g * 14 + wid * 2) * 256 + ((q4 ^ ((g & 1) * 4)) * 32) + p4 * 8;
  f32x4 acc[5][2];
#pragma unroll
  for (int i = 0; i < 5; ++i) { acc[i][0] = (f32x4){0, 0, 0, 0}; acc[i][1] = (f32x4){0, 0, 0, 0}; }

  for (int kt = 0; kt < 9; ++kt) {
    WAIT_V(0); WAIT_L0(); BAR();
    if (kt + 1 < 9) stageW(kt + 1, (kt + 1) & 1);
    if (wid < 7) {
      const LAS char* xb = lds + kt * (K1_ROWS * 128);
      const LAS char* wb = lds + K1_XBYTES + (kt & 1) * K1_WSTG;
#pragma unroll
      for (int ks = 0; ks < 2; ++ks) {
        bf16x8 xf[5], wf[2];
#pragma unroll
        for (int i = 0; i < 5; ++i) xf[i] = lds_b128(xb + ((xa0 ^ (ks * 64)) + i * 2048));
#pragma unroll
        for (int j = 0; j < 2; ++j) wf[j] = lds_tr2(wb + (wa0 + ks * (4 * 14 * 256) + j * 256), wb + ((wa0 ^ 128) + ks * (4 * 14 * 256) + j * 256));
#pragma unroll
        for (int i = 0; i < 5; ++i)
#pragma unroll
          for (int j = 0; j < 2; ++j) acc[i][j] = mfma16(wf[j], xf[i], acc[i][j]);
      }
    }
  }
  WAIT_L0(); BAR();
  LAS float* qs = (LAS float*)lds;
  if (wid < 7) {
#pragma unroll
    for (int i = 0; i < 5; ++i)
#pragma unroll
      for (int j = 0; j < 2; ++j) {
        const int col = (wid * 2 + j) * 16 + g * 4;
        if (col < 216) {
          f32x4 v = acc[i][j];
          v[0] = rbf(v[0]); v[1] = rbf(v[1]); v[2] = rbf(v[2]); v[3] = rbf(v[3]);
          *(LAS f32x4*)(qs + (i * 16 + m) * K1_QS + col) = v;
        }
      }
  }
  WAIT_L0(); BAR();
  {
    const int half = tid & 1, hh = (tid >> 1) & 3, tok = tid >> 3;
    const int d0 = half * 10, nd2 = half ? 4 : 5;
    const int row = tok + 16;
    const int gpos = t0 + row;
    const LAS float* qp = qs + row * K1_QS + 144 + hh * 18 + d0;
    f32x2 qv[5];
#pragma unroll
    for (int d = 0; d < 5; ++d) qv[d] = (d < nd2) ? *(const LAS f32x2*)(qp + 2 * d) : (f32x2){0.f, 0.f};
    float lg[8];
#pragma unroll
    for (int w = 0; w < 8; ++w) {
      const LAS float* kp = qs + (row - 2 * w) * K1_QS + hh * 18 + d0;
      float s = 0.f;
#pragma unroll
      for (int d = 0; d < 5; ++d) if (d < 4 || !half) { const f32x2 kv = *(const LAS f32x2*)(kp + 2 * d); s += qv[d].x * kv.x + qv[d].y * kv.y; }
      s += __shfl_xor(s, 1);
      lg[w] = (gpos - 2 * w >= 0) ? s * 0.23570226039551584f : -1e30f;
    }
    float mx = lg[0];
#pragma unroll
    for (int w = 1; w < 8; ++w) mx = fmaxf(mx, lg[w]);
    float den = 0.f;
#pragma unroll
    for (int w = 0; w < 8; ++w) { lg[w] = (gpos - 2 * w >= 0) ? __expf(lg[w] - mx) : 0.f; den += lg[w]; }
    const float inv = 1.0f / den;
    f32x2 ov[5];
#pragma unroll
    for (int d = 0; d < 5; ++d) ov[d] = (f32x2){0.f, 0.f};
#pragma unroll
    for (int w = 0; w < 8; ++w) {
      const LAS float* vp = qs + (row - 2 * w) * K1_QS + 72 + hh * 18 + d0;
      const float pw = lg[w] * inv;
#pragma unroll
      for (int d = 0; d < 5; ++d) if (d < 4 || !half) { const f32x2 vv = *(const LAS f32x2*)(vp + 2 * d); ov[d].x += pw * vv.x; ov[d].y += pw * vv.y; }
    }
    unsigned* op = (unsigned*)(O + (size_t)gpos * D_ + hg * 72 + hh * 18 + d0);
#pragma unroll
    for (int d = 0; d < 5; ++d) if (d < 4 || !half) op[d] = pk2(ov[d].x, ov[d].y);
  }
}

template <int WM, int MI, int NKT, int NST, int EPI>
__global__ __launch_bounds__(WM * 128, 2) void gemm144(const bf16_t* __restrict__ X, int ldx, const bf16_t* __restrict__ W,
                                                        const bf16_t* __restrict__ R, bf16_t* __restrict__ Y, f32x2* __restrict__ stats, float* __restrict__ P) {
  constexpr int NW = WM * 2, BM = WM * MI * 16;
  constexpr int XB = BM * 128, WB = 18432, STG = XB + WB;
  constexpr int XI = BM / 8 / NW;
  constexpr int WCH = 1152 / NW;
  constexpr int WI = (WCH + 63) / 64;
  constexpr int NI = XI + WI;
  constexpr int RED = BM * 144 * 4;
  constexpr int LDSB = (NST * STG > RED) ? NST * STG : RED;
  __shared__ __attribute__((aligned(16))) char lds_[LDSB];
  LAS char* lds = (LAS char*)lds_;
  const int tid = threadIdx.x, lane = tid & 63, wid = __builtin_amdgcn_readfirstlane(tid >> 6);
  const int bid = blockIdx.x, xcd = bid & 7, idx = bid >> 3;
  int m0, n0, k0, split = 0;
  if (EPI == 1) {
    const int a = xcd >> 2, b = xcd & 3;
    m0 = a * 1024 + (idx >> 2) * 128; n0 = (idx & 3) * 144; k0 = b * 1152; split = b;
  } else {
    m0 = (xcd * 8 + (idx >> 2)) * 32; n0 = (idx & 3) * 144; k0 = 0;
  }
  const int wm = wid % WM, wk = wid / WM;

  unsigned xoff[XI], woff[WI];
#pragma unroll
  for (int i = 0; i < XI; ++i) {
    const int row = (wid * XI + i) * 8 + (lane >> 3);
    const int c = (lane & 7) ^ ((row >> 1) & 7);
    xoff[i] = (unsigned)(((m0 + row) * ldx + k0 + c * 8) * 2);
  }
#pragma unroll
  for (int i = 0; i < WI; ++i) {
    int ci = i * 64 + lane; if (ci > WCH - 1) ci = WCH - 1;
    ci += wid * WCH;
    const int s = ci >> 4, kb = s / 9, f = s - kb * 9, rp = (ci >> 1) & 7, c = ci & 1;
    const int r = rp ^ ((kb & 1) * 4);
    woff[i] = (unsigned)(((k0 + kb * 8 + r) * D_ + n0 + f * 16 + c * 8) * 2);
  }
  auto stage = [&](int kt, int buf) {
    const char* xb = (const char*)X + kt * 128;
    const char* wb = (const char*)W + (size_t)kt * 64 * D_ * 2;
    LAS char* dx = lds + buf * STG + wid * XI * 1024;
    LAS char* dw = lds + buf * STG + XB + wid * WCH * 16;
#pragma unroll
    for (int i = 0; i < XI; ++i) glds16(xb, xoff[i], ldsa(dx + i * 1024));
#pragma unroll
    for (int i = 0; i < WI; ++i) {
      if ((i + 1) * 64 <= WCH) glds16(wb, woff[i], ldsa(dw + i * 1024));
      else if (lane < WCH - i * 64) glds16(wb, woff[i], ldsa(dw + i * 1024));
    }
  };
#pragma unroll
  for (int s = 0; s < NST - 1; ++s) stage(s, s);

  const int g = lane >> 4, m = lane & 15, q4 = (lane >> 2) & 3, p4 = lane & 3;
  const int xa = (wm * MI * 16 + m) * 128 + (((wk * 4 + g) ^ (m >> 1)) * 16);
  const int wa = XB + ((wk * 4 + g) * 9) * 256 + ((q4 ^ ((g & 1) * 4)) * 32) + p4 * 8;
  f32x4 acc[MI][9];
#pragma unroll
  for (int i = 0; i < MI; ++i)
#pragma unroll
    for (int j = 0; j < 9; ++j) acc[i][j] = (f32x4){0, 0, 0, 0};

  int buf = 0;
  for (int kt = 0; kt < NKT; ++kt) {
    if (kt + NST - 2 < NKT) { asm volatile("s_waitcnt vmcnt(%0)" ::"n"((NST - 2) * NI) : "memory"); }
    else { WAIT_V(0); }
    WAIT_L0(); BAR();
    if (kt + NST - 1 < NKT) { int nb = buf + NST - 1; if (nb >= NST) nb -= NST; stage(kt + NST - 1, nb); }
    const LAS char* sb = lds + buf * STG;
    bf16x8 xf[MI], wf[9];
#pragma unroll
    for (int i = 0; i < MI; ++i) xf[i] = lds_b128(sb + xa + i * 2048);
#pragma unroll
    for (int j = 0; j < 9; ++j) wf[j] = lds_tr2(sb + wa + j * 256, sb + (wa ^ 128) + j * 256);
#pragma unroll
    for (int j = 0; j < 9; ++j)
#pragma unroll
      for (int i = 0; i < MI; ++i) acc[i][j] = mfma16(wf[j], xf[i], acc[i][j]);
    ++buf; if (buf == NST) buf = 0;
  }
  WAIT_L0(); BAR();
  LAS f32x4* red = (LAS f32x4*)lds;
  if (EPI == 1) {
#pragma unroll
    for (int i = 0; i < MI; ++i)
#pragma unroll
      for (int j = 0; j < 9; ++j)
        if ((j < 5) != (wk == 0)) red[((wm * MI + i) * 9 + j) * 64 + lane] = acc[i][j];
    WAIT_L0(); BAR();
#pragma unroll
    for (int i = 0; i < MI; ++i)
#pragma unroll
      for (int j = 0; j < 9; ++j)
        if ((j < 5) == (wk == 0)) {
          const f32x4 v = acc[i][j] + red[((wm * MI + i) * 9 + j) * 64 + lane];
          const int tok = m0 + (wm * MI + i) * 16 + m;
          *(f32x4*)(P + ((size_t)split * S_ + tok) * D_ + n0 + j * 16 + g * 4) = v;
        }
  } else {
    if (wk == 1) {
#pragma unroll
      for (int i = 0; i < MI; ++i)
#pragma unroll
        for (int j = 0; j < 9; ++j) red[((wm * MI + i) * 9 + j) * 64 + lane] = acc[i][j];
    }
    WAIT_L0(); BAR();
    if (wk == 0) {
#pragma unroll
      for (int i = 0; i < MI; ++i) {
        const int tok = m0 + (wm * MI + i) * 16 + m;
        float s = 0.f;
#pragma unroll
        for (int j = 0; j < 9; ++j) {
          f32x4 v = acc[i][j] + red[((wm * MI + i) * 9 + j) * 64 + lane];
          const u32x2 r = *(const u32x2*)(R + (size_t)tok * D_ + n0 + j * 16 + g * 4);
          v[0] = rbf(bflo(r.x) + rbf(v[0])); v[1] = rbf(bfhi(r.x) + rbf(v[1]));
          v[2] = rbf(bflo(r.y) + rbf(v[2])); v[3] = rbf(bfhi(r.y) + rbf(v[3]));
          acc[i][j] = v;
          u32x2 w; w.x = pk2(v[0], v[1]); w.y = pk2(v[2], v[3]);
          *(u32x2*)(Y + (size_t)tok * D_ + n0 + j * 16 + g * 4) = w;
          s += (v[0] + v[1]) + (v[2] + v[3]);
        }
        s += __shfl_xor(s, 16); s += __shfl_xor(s, 32);
        const float mean = s * (1.0f / 144.0f);
        float q = 0.f;
#pragma unroll
        for (int j = 0; j < 9; ++j) { const f32x4 d = acc[i][j] - mean; q += (d[0] * d[0] + d[1] * d[1]) + (d[2] * d[2] + d[3] * d[3]); }
        q += __shfl_xor(q, 16); q += __shfl_xor(q, 32);
        if (g == 0) stats[tok * 4 + (idx & 3)] = (f32x2){mean, q};
      }
    }
  }
}

constexpr int K3_XB = 32768, K3_UOFF = 18432 + 128, K3_WB = K3_UOFF + 18432, K3_STG = K3_XB + K3_WB;

__global__ __launch_bounds__(512, 2) void k3_gateup(const bf16_t* __restrict__ X, const bf16_t* __restrict__ Wg, const bf16_t* __restrict__ Wu,
                                                     const float* __restrict__ csum, const f32x2* __restrict__ stats, bf16_t* __restrict__ H) {
  __shared__ __attribute__((aligned(16))) char lds_[2 * K3_STG];
  LAS char* lds = (LAS char*)lds_;
  const int tid = threadIdx.x, lane = tid & 63, wid = __builtin_amdgcn_readfirstlane(tid >> 6);
  const int bid = blockIdx.x, xcd = bid & 7, idx = bid >> 3;
  const int pm = (xcd >> 2) * 4 + (idx & 3), pf = (xcd & 3) * 8 + (idx >> 2);
  const int wr = wid & 3, wc = wid >> 2;

  unsigned xoff[4], woff[5];
#pragma unroll
  for (int i = 0; i < 4; ++i) {
    const int row = (wid * 4 + i) * 8 + (lane >> 3);
    const int c = (lane & 7) ^ ((row >> 1) & 7);
    xoff[i] = (unsigned)(((pm * 256 + row) * D_ + c * 8) * 2);
  }
#pragma unroll
  for (int i = 0; i < 5; ++i) {
    int ci = i * 64 + lane; if (ci > 287) ci = 287;
    const int kk = ci / 18, cp = ci - kk * 18;
    const int k = (wid & 3) * 16 + kk;
    const int c = cp ^ ((k >> 3) & 1);
    woff[i] = (unsigned)((k * F_ + pf * 144 + c * 8) * 2);
  }
  const char* wsrc = (const char*)((wid >> 2) ? Wu : Wg);
  auto stage = [&](int kt, int buf) {
    const char* xb = (const char*)X + kt * 128;
    const char* wb = wsrc + (size_t)kt * 64 * F_ * 2;
    LAS char* dx = lds + buf * K3_STG + wid * 4096;
    LAS char* dw = lds + buf * K3_STG + K3_XB + (wid >> 2) * K3_UOFF + (wid & 3) * 4608;
#pragma unroll
    for (int i = 0; i < 4; ++i) glds16(xb, xoff[i], ldsa(dx + i * 1024));
#pragma unroll
    for (int i = 0; i < 4; ++i) glds16(wb, woff[i], ldsa(dw + i * 1024));
    if (lane < 32) glds16(wb, woff[4], ldsa(dw + 4096));
  };
  stage(0, 0);

  const int g = lane >> 4, m = lane & 15, q4 = (lane >> 2) & 3, p4 = lane & 3;
  const int xa = (wr * 64 + m) * 128 + ((g ^ (m >> 1)) * 16);
  int wa[4];
  {
    const int u0 = wc * 18 + 9 * (p4 & 1);
#pragma unroll
    for (int jj = 0; jj < 4; ++jj)
      wa[jj] = K3_XB + (p4 >> 1) * K3_UOFF + (g * 8 + q4) * 288 + (((u0 + jj) ^ ((g & 1) * 2)) * 8);
  }
  f32x4 acc[4][9];
#pragma unroll
  for (int i = 0; i < 4; ++i)
#pragma unroll
    for (int j = 0; j < 9; ++j) acc[i][j] = (f32x4){0, 0, 0, 0};

#pragma unroll 1
  for (int kt = 0; kt < 9; ++kt) {
    WAIT_V(0); WAIT_L0(); BAR();
    if (kt + 1 < 9) stage(kt + 1, (kt + 1) & 1);
    const LAS char* sb = lds + (kt & 1) * K3_STG;
#pragma unroll
    for (int ks = 0; ks < 2; ++ks) {
      bf16x8 xf[4];
#pragma unroll
      for (int i = 0; i < 4; ++i) xf[i] = lds_b128(sb + ((xa ^ (ks * 64)) + i * 2048));
#pragma unroll
      for (int j = 0; j < 9; ++j) {
        const LAS char* p = sb + wa[j & 3] + (j >> 2) * 32 + ks * (32 * 288);
        const bf16x8 wf = lds_tr2(p, p + 4 * 288);
#pragma unroll
        for (int i = 0; i < 4; ++i) acc[i][j] = mfma16(wf, xf[i], acc[i][j]);
      }
    }
  }
  __builtin_amdgcn_sched_barrier(0);
  const int hi = g & 1;
  const int f0 = pf * 144 + wc * 72 + hi * 36;
  float mean[2], rstd[2];
  bf16_t* hp[2];
#pragma unroll
  for (int pi = 0; pi < 2; ++pi) {
    const int tok = pm * 256 + wr * 64 + pi * 32 + (lane >> 5) * 16 + m;
    const f32x4 s01 = *(const f32x4*)(stats + tok * 4), s23 = *(const f32x4*)(stats + tok * 4 + 2);
    mean[pi] = (s01[0] + s01[2] + s23[0] + s23[2]) * 0.25f;
    const float d0 = s01[0] - mean[pi], d1 = s01[2] - mean[pi], d2 = s23[0] - mean[pi], d3 = s23[2] - mean[pi];
    const float m2 = (s01[1] + s01[3]) + (s23[1] + s23[3]) + 144.0f * ((d0 * d0 + d1 * d1) + (d2 * d2 + d3 * d3));
    rstd[pi] = rsqrtf(m2 * (1.0f / D_) + LN_EPS);
    hp[pi] = H + (size_t)tok * F_ + f0;
  }
  unsigned hw[2][4];
#pragma unroll
  for (int j = 0; j < 9; ++j) {
    const f32x4 cg = *(const f32x4*)(csum + f0 + 4 * j), cu = *(const f32x4*)(csum + F_ + f0 + 4 * j);
#pragma unroll
    for (int pi = 0; pi < 2; ++pi) {
      float hv[4];
#pragma unroll
      for (int r = 0; r < 4; ++r) {
        auto sw = __builtin_amdgcn_permlane32_swap(__float_as_uint(acc[2 * pi][j][r]), __float_as_uint(acc[2 * pi + 1][j][r]), false, false);
        const float gp = rbf((__uint_as_float(sw[0]) - mean[pi] * cg[r]) * rstd[pi]);
        const float up = rbf((__uint_as_float(sw[1]) - mean[pi] * cu[r]) * rstd[pi]);
        const float sg = rbf(gp / (1.0f + __expf(-gp)));
        hv[r] = sg * up;
      }
      hw[pi][2 * (j & 1)] = pk2(hv[0], hv[1]); hw[pi][2 * (j & 1) + 1] = pk2(hv[2], hv[3]);
      if (j & 1) *(u32x4*)(hp[pi] + 4 * (j - 1)) = (u32x4){hw[pi][0], hw[pi][1], hw[pi][2], hw[pi][3]};
      if (j == 8) *(u32x2*)(hp[pi] + 32) = (u32x2){hw[pi][0], hw[pi][1]};
    }
    if (j & 1) asm volatile("" ::: "memory");
  }
}

__global__ __launch_bounds__(256) void k5_reduce(const float* __restrict__ P, const bf16_t* __restrict__ X, bf16_t* __restrict__ Y) {
  const size_t i = ((size_t)blockIdx.x * 256 + threadIdx.x) * 8;
  constexpr size_t N = (size_t)S_ * D_;
  f32x4 a = *(const f32x4*)(P + i), b = *(const f32x4*)(P + i + 4);
#pragma unroll
  for (int s = 1; s < 4; ++s) { a += *(const f32x4*)(P + s * N + i); b += *(const f32x4*)(P + s * N + i + 4); }
  const u32x4 x = *(const u32x4*)(X + i);
  u32x4 w;
  w.x = pk2(bflo(x.x) + rbf(a[0]), bfhi(x.x) + rbf(a[1]));
  w.y = pk2(bflo(x.y) + rbf(a[2]), bfhi(x.y) + rbf(a[3]));
  w.z = pk2(bflo(x.z) + rbf(b[0]), bfhi(x.z) + rbf(b[1]));
  w.w = pk2(bflo(x.w) + rbf(b[2]), bfhi(x.w) + rbf(b[3]));
  *(u32x4*)(Y + i) = w;
}

extern "C" void kernel_launch(void* const* d_in, const int* in_sizes, int n_in,
                              void* d_out, int out_size, void* d_ws, size_t ws_size,
                              hipStream_t stream) {
  const bf16_t* x0 = (const bf16_t*)d_in[0];
  const bf16_t* wqkv = (const bf16_t*)d_in[1];
  const bf16_t* wo = (const bf16_t*)d_in[2];
  const bf16_t* wg = (const bf16_t*)d_in[4];
  const bf16_t* wu = (const bf16_t*)d_in[5];
  const bf16_t* wd = (const bf16_t*)d_in[6];
  bf16_t* out = (bf16_t*)d_out;
  char* ws = (char*)d_ws;
  constexpr size_t XB = (size_t)S_ * D_ * 2, HB = (size_t)S_ * F_ * 2, PB = (size_t)4 * S_ * D_ * 4;
  size_t off = 0;
  float* csum = (float*)(ws + off); off += 65536;
  const bf16_t* xin = x0;
  k0_colsum<<<144, 256, 0, stream>>>(wg, wu, csum);
  for (int l = 0; l < 2; ++l) {
    bf16_t* o = (bf16_t*)(ws + off); off += XB;
    bf16_t* x1 = (bf16_t*)(ws + off); off += XB;
    f32x2* stats = (f32x2*)(ws + off); off += 65536;
    bf16_t* h = (bf16_t*)(ws + off); off += HB;
    float* P = (float*)(ws + off); off += PB;
    bf16_t* x2 = (l == 1) ? out : (bf16_t*)(ws + off); off += XB;
    k1_qkv_attn<<<256, 512, 0, stream>>>(xin, wqkv, o);
    gemm144<2, 1, 9, 4, 0><<<256, 256, 0, stream>>>(o, D_, wo, xin, x1, stats, nullptr);
    k3_gateup<<<256, 512, 0, stream>>>(x1, wg, wu, csum, stats, h);
    gemm144<4, 2, 18, 4, 1><<<256, 512, 0, stream>>>(h, F_, wd, nullptr, nullptr, nullptr, P);
    k5_reduce<<<(S_ * D_) / 2048, 256, 0, stream>>>(P, x1, x2);
    xin = x2;
  }
}
```

```cpp
#include <hip/hip_runtime.h>
#include <hip/hip_bf16.h>
#include <stdint.h>

typedef unsigned short bf16_t;
typedef short bf16x8 __attribute__((ext_vector_type(8)));
typedef short bf16x4 __attribute__((ext_vector_type(4)));
typedef float f32x4 __attribute__((ext_vector_type(4)));
typedef float f32x2 __attribute__((ext_vector_type(2)));
typedef float f32x16 __attribute__((ext_vector_type(16)));
typedef unsigned u32x4 __attribute__((ext_vector_type(4)));
typedef unsigned u32x2 __attribute__((ext_vector_type(2)));
#define LAS __attribute__((address_space(3)))
#define DEVI __device__ __forceinline__

constexpr int S_ = 2048, D_ = 576, F_ = 4608, NH_ = 32, DH_ = 18;
constexpr float LN_EPS = 1e-5f;

DEVI float bf2f(unsigned b) { return __uint_as_float(b << 16); }
DEVI float bflo(unsigned w) { return __uint_as_float(w << 16); }
DEVI float bfhi(unsigned w) { return __uint_as_float(w & 0xffff0000u); }
DEVI unsigned pk2(float lo, float hi) {
  typedef __bf16 bf2v __attribute__((ext_vector_type(2)));
  bf2v v; v.x = (__bf16)lo; v.y = (__bf16)hi;
  return __builtin_bit_cast(unsigned, v);
}
DEVI float rbf(float x) { return (float)((__bf16)x); }

#define STAMPW(kid, i, cond)
#define STAMPK(kid, i)
#define STAMPEND(kid, i)
#define STAMP(i) STAMPK(3, i)
#define ABL_G(x) x
#define ABL_B(x) x
#define ABL_L(...) __VA_ARGS__
#define HAUX 16
#define GV 3
#define PRIO 0
template <int N> struct IC { static constexpr int value = N; };
template <int A, int B, class F> __device__ __forceinline__ void static_for(F&& f) {
  if constexpr (A < B) { f(IC<A>{}); static_for<A + 1, B>(f); }
}
#define WAIT_V(n) asm volatile("s_waitcnt vmcnt(" #n ")" ::: "memory")
#define WAIT_L0() asm volatile("s_waitcnt lgkmcnt(0)" ::: "memory")
#define BAR() do { asm volatile("" ::: "memory"); __builtin_amdgcn_s_barrier(); asm volatile("" ::: "memory"); } while (0)

DEVI void glds16(const char* sbase, unsigned voff, unsigned ldsaddr) {
  asm volatile("s_mov_b32 m0, %2\n\ts_nop 0\n\tglobal_load_lds_dwordx4 %0, %1" ::"v"(voff), "s"(sbase), "s"(ldsaddr) : "memory");
}
DEVI void glds_1(const char* sb, unsigned v0, unsigned la) { glds16(sb, v0, la); }
DEVI void glds_2(const char* sb, unsigned v0, unsigned v1, unsigned la) {
  asm volatile("s_mov_b32 m0, %3\n\ts_nop 0\n\tglobal_load_lds_dwordx4 %0, %2\n\tglobal_load_lds_dwordx4 %1, %2 offset:1024"
               ::"v"(v0), "v"(v1), "s"(sb), "s"(la) : "memory");
}
DEVI void glds_4(const char* sb, unsigned v0, unsigned v1, unsigned v2, unsigned v3, unsigned la) {
  asm volatile("s_mov_b32 m0, %5\n\ts_nop 0\n\tglobal_load_lds_dwordx4 %0, %4\n\tglobal_load_lds_dwordx4 %1, %4 offset:1024\n\t"
               "global_load_lds_dwordx4 %2, %4 offset:2048\n\tglobal_load_lds_dwordx4 %3, %4 offset:3072"
               ::"v"(v0), "v"(v1), "v"(v2), "v"(v3), "s"(sb), "s"(la) : "memory");
}
DEVI void glds_2q(const char* sb, unsigned v0, unsigned v1, unsigned v2, unsigned la) {
  asm volatile("s_mov_b32 m0, %4\n\ts_nop 0\n\tglobal_load_lds_dwordx4 %0, %3\n\tglobal_load_lds_dwordx4 %1, %3 offset:1024\n\t"
               "s_mov_b64 exec, 0xffff\n\tglobal_load_lds_dwordx4 %2, %3 offset:2048\n\ts_mov_b64 exec, -1"
               ::"v"(v0), "v"(v1), "v"(v2), "s"(sb), "s"(la) : "memory");
}
DEVI void glds_2m(const char* sb, unsigned v0, unsigned v1, unsigned la, unsigned long long m0_, unsigned long long m1_) {
  asm volatile("s_mov_b32 m0, %3\n\ts_mov_b64 exec, %4\n\tglobal_load_lds_dwordx4 %0, %2\n\t"
               "s_mov_b64 exec, %5\n\tglobal_load_lds_dwordx4 %1, %2 offset:1024\n\ts_mov_b64 exec, -1"
               ::"v"(v0), "v"(v1), "s"(sb), "s"(la), "s"(m0_), "s"(m1_) : "memory");
}
DEVI unsigned ldsa(const LAS char* p) { return (unsigned)(size_t)p; }
DEVI bf16x8 lds_b128(const LAS char* p) { return *(const LAS bf16x8*)p; }
DEVI bf16x8 lds_tr2(const LAS char* p0, const LAS char* p1) {
  bf16x4 a = __builtin_amdgcn_ds_read_tr16_b64_v4i16((LAS bf16x4*)p0);
  bf16x4 b = __builtin_amdgcn_ds_read_tr16_b64_v4i16((LAS bf16x4*)p1);
  return __builtin_shufflevector(a, b, 0, 1, 2, 3, 4, 5, 6, 7);
}
DEVI f32x4 mfma16(bf16x8 a, bf16x8 b, f32x4 c) { return __builtin_amdgcn_mfma_f32_16x16x32_bf16(a, b, c, 0, 0, 0); }

constexpr int K1_WR = 16 * 448 + 32;
constexpr int K1_WS = 14464;
constexpr int NCNT = 1024;

__global__ __launch_bounds__(256) void k0_prep(const bf16_t* __restrict__ Wg, const bf16_t* __restrict__ Wu, float* __restrict__ csum,
                                                const bf16_t* __restrict__ Wqkv, char* __restrict__ Wp, unsigned* __restrict__ cnt) {
  __shared__ float red[32][64 + 1];
  const int b = blockIdx.x;
  if (b < 144) {
    const bf16_t* W = (b < 72) ? Wg : Wu;
    const int col0 = (b % 72) * 64;
    const int ch = threadIdx.x & 7, rg = threadIdx.x >> 3;
    float s[8] = {0, 0, 0, 0, 0, 0, 0, 0};
#pragma unroll
    for (int i = 0; i < 18; ++i) {
      const u32x4 v = *(const u32x4*)(W + (size_t)(rg * 18 + i) * F_ + col0 + ch * 8);
      s[0] += bflo(v.x); s[1] += bfhi(v.x); s[2] += bflo(v.y); s[3] += bfhi(v.y);
      s[4] += bflo(v.z); s[5] += bfhi(v.z); s[6] += bflo(v.w); s[7] += bfhi(v.w);
    }
#pragma unroll
    for (int j = 0; j < 8; ++j) red[rg][ch * 8 + j] = s[j];
    __syncthreads();
    if (threadIdx.x < 64) {
      float t = 0.f;
#pragma unroll
      for (int r = 0; r < 32; ++r) t += red[r][threadIdx.x];
      csum[(b < 72 ? 0 : F_) + col0 + threadIdx.x] = t;
    }
  } else {
    const int u = b - 144, hg = u / 18, sl = u - hg * 18;
    char* dst = Wp + (size_t)u * K1_WS;
    u32x4 v[4];
#pragma unroll
    for (int i = 0; i < 4; ++i) {
      const int ci = i * 256 + threadIdx.x;
      const int reg = ci >= 450, cj = ci - reg * 450;
      const int rr = cj / 28;
      int ch = cj - rr * 28; if (ch > 26) ch = 26;
      const int seg = ch / 9, cc = ch - seg * 9;
      const int gcol = (seg == 2 ? 0 : (seg + 1) * D_) + hg * 72 + cc * 8;
      const int rc = rr > 15 ? 15 : rr;
      const int k = sl * 32 + (rc >> 3) * 16 + reg * 8 + (rc & 7);
      v[i] = *(const u32x4*)(Wqkv + (size_t)k * (3 * D_) + gcol);
    }
#pragma unroll
    for (int i = 0; i < 4; ++i) {
      const int ci = i * 256 + threadIdx.x;
      if (ci < 904) *(u32x4*)(dst + ci * 16) = v[i];
    }
    if (u < NCNT / 256) cnt[(u * 256 + threadIdx.x) * 32] = 0u;
  }
}

constexpr int K1_XT = 80 * 128;
constexpr int K1_WBASE = 4 * K1_XT;
constexpr int K1_AUX = 156928;
constexpr int K1_LDS = K1_AUX + 768;
constexpr int K1_QS = 440;
constexpr int K1_OB = 35200, K1_OS = 160;
constexpr int K2_WR = 9216 + 128, K2_WS = K2_WR + 9216, K2_WBASE = 45568;

DEVI float dot2bf(unsigned a, unsigned b, float c) {
  typedef __bf16 bf2v __attribute__((ext_vector_type(2)));
  return __builtin_amdgcn_fdot2_f32_bf16(__builtin_bit_cast(bf2v, a), __builtin_bit_cast(bf2v, b), c, false);
}

template <int NJ, int J0>
DEVI void k2_gemm(LAS char* lds, f32x4 (&acc)[5], int lane, int wm, int wk, const char* Wo, const unsigned (&woff)[3], unsigned wdst,
                  const char* OB, int obase, unsigned* __restrict__ cnt, int tid, int wid) {
  const int g = lane >> 4, m = lane & 15, q4 = (lane >> 2) & 3, p4 = lane & 3;
  const int xa = (wk * 4 + g) * 512 + (wm * 16 + m) * 16;
  const int wa = K2_WBASE + (g & 1) * K2_WR + ((wk * 2 + (g >> 1)) * 8 + q4) * 288 + J0 * 32 + p4 * 8;
  auto rdW = [&](bf16x8 (&wf)[NJ], int slot) {
#pragma unroll
    for (int j = 0; j < NJ; ++j) wf[j] = lds_tr2(lds + wa + slot * K2_WS + j * 32, lds + wa + slot * K2_WS + j * 32 + 4 * 288);
  };
  bf16x8 w0[NJ], w1[NJ], w2[NJ];
  rdW(w0, 2); rdW(w1, 3); rdW(w2, 4);
  WAIT_L0(); BAR();
#pragma unroll
  for (int t = 6; t < 9; ++t) glds_2q(Wo + (size_t)t * 64 * D_ * 2, woff[0], woff[1], woff[2], wdst + (t - 4) * K2_WS);
  if (wid == 0) {
    unsigned spins = 0;
    while ((unsigned)__builtin_amdgcn_readfirstlane(__hip_atomic_load(cnt, __ATOMIC_RELAXED, __HIP_MEMORY_SCOPE_AGENT)) < 8u) {
      __builtin_amdgcn_s_sleep(1);
      if (++spins > (1u << 22)) break;
    }
  }
  BAR();
  STAMPK(1, 5);
  {
    const __amdgpu_buffer_rsrc_t rso = __builtin_amdgcn_make_buffer_rsrc((void*)OB, 0, S_ * D_ * 2, 0x00020000);
    u32x4 ov[5];
#pragma unroll
    for (int n = 0; n < 5; ++n) ov[n] = __builtin_amdgcn_raw_buffer_load_b128(rso, obase + ((n < 4 || tid < 256) ? n * 512 + tid : tid) * 16, 0, 16);
#pragma unroll
    for (int n = 0; n < 5; ++n) if (n < 4 || tid < 256) *(LAS u32x4*)(lds + (n * 512 + tid) * 16) = ov[n];
  }
  WAIT_V(0); WAIT_L0(); BAR();
  STAMPK(1, 6);
  bf16x8 xf[9];
#pragma unroll
  for (int t = 0; t < 9; ++t) xf[t] = lds_b128(lds + xa + t * 4096);
#pragma unroll
  for (int j = 0; j < NJ; ++j) acc[j] = mfma16(w0[j], xf[0], (f32x4){0, 0, 0, 0});
  rdW(w0, 5);
#pragma unroll
  for (int j = 0; j < NJ; ++j) acc[j] = mfma16(w1[j], xf[1], acc[j]);
  rdW(w1, 0);
#pragma unroll
  for (int j = 0; j < NJ; ++j) acc[j] = mfma16(w2[j], xf[2], acc[j]);
  rdW(w2, 1);
#pragma unroll
  for (int j = 0; j < NJ; ++j) acc[j] = mfma16(w0[j], xf[3], acc[j]);
  rdW(w0, 2);
#pragma unroll
  for (int j = 0; j < NJ; ++j) acc[j] = mfma16(w1[j], xf[4], acc[j]);
  rdW(w1, 3);
#pragma unroll
  for (int j = 0; j < NJ; ++j) acc[j] = mfma16(w2[j], xf[5], acc[j]);
  rdW(w2, 4);
#pragma unroll
  for (int j = 0; j < NJ; ++j) acc[j] = mfma16(w0[j], xf[6], acc[j]);
#pragma unroll
  for (int j = 0; j < NJ; ++j) acc[j] = mfma16(w1[j], xf[7], acc[j]);
#pragma unroll
  for (int j = 0; j < NJ; ++j) acc[j] = mfma16(w2[j], xf[8], acc[j]);
  WAIT_L0(); BAR();
  LAS float* rt = (LAS float*)lds;
#pragma unroll
  for (int j = 0; j < NJ; ++j) *(LAS f32x4*)(rt + (wk * 32 + wm * 16 + m) * 148 + (J0 + j) * 16 + g * 4) = acc[j];
}

__global__ __launch_bounds__(512, 2) void k1_qkv_attn(const bf16_t* __restrict__ X, const char* __restrict__ Wp, char* __restrict__ OB,
                                                       const bf16_t* __restrict__ Wo, bf16_t* __restrict__ Y, f32x2* __restrict__ stats,
                                                       unsigned* __restrict__ cnt) {
  __shared__ __attribute__((aligned(16))) char lds_[K1_LDS];
  LAS char* lds = (LAS char*)lds_;
  const unsigned lbase = ldsa(lds);
  const int tid = threadIdx.x, lane = tid & 63, wid = __builtin_amdgcn_readfirstlane(tid >> 6);
  const int bid = blockIdx.x, xcd = bid & 7, idx = bid >> 3;
  const int tt = (xcd >> 1) * 8 + (idx & 7), hg = (xcd & 1) * 4 + (idx >> 3);
  const int t0 = tt * 64 - 16;
  STAMPK(1, 0);

  unsigned xoff[2], woff[2];
#pragma unroll
  for (int i = 0; i < 2; ++i) {
    int ci = i * 64 + lane; if (ci > 79) ci = 79;
    ci += wid * 80;
    const int row = ci >> 3, c = (ci & 7) ^ ((row >> 1) & 7);
    int grow = t0 + row; if (grow < 0) grow = 0;
    xoff[i] = (unsigned)((grow * D_ + c * 8) * 2 + 1024 - i * 1024);
  }
  woff[0] = (unsigned)((wid * 113 + lane) * 16); woff[1] = woff[0];
  const unsigned wdst = lbase + K1_WBASE + wid * 1808;
  const unsigned long long xmask = wid == 0 ? ~0xffull : ~0ull;
  auto stageX = [&](int kt) {
    glds_2m((const char*)X + kt * 128 - 1024, xoff[0], xoff[1], lbase + (kt & 3) * K1_XT + wid * 1280, xmask, 0xffffull);
  };
  auto stageW = [&](int s) {
    glds_2m(Wp + (size_t)(hg * 18 + s) * K1_WS, woff[0], woff[1], wdst + (s & 7) * K1_WS, ~0ull, 0x1ffffffffffffull);
  };
  auto stageT = [&](int t) { stageW(2 * t); stageX(t); stageW(2 * t + 1); };
  stageT(0); stageT(1);
  if (tid >= 64 && tid < 64 + 32) {
    const int c = tid - 64;
    *(LAS u32x4*)(lds + (c >> 3) * K1_XT + (c & 7) * 16) = (u32x4){0x3f803f80u, 0x3f803f80u, 0x3f803f80u, 0x3f803f80u};
  }
  const int th = hg >> 2, ct = hg & 3;
  unsigned w2off[3];
#pragma unroll
  for (int i = 0; i < 3; ++i) {
    int ci = i * 64 + lane; if (ci > 143) ci = 143;
    const int rr = ci / 18, c = ci - rr * 18;
    w2off[i] = (unsigned)(((wid * 8 + rr) * D_ + ct * 144 + c * 8) * 2 - i * 1024);
  }
  const unsigned w2dst = lbase + K2_WBASE + (wid & 1) * K2_WR + (wid >> 1) * 2304;
  auto stageB = [&](int kt) { glds_2q((const char*)Wo + (size_t)kt * 64 * D_ * 2, w2off[0], w2off[1], w2off[2], w2dst + ((kt + 2) % 6) * K2_WS); };

  const int g = lane >> 4, m = lane & 15, q4 = (lane >> 2) & 3, p4 = lane & 3;
  const int xa0 = m * 128 + ((g ^ (m >> 1)) * 16);
  const int wa0 = K1_WBASE + (g & 1) * K1_WR + ((g >> 1) * 8 + q4) * 448 + (wid < 7 ? wid : 0) * 64 + p4 * 8;
  f32x4 acc[5][2];
#pragma unroll
  for (int i = 0; i < 5; ++i) { acc[i][0] = (f32x4){0, 0, 0, 0}; acc[i][1] = (f32x4){0, 0, 0, 0}; }
  LAS f32x2* ms = (LAS f32x2*)(lds + K1_AUX);
  auto top = [&](int t) {
    __builtin_amdgcn_sched_barrier(0);
    if (t < 6) { asm volatile("s_waitcnt vmcnt(12) lgkmcnt(0)" ::: "memory"); } else { asm volatile("s_waitcnt vmcnt(6) lgkmcnt(0)" ::: "memory"); }
    BAR();
    __builtin_amdgcn_sched_barrier(0);
  };
  auto stg = [&](int t) {
    if (t < 5) stageT(t + 4);
    if (t == 6) { stageB(0); stageB(1); }
    if (t == 7) { stageB(2); stageB(3); }
  };
  WAIT_V(6); WAIT_L0(); BAR();
  STAMPK(1, 1);
  auto rdX = [&](bf16x8 (&xf)[5], int kt, int ks) {
#pragma unroll
    for (int i = 0; i < 5; ++i) xf[i] = lds_b128(lds + ((xa0 ^ (ks * 64)) + (kt & 3) * K1_XT + i * 2048));
  };
  auto mainloop = [&](auto EARLY) {
    bf16x8 xfa[5], xfb[5], wfa[2], wfb[2];
    auto rdW = [&](bf16x8 (&wf)[2], int s) {
#pragma unroll
      for (int j = 0; j < 2; ++j) wf[j] = lds_tr2(lds + (wa0 + (s & 7) * K1_WS + j * 32), lds + (wa0 + (s & 7) * K1_WS + j * 32 + 4 * 448));
    };
    auto step = [&](bf16x8 (&wc)[2], bf16x8 (&xc)[5], bf16x8 (&wn)[2], bf16x8 (&xn)[5], int s) {
#pragma unroll
      for (int i = 0; i < 5; ++i) {
        if (i < 2) wn[i] = lds_tr2(lds + (wa0 + (s & 7) * K1_WS + i * 32), lds + (wa0 + (s & 7) * K1_WS + i * 32 + 4 * 448));
        acc[i][0] = mfma16(wc[0], xc[i], acc[i][0]);
        __builtin_amdgcn_sched_barrier(0);
        xn[i] = lds_b128(lds + ((xa0 ^ ((s & 1) * 64)) + ((s >> 1) & 3) * K1_XT + i * 2048));
        acc[i][1] = mfma16(wc[1], xc[i], acc[i][1]);
        __builtin_amdgcn_sched_barrier(0);
      }
    };
    rdX(xfa, 0, 0); rdW(wfa, 0);
    stageT(2);
    step(wfa, xfa, wfb, xfb, 1);
    stageT(3);
#pragma unroll
    for (int t = 0; t < 8; ++t) {
      top(t);
      if (decltype(EARLY)::value) stg(t);
      step(wfb, xfb, wfa, xfa, 2 * t + 2);
      if (!decltype(EARLY)::value) stg(t);
      step(wfa, xfa, wfb, xfb, 2 * t + 3);
    }
#pragma unroll
    for (int i = 0; i < 5; ++i)
#pragma unroll
      for (int j = 0; j < 2; ++j) acc[i][j] = mfma16(wfb[j], xfb[i], acc[i][j]);
  };
  if (wid < 4) {
    mainloop(IC<1>{});
  } else if (wid < 7) {
    mainloop(IC<0>{});
  } else {
    bf16x8 xfa[5], xfb[5];
    const bf16x8 ones = {0x3f80, 0x3f80, 0x3f80, 0x3f80, 0x3f80, 0x3f80, 0x3f80, 0x3f80};
    auto step = [&](bf16x8 (&xc)[5], bf16x8 (&xn)[5], int s) {
#pragma unroll
      for (int i = 0; i < 5; ++i) {
        acc[i][0] = mfma16(xc[i], xc[i], acc[i][0]);
        xn[i] = lds_b128(lds + ((xa0 ^ ((s & 1) * 64)) + ((s >> 1) & 3) * K1_XT + i * 2048));
        acc[i][1] = mfma16(ones, xc[i], acc[i][1]);
        __builtin_amdgcn_sched_barrier(0);
      }
    };
    rdX(xfa, 0, 0);
    stageT(2);
    step(xfa, xfb, 1);
    stageT(3);
#pragma unroll
    for (int t = 0; t < 8; ++t) {
      top(t);
      step(xfb, xfa, 2 * t + 2);
      stg(t);
      step(xfa, xfb, 2 * t + 3);
    }
#pragma unroll
    for (int i = 0; i < 5; ++i) { acc[i][0] = mfma16(xfb[i], xfb[i], acc[i][0]); acc[i][1] = mfma16(ones, xfb[i], acc[i][1]); }
#pragma unroll
    for (int i = 0; i < 5; ++i) {
      const float q = (m & 2) ? ((m & 1) ? acc[i][0][3] : acc[i][0][2]) : ((m & 1) ? acc[i][0][1] : acc[i][0][0]);
      const float mean = acc[i][1][0] * (1.0f / D_);
      const float var = fmaxf(q * (1.0f / D_) - mean * mean, 0.f);
      if (g == (m >> 2)) ms[i * 16 + m] = (f32x2){mean, rsqrtf(var + LN_EPS)};
    }
  }
  WAIT_L0(); BAR();
  STAMPK(1, 2);
  stageB(4); stageB(5);
  if (wid < 7) {
    f32x4 cs[2];
#pragma unroll
    for (int j = 0; j < 2; ++j)
#pragma unroll
      for (int r = 0; r < 4; ++r) cs[j][r] = __shfl(acc[0][j][r], lane & 48);
#pragma unroll
    for (int i = 0; i < 5; ++i) {
      const f32x2 mr = ms[i * 16 + m];
#pragma unroll
      for (int j = 0; j < 2; ++j) {
        const int col = (wid * 2 + j) * 16 + g * 4;
        const f32x4 v = (acc[i][j] - cs[j] * mr.x) * mr.y;
        if (col < 216) *(LAS u32x2*)(lds + (i * 16 + m) * K1_QS + col * 2) = (u32x2){pk2(v[0], v[1]), pk2(v[2], v[3])};
      }
    }
  }
  WAIT_L0(); BAR();
  STAMPK(1, 3);
  {
    const int half = lane & 1, hh = wid & 3, tok = (wid >> 2) * 32 + (lane >> 1);
    const int row = tok + 16;
    const int gpos = t0 + row;
    const LAS unsigned* bp = (const LAS unsigned*)(lds + row * K1_QS + hh * 36 + half * 20);
    unsigned qv[5];
#pragma unroll
    for (int d = 0; d < 4; ++d) qv[d] = bp[72 + d];
    qv[4] = half ? 0u : bp[72 + 4];
    float lg[8];
#pragma unroll
    for (int w = 0; w < 8; ++w) {
      const LAS unsigned* kp = bp - w * (2 * K1_QS / 4);
      float s = 0.f;
#pragma unroll
      for (int d = 0; d < 5; ++d) s = dot2bf(qv[d], kp[d], s);
      s += __shfl_xor(s, 1);
      lg[w] = (gpos - 2 * w >= 0) ? s * (0.23570226039551584f * 1.4426950408889634f) : -1e30f;
    }
    float mx = lg[0];
#pragma unroll
    for (int w = 1; w < 8; ++w) mx = fmaxf(mx, lg[w]);
    float den = 0.f;
#pragma unroll
    for (int w = 0; w < 8; ++w) { lg[w] = (gpos - 2 * w >= 0) ? __builtin_amdgcn_exp2f(lg[w] - mx) : 0.f; den += lg[w]; }
    const float inv = __builtin_amdgcn_rcpf(den);
    float ov[10];
#pragma unroll
    for (int d = 0; d < 10; ++d) ov[d] = 0.f;
#pragma unroll
    for (int w = 0; w < 8; ++w) {
      const LAS unsigned* vp = bp - w * (2 * K1_QS / 4) + 36;
      const float pw = lg[w] * inv;
#pragma unroll
      for (int d = 0; d < 5; ++d) { const unsigned vv = vp[d]; ov[2 * d] += pw * bflo(vv); ov[2 * d + 1] += pw * bfhi(vv); }
    }
    LAS unsigned* op = (LAS unsigned*)(lds + K1_OB + tok * K1_OS + hh * 36 + half * 20);
#pragma unroll
    for (int d = 0; d < 5; ++d) if (d < 4 || !half) op[d] = pk2(ov[2 * d], ov[2 * d + 1]);
  }
  WAIT_L0(); BAR();
  {
    const __amdgpu_buffer_rsrc_t rso = __builtin_amdgcn_make_buffer_rsrc((void*)OB, 0, S_ * D_ * 2, 0x00020000);
#pragma unroll
    for (int i = 0; i < 2; ++i) {
      const int ci = i * 512 + tid;
      if (ci < 576) {
        const int r = ci & 31, q = ci >> 5, hf = q / 9, cc = q - hf * 9;
        const u32x4 v = *(const LAS u32x4*)(lds + K1_OB + (hf * 32 + r) * K1_OS + cc * 16);
        __builtin_amdgcn_raw_buffer_store_b128(v, rso, (((tt * 2 + hf) * 72 + hg * 9 + cc) * 32 + r) * 16, 0, 16);
      }
    }
  }
  const int prow = (tid >> 3) & 31, sub = tid & 7;
  const size_t gofs = (size_t)(tt * 64 + th * 32 + prow) * D_ + ct * 144;
  u32x4 rv[3];
#pragma unroll
  for (int c = 0; c < 3; ++c) { const int ch = c * 8 + sub; rv[c] = *(const u32x4*)(X + gofs + (ch < 18 ? ch : 0) * 8); }
  WAIT_V(0); BAR();
  if (tid == 0) __hip_atomic_fetch_add(cnt + tt * 32, 1u, __ATOMIC_RELAXED, __HIP_MEMORY_SCOPE_AGENT);
  STAMPK(1, 4);
  {
    f32x4 acc2[5];
    const int wm = wid & 1, wk = (wid >> 1) & 1;
    const int obase = (tt * 2 + th) * 72 * 32 * 16;
    if (wid < 4) k2_gemm<5, 0>(lds, acc2, lane, wm, wk, (const char*)Wo, w2off, w2dst, OB, obase, cnt + tt * 32, tid, wid);
    else k2_gemm<4, 5>(lds, acc2, lane, wm, wk, (const char*)Wo, w2off, w2dst, OB, obase, cnt + tt * 32, tid, wid);
  }
  WAIT_L0(); BAR();
  STAMPK(1, 7);
  if (tid < 256) {
    const LAS float* rt = (const LAS float*)lds;
    float xv[3][8];
    float s = 0.f;
#pragma unroll
    for (int c = 0; c < 3; ++c) {
      const int ch = c * 8 + sub;
      const bool ok = ch < 18;
      const LAS float* p0 = rt + prow * 148 + (ok ? ch : 0) * 8;
      const f32x4 a0 = *(const LAS f32x4*)p0 + *(const LAS f32x4*)(p0 + 32 * 148);
      const f32x4 a1 = *(const LAS f32x4*)(p0 + 4) + *(const LAS f32x4*)(p0 + 32 * 148 + 4);
      xv[c][0] = rbf(bflo(rv[c].x) + rbf(a0[0])); xv[c][1] = rbf(bfhi(rv[c].x) + rbf(a0[1]));
      xv[c][2] = rbf(bflo(rv[c].y) + rbf(a0[2])); xv[c][3] = rbf(bfhi(rv[c].y) + rbf(a0[3]));
      xv[c][4] = rbf(bflo(rv[c].z) + rbf(a1[0])); xv[c][5] = rbf(bfhi(rv[c].z) + rbf(a1[1]));
      xv[c][6] = rbf(bflo(rv[c].w) + rbf(a1[2])); xv[c][7] = rbf(bfhi(rv[c].w) + rbf(a1[3]));
      if (ok) {
        *(u32x4*)(Y + gofs + ch * 8) = (u32x4){pk2(xv[c][0], xv[c][1]), pk2(xv[c][2], xv[c][3]), pk2(xv[c][4], xv[c][5]), pk2(xv[c][6], xv[c][7])};
#pragma unroll
        for (int e = 0; e < 8; ++e) s += xv[c][e];
      }
    }
    s += __shfl_xor(s, 1); s += __shfl_xor(s, 2); s += __shfl_xor(s, 4);
    const float mean = s * (1.0f / 144.0f);
    float q = 0.f;
#pragma unroll
    for (int c = 0; c < 3; ++c)
      if (c * 8 + sub < 18) {
#pragma unroll
        for (int e = 0; e < 8; ++e) { const float d = xv[c][e] - mean; q += d * d; }
      }
    q += __shfl_xor(q, 1); q += __shfl_xor(q, 2); q += __shfl_xor(q, 4);
    if (sub == 0) stats[(tt * 64 + th * 32 + prow) * 4 + ct] = (f32x2){mean, q};
  }
  STAMPEND(1, 8);
}

constexpr int K3_XS = 32768, K3_UOFF = 9216 + 128, K3_WS = K3_UOFF + 9216;
constexpr int K3_WBASE = 2 * K3_XS, K3_AUX = K3_WBASE + 4 * K3_WS;
constexpr int K3_CS = K3_AUX + 8192;
constexpr int K3_LDS = K3_CS + 2048;

template <int NJ, int J0>
DEVI void k3_body(LAS char* lds, const char* X, const char* wsrc, bf16_t* __restrict__ H, const unsigned (&xoff)[4], const unsigned (&woff)[3],
                  unsigned xdst, unsigned wdst, int lane, int wr, int pm, int pf) {
  auto stageX = [&](int t) { glds_4(X + t * 128, xoff[0], xoff[1], xoff[2], xoff[3], xdst + (t & 1) * K3_XS); };
  auto stageW = [&](int s) { glds_2q(wsrc + (size_t)s * 32 * F_ * 2, woff[0], woff[1], woff[2], wdst + (s & 3) * K3_WS); };
  const int tk = lane & 31, h = lane >> 5, q4 = (lane >> 2) & 3, p4 = lane & 3, isup = (lane >> 4) & 1;
  const int xa = (wr * 64 + tk) * 128 + ((h ^ ((tk >> 1) & 7)) * 16);
  const int wa = K3_WBASE + isup * K3_UOFF + (h * 8 + q4) * 288 + J0 * 32 + p4 * 8;
  auto rdW = [&](int wo, int kk, int j) -> bf16x8 {
    const LAS char* p = lds + (wa + wo + kk * (16 * 288) + j * 32);
    return lds_tr2(p, p + 4 * 288);
  };
  auto rdX = [&](int xo, int k4, int i) -> bf16x8 {
    return lds_b128(lds + ((xa ^ (k4 * 32)) + xo + i * 4096));
  };
  f32x16 acc[2][NJ];
  f32x16 zero16;
#pragma unroll
  for (int r = 0; r < 16; ++r) zero16[r] = 0.f;
  WAIT_V(10); BAR();
  float rs[2];
  {
    bf16x8 mf[2];
#pragma unroll
    for (int i = 0; i < 2; ++i) {
      const LAS f32x4* sp = (const LAS f32x4*)(lds + K3_AUX + (wr * 64 + i * 32 + tk) * 32);
      const f32x4 s01 = sp[0], s23 = sp[1];
      const float mean = (s01[0] + s01[2] + s23[0] + s23[2]) * 0.25f;
      const float d0 = s01[0] - mean, d1 = s01[2] - mean, d2 = s23[0] - mean, d3 = s23[2] - mean;
      const float m2 = (s01[1] + s01[3]) + (s23[1] + s23[3]) + 144.0f * ((d0 * d0 + d1 * d1) + (d2 * d2 + d3 * d3));
      rs[i] = m2 * (1.0f / D_) + LN_EPS;
      const float m0 = rbf(-mean), m1 = rbf(-mean - m0), m2_ = (-mean - m0) - m1;
      u32x4 w = {pk2(m0, m0), pk2(m0, m1), pk2(m1, m2_), 0u};
      if (h) w = (u32x4){0u, 0u, 0u, 0u};
      mf[i] = __builtin_bit_cast(bf16x8, w);
    }
#pragma unroll
    for (int j = 0; j < NJ; ++j) {
      const float c = *(const LAS float*)(lds + K3_CS + (isup * 144 + (J0 + j) * 16 + (lane & 15)) * 4);
      const float c0 = rbf(c), c1 = rbf(c - c0), c2 = (c - c0) - c1;
      const u32x4 w = {pk2(c0, c1), pk2(c2, c0), pk2(c1, c0), 0u};
      const bf16x8 cf = __builtin_bit_cast(bf16x8, w);
#pragma unroll
      for (int i = 0; i < 2; ++i) acc[i][j] = __builtin_amdgcn_mfma_f32_32x32x16_bf16(cf, mf[i], zero16, 0, 0, 0);
    }
  }
  bf16x8 xfa[2], xfb[2], xfc[2], wf[4];
  WAIT_V(3); BAR();
  STAMP(1);
  xfa[0] = rdX(0, 0, 0); xfa[1] = rdX(0, 0, 1);
  wf[0] = rdW(0, 0, 0); wf[1] = rdW(0, 0, 1);
  stageX(1); stageW(2);

  if (PRIO == 1 && NJ == 5) __builtin_amdgcn_s_setprio(1);
  if (PRIO == 2 && NJ == 4) __builtin_amdgcn_s_setprio(1);
#pragma unroll 1
  for (int t = 0; t < 9; ++t) {
    const int tp = t & 1;
    const int wo = tp * (2 * K3_WS), won = (1 - tp) * (2 * K3_WS);
    const int xo = tp * K3_XS, xon = (1 - tp) * K3_XS;
#pragma unroll
    for (int k4 = 0; k4 < 4; ++k4) {
      if (k4 == 0) {
        if (t < 8) { WAIT_V(7); } else { WAIT_V(0); }
        ABL_B(BAR());
      }
      if (k4 == 2) {
        if (t < 7) { WAIT_V(3); } else { WAIT_V(0); }
        WAIT_L0();
        ABL_B(BAR());
      }
      __builtin_amdgcn_sched_barrier(0);
#pragma unroll
      for (int j = 0; j < NJ; ++j) {
        const int n = k4 * NJ + j;
        if (j == 0) {
          if (k4 == (NJ == 4 ? 0 : 1) && t < 8) stageW(2 * t + 3);
          if (k4 == (NJ == 4 ? 2 : 3) && t < 7) { stageW(2 * t + 4); stageX(t + 2); }
        }
        {
          const int n2 = n + 2, k42 = n2 / NJ, j2 = n2 % NJ;
          if (k42 < 4) { ABL_L(wf[n2 & 3] = rdW(wo + (k42 >> 1) * K3_WS, k42 & 1, j2);) }
          else { ABL_L(wf[n2 & 3] = rdW(won, 0, j2);) }
        }
        if (j == 1) {
          if (k4 == 0) { ABL_L(xfb[0] = rdX(xo, 1, 0); xfb[1] = rdX(xo, 1, 1);) }
          if (k4 == 1) { ABL_L(xfa[0] = rdX(xo, 2, 0); xfa[1] = rdX(xo, 2, 1);) }
          if (k4 == 3) { ABL_L(xfa[0] = rdX(xon, 0, 0); xfa[1] = rdX(xon, 0, 1);) }
        }
        if (j == 2 && k4 == 1) { ABL_L(xfc[0] = rdX(xo, 3, 0); xfc[1] = rdX(xo, 3, 1);) }
#pragma unroll
        for (int i = 0; i < 2; ++i)
          acc[i][j] = __builtin_amdgcn_mfma_f32_32x32x16_bf16(wf[n & 3], k4 == 1 ? xfb[i] : (k4 == 3 ? xfc[i] : xfa[i]), acc[i][j], 0, 0, 0);
        __builtin_amdgcn_sched_barrier(0);
      }
    }
  }
  STAMP(2);
  const __amdgpu_buffer_rsrc_t rsh = __builtin_amdgcn_make_buffer_rsrc((void*)H, 0, S_ * F_ * 2, 0x00020000);
  const int hofs = (((pf * 9 + J0) * 2 + h) * S_ + pm * 256 + wr * 64 + tk) * 16;
#pragma unroll
  for (int i = 0; i < 2; ++i) {
    const float c1 = -1.4426950408889634f * __builtin_amdgcn_rsqf(rs[i]);
    const f32x2 c1v = {c1, c1}, inv = {rs[i], rs[i]};
#pragma unroll
    for (int j = 0; j < NJ; ++j) {
      u32x4 hw;
#pragma unroll
      for (int r = 0; r < 4; ++r) {
        const f32x2 ag = {acc[i][j][2 * r], acc[i][j][2 * r + 1]}, au = {acc[i][j][8 + 2 * r], acc[i][j][8 + 2 * r + 1]};
        const f32x2 z = ag * c1v;
        const f32x2 e = {__builtin_amdgcn_exp2f(z.x), __builtin_amdgcn_exp2f(z.y)};
        const f32x2 d = e * inv + inv;
        const f32x2 rc = {__builtin_amdgcn_rcpf(d.x), __builtin_amdgcn_rcpf(d.y)};
        const f32x2 hv = (ag * au) * rc;
        hw[r] = pk2(hv.x, hv.y);
      }
      __builtin_amdgcn_raw_buffer_store_b128(hw, rsh, hofs + j * (2 * S_ * 16) + i * 512, 0, HAUX);
    }
  }
  STAMP(3);
}

__global__ __launch_bounds__(512, 2) void k3_gateup(const bf16_t* __restrict__ X, const bf16_t* __restrict__ Wg, const bf16_t* __restrict__ Wu,
                                                     const float* __restrict__ csum, const f32x2* __restrict__ stats, bf16_t* __restrict__ H) {
  __shared__ __attribute__((aligned(16))) char lds_[K3_LDS];
  LAS char* lds = (LAS char*)lds_;
  const unsigned lbase = ldsa(lds);
  const int tid = threadIdx.x, lane = tid & 63, wid = __builtin_amdgcn_readfirstlane(tid >> 6);
  const int bid = blockIdx.x, xcd = bid & 7, idx = bid >> 3;
  const int pm = (xcd >> 2) * 4 + (idx & 3), pf = (xcd & 3) * 8 + (idx >> 2);
  const int wr = wid & 3, wc = wid >> 2;
  STAMP(0);
  glds16((const char*)(stats + pm * 256 * 4), (unsigned)(tid * 16), lbase + K3_AUX + wid * 1024);
  {
    const int l36 = lane < 36 ? lane : 35;
    if (lane < 36) glds16((const char*)(csum + (wid & 1) * F_ + pf * 144), (unsigned)(l36 * 16), lbase + K3_CS + (wid & 1) * 576);
  }
  unsigned xoff[4], woff[3];
#pragma unroll
  for (int i = 0; i < 4; ++i) {
    const int row = (wid * 4 + i) * 8 + (lane >> 3);
    const int c = (lane & 7) ^ ((row >> 1) & 7);
    xoff[i] = (unsigned)(((pm * 256 + row) * D_ + c * 8) * 2 - i * 1024);
  }
  const unsigned xdst = lbase + wid * 4096;
  glds_4((const char*)X, xoff[0], xoff[1], xoff[2], xoff[3], xdst);
#pragma unroll
  for (int i = 0; i < 3; ++i) {
    int ci = i * 64 + lane; if (ci > 143) ci = 143;
    const int kk = ci / 18, cp = ci - kk * 18;
    woff[i] = (unsigned)((((wid & 3) * 8 + kk) * F_ + pf * 144 + cp * 8) * 2 - i * 1024);
  }
  const char* wsrc = (const char*)((wid >> 2) ? Wu : Wg);
  const unsigned wdst = lbase + K3_WBASE + (wid >> 2) * K3_UOFF + (wid & 3) * 2304;
  glds_2q(wsrc, woff[0], woff[1], woff[2], wdst);
  glds_2q(wsrc + 32 * F_ * 2, woff[0], woff[1], woff[2], wdst + K3_WS);
  if (wc == 0) k3_body<5, 0>(lds, (const char*)X, wsrc, H, xoff, woff, xdst, wdst, lane, wr, pm, pf);
  else k3_body<4, 5>(lds, (const char*)X, wsrc, H, xoff, woff, xdst, wdst, lane, wr, pm, pf);
  STAMPEND(3, 4);
}

constexpr int K4_XB = 16384, K4_STG = K4_XB + 18432, K4_LDS = 4 * K4_STG;

template <int NJ, int J0>
DEVI void k4_body(LAS char* lds, const char* X, const char* W, unsigned xoff, const unsigned (&woff)[3], unsigned sdst, unsigned wdst,
                  int lane, int wm, int split, int tile, float* __restrict__ P, unsigned* __restrict__ cnt) {
  auto stage = [&](int kt, int buf) {
    glds_2(X + (size_t)kt * (8 * S_ * 16), xoff, xoff, sdst + buf * K4_STG);
    glds_2q(W + (size_t)kt * 64 * D_ * 2, woff[0], woff[1], woff[2], wdst + buf * K4_STG);
  };
  const int g = lane >> 4, m = lane & 15, q4 = (lane >> 2) & 3, p4 = lane & 3;
  const int xa = g * 2048 + (wm * 32 + m) * 16;
  const int wa = K4_XB + ((g >> 1) * 16 + (g & 1) * 4 + q4) * 288 + J0 * 32 + p4 * 8;
  f32x4 acc[2][NJ];
#pragma unroll
  for (int i = 0; i < 2; ++i)
#pragma unroll
    for (int j = 0; j < NJ; ++j) acc[i][j] = (f32x4){0, 0, 0, 0};
  bf16x8 xa_[2], wa_[NJ], xb_[2], wb_[NJ];
  auto rd = [&](bf16x8 (&xf)[2], bf16x8 (&wf)[NJ], const LAS char* sb, int ks) {
#pragma unroll
    for (int i = 0; i < 2; ++i) xf[i] = lds_b128(sb + xa + ks * 8192 + i * 256);
#pragma unroll
    for (int j = 0; j < NJ; ++j) wf[j] = lds_tr2(sb + wa + ks * (32 * 288) + j * 32, sb + wa + ks * (32 * 288) + j * 32 + 8 * 288);
  };
  auto step = [&](bf16x8 (&xc)[2], bf16x8 (&wc)[NJ], bf16x8 (&xn)[2], bf16x8 (&wn)[NJ], const LAS char* sb, int ks) {
#pragma unroll
    for (int j = 0; j < NJ; ++j) {
      if (j < 2) xn[j] = lds_b128(sb + xa + ks * 8192 + j * 256);
      wn[j] = lds_tr2(sb + wa + ks * (32 * 288) + j * 32, sb + wa + ks * (32 * 288) + j * 32 + 8 * 288);
#pragma unroll
      for (int i = 0; i < 2; ++i) acc[i][j] = mfma16(wc[j], xc[i], acc[i][j]);
      __builtin_amdgcn_sched_barrier(0);
    }
  };
  WAIT_V(5); BAR();
  STAMPK(4, 1);
  rd(xa_, wa_, lds, 0);
  stage(2, 2);
  step(xa_, wa_, xb_, wb_, lds, 1);
  stage(3, 3);
  auto iter = [&](auto INFL, auto MORE, int t) {
    asm volatile("s_waitcnt vmcnt(%0) lgkmcnt(0)" ::"n"(decltype(INFL)::value * 5) : "memory");
    BAR();
    __builtin_amdgcn_sched_barrier(0);
    const LAS char* sn = lds + ((t + 1) & 3) * K4_STG;
    if (decltype(MORE)::value && NJ == 4) stage(t + 4, t & 3);
    step(xb_, wb_, xa_, wa_, sn, 0);
    if (decltype(MORE)::value && NJ == 5) stage(t + 4, t & 3);
    step(xa_, wa_, xb_, wb_, sn, 1);
  };
#pragma unroll 1
  for (int t = 0; t < 14; ++t) iter(IC<2>{}, IC<1>{}, t);
  iter(IC<2>{}, IC<0>{}, 14);
  iter(IC<1>{}, IC<0>{}, 15);
  iter(IC<0>{}, IC<0>{}, 16);
#pragma unroll
  for (int j = 0; j < NJ; ++j)
#pragma unroll
    for (int i = 0; i < 2; ++i) acc[i][j] = mfma16(wb_[j], xb_[i], acc[i][j]);
  STAMPK(4, 2);
  const __amdgpu_buffer_rsrc_t rs = __builtin_amdgcn_make_buffer_rsrc((void*)P, 0, 64 * 16 * 18 * 1024, 0x00020000);
  if (wm != split) {
    const int sb = ((tile * 4 + wm) * 4 + split) * 18 * 1024 + lane * 16;
#pragma unroll
    for (int i = 0; i < 2; ++i)
#pragma unroll
      for (int j = 0; j < NJ; ++j)
        __builtin_amdgcn_raw_buffer_store_b128(__builtin_bit_cast(u32x4, acc[i][j]), rs, sb + (i * 9 + J0 + j) * 1024, 0, 16);
    WAIT_V(0);
    if (lane == 0) __hip_atomic_fetch_add(cnt + (tile * 4 + wm) * 32, 1u, __ATOMIC_RELAXED, __HIP_MEMORY_SCOPE_AGENT);
  } else {
    LAS f32x4* own = (LAS f32x4*)(lds + 2 * K4_STG);
#pragma unroll
    for (int i = 0; i < 2; ++i)
#pragma unroll
      for (int j = 0; j < NJ; ++j) own[(i * 9 + J0 + j) * 64 + lane] = acc[i][j];
    if (NJ == 5) {
      unsigned spins = 0;
      while ((unsigned)__builtin_amdgcn_readfirstlane(__hip_atomic_load(cnt + (tile * 4 + split) * 32, __ATOMIC_RELAXED, __HIP_MEMORY_SCOPE_AGENT)) < 6u) {
        __builtin_amdgcn_s_sleep(1);
        if (++spins > (1u << 22)) break;
      }
    }
  }
}

__global__ __launch_bounds__(512, 2) void k4_down(const bf16_t* __restrict__ X, const bf16_t* __restrict__ W, const bf16_t* __restrict__ R,
                                                   bf16_t* __restrict__ Y, unsigned* __restrict__ cnt, float* __restrict__ P) {
  __shared__ __attribute__((aligned(16))) char lds_[K4_LDS];
  LAS char* lds = (LAS char*)lds_;
  const unsigned lbase = ldsa(lds);
  const int tid = threadIdx.x, lane = tid & 63, wid = __builtin_amdgcn_readfirstlane(tid >> 6);
  const int bid = blockIdx.x, xcd = bid & 7, idx = bid >> 3;
  const int split = xcd & 3;
  const int m0 = (xcd >> 2) * 1024 + (idx >> 2) * 128, n0 = (idx & 3) * 144, k0 = split * 1152;
  const int wm = wid & 3, wn = wid >> 2;
  STAMPK(4, 0);
  const unsigned xoff = (unsigned)((((k0 >> 3) + wid) * S_ + m0 + lane) * 16);
  unsigned woff[3];
#pragma unroll
  for (int i = 0; i < 3; ++i) {
    int ci = i * 64 + lane; if (ci > 143) ci = 143;
    const int rr = ci / 18, c = ci - rr * 18;
    woff[i] = (unsigned)(((k0 + wid * 8 + rr) * D_ + n0 + c * 8) * 2 - i * 1024);
  }
  const unsigned sdst = lbase + wid * 2048, wdst = lbase + K4_XB + wid * 2304;
#pragma unroll
  for (int s = 0; s < 2; ++s) {
    glds_2((const char*)X + (size_t)s * (8 * S_ * 16), xoff, xoff, sdst + s * K4_STG);
    glds_2q((const char*)W + (size_t)s * 64 * D_ * 2, woff[0], woff[1], woff[2], wdst + s * K4_STG);
  }
  const int tile = (m0 >> 7) * 4 + (idx & 3);
  if (wn == 0) k4_body<5, 0>(lds, (const char*)X, (const char*)W, xoff, woff, sdst, wdst, lane, wm, split, tile, P, cnt);
  else k4_body<4, 5>(lds, (const char*)X, (const char*)W, xoff, woff, sdst, wdst, lane, wm, split, tile, P, cnt);
  const int row = (tid >> 3) & 31, sub = tid & 7;
  const size_t gofs = (size_t)(m0 + split * 32 + row) * D_ + n0;
  u32x4 rv[3];
#pragma unroll
  for (int c = 0; c < 3; ++c) { const int ch = c * 8 + sub; rv[c] = *(const u32x4*)(R + gofs + (ch < 18 ? ch : 0) * 8); }
  WAIT_L0(); BAR();
  STAMPK(4, 3);
  const int g = lane >> 4, m = lane & 15;
  LAS f32x4* own = (LAS f32x4*)(lds + 2 * K4_STG);
  LAS float* rt = (LAS float*)lds;
  {
    const __amdgpu_buffer_rsrc_t rs = __builtin_amdgcn_make_buffer_rsrc((void*)P, 0, 64 * 16 * 18 * 1024, 0x00020000);
    u32x4 pv[3][3];
#pragma unroll
    for (int n = 0; n < 3; ++n) {
      const int f = wid + 8 * n < 18 ? wid + 8 * n : wid;
#pragma unroll
      for (int sx = 0; sx < 3; ++sx) {
        const int src = sx + (sx >= split);
        pv[n][sx] = __builtin_amdgcn_raw_buffer_load_b128(rs, (((tile * 4 + split) * 4 + src) * 18 + f) * 1024 + lane * 16, 0, 16);
      }
    }
#pragma unroll
    for (int n = 0; n < 3; ++n) {
      const int f = wid + 8 * n;
      if (f < 18) {
        const f32x4 o = own[f * 64 + lane];
        const f32x4 p0 = __builtin_bit_cast(f32x4, pv[n][0]), p1 = __builtin_bit_cast(f32x4, pv[n][1]), p2 = __builtin_bit_cast(f32x4, pv[n][2]);
        const f32x4 s0 = split == 0 ? o : p0, s1 = split == 0 ? p0 : (split == 1 ? o : p1), s2 = split <= 1 ? p1 : (split == 2 ? o : p2), s3 = split == 3 ? o : p2;
        const f32x4 v = ((s0 + s1) + s2) + s3;
        const int i = f / 9, j = f - i * 9;
        *(LAS f32x4*)(rt + (i * 16 + m) * 148 + j * 16 + g * 4) = v;
      }
    }
  }
  WAIT_L0(); BAR();
  if (tid < 256) {
#pragma unroll
    for (int c = 0; c < 3; ++c) {
      const int ch = c * 8 + sub;
      if (ch < 18) {
        const LAS float* p0 = rt + row * 148 + ch * 8;
        const f32x4 a0 = *(const LAS f32x4*)p0, a1 = *(const LAS f32x4*)(p0 + 4);
        *(u32x4*)(Y + gofs + ch * 8) = (u32x4){pk2(bflo(rv[c].x) + rbf(a0[0]), bfhi(rv[c].x) + rbf(a0[1])), pk2(bflo(rv[c].y) + rbf(a0[2]), bfhi(rv[c].y) + rbf(a0[3])),
                                               pk2(bflo(rv[c].z) + rbf(a1[0]), bfhi(rv[c].z) + rbf(a1[1])), pk2(bflo(rv[c].w) + rbf(a1[2]), bfhi(rv[c].w) + rbf(a1[3]))};
      }
    }
  }
  STAMPEND(4, 4);
}

extern "C" void kernel_launch(void* const* d_in, const int* in_sizes, int n_in,
                              void* d_out, int out_size, void* d_ws, size_t ws_size,
                              hipStream_t stream) {
  const bf16_t* x0 = (const bf16_t*)d_in[0];
  const bf16_t* wqkv = (const bf16_t*)d_in[1];
  const bf16_t* wo = (const bf16_t*)d_in[2];
  const bf16_t* wg = (const bf16_t*)d_in[4];
  const bf16_t* wu = (const bf16_t*)d_in[5];
  const bf16_t* wd = (const bf16_t*)d_in[6];
  bf16_t* out = (bf16_t*)d_out;
  char* ws = (char*)d_ws;
  constexpr size_t XB = (size_t)S_ * D_ * 2, HB = (size_t)S_ * F_ * 2, PB = (size_t)4 * S_ * D_ * 4;
  size_t off = 0;
  float* csum = (float*)(ws + off); off += 65536;
  unsigned* cnt = (unsigned*)(ws + off); off += NCNT * 128;
  char* wp = ws + off; off += (size_t)144 * K1_WS;
  const bf16_t* xin = x0;
  k0_prep<<<288, 256, 0, stream>>>(wg, wu, csum, wqkv, wp, cnt);
  for (int l = 0; l < 2; ++l) {
    char* o = ws + off; off += XB;
    bf16_t* x1 = (bf16_t*)(ws + off); off += XB;
    f32x2* stats = (f32x2*)(ws + off); off += 65536;
    bf16_t* h = (bf16_t*)(ws + off); off += HB;
    float* P = (float*)(ws + off); off += PB;
    bf16_t* x2 = (l == 1) ? out : (bf16_t*)(ws + off); off += XB;
    k1_qkv_attn<<<256, 512, 0, stream>>>(xin, wp, o, wo, x1, stats, cnt + (l * 512 + 256) * 32);
    k3_gateup<<<256, 512, 0, stream>>>(x1, wg, wu, csum, stats, h);
    k4_down<<<256, 512, 0, stream>>>(h, wd, x1, x2, cnt + (l * 512) * 32, P);
    xin = x2;
  }
}
```

```cpp
#include <hip/hip_runtime.h>
#include <hip/hip_bf16.h>
#include <stdint.h>

typedef unsigned short bf16_t;
typedef short bf16x8 __attribute__((ext_vector_type(8)));
typedef short bf16x4 __attribute__((ext_vector_type(4)));
typedef float f32x4 __attribute__((ext_vector_type(4)));
typedef float f32x2 __attribute__((ext_vector_type(2)));
typedef float f32x16 __attribute__((ext_vector_type(16)));
typedef unsigned u32x4 __attribute__((ext_vector_type(4)));
typedef unsigned u32x2 __attribute__((ext_vector_type(2)));
#define LAS __attribute__((address_space(3)))
#define DEVI __device__ __forceinline__

constexpr int S_ = 2048, D_ = 576, F_ = 4608, NH_ = 32, DH_ = 18;
constexpr float LN_EPS = 1e-5f;

DEVI float bf2f(unsigned b) { return __uint_as_float(b << 16); }
DEVI float bflo(unsigned w) { return __uint_as_float(w << 16); }
DEVI float bfhi(unsigned w) { return __uint_as_float(w & 0xffff0000u); }
DEVI unsigned pk2(float lo, float hi) {
  typedef __bf16 bf2v __attribute__((ext_vector_type(2)));
  bf2v v; v.x = (__bf16)lo; v.y = (__bf16)hi;
  return __builtin_bit_cast(unsigned, v);
}
DEVI float rbf(float x) { return (float)((__bf16)x); }

#define STAMPW(kid, i, cond)
#define STAMPK(kid, i)
#define STAMPEND(kid, i)
#define STAMP(i) STAMPK(3, i)
#define ABL_G(x) x
#define ABL_B(x) x
#define ABL_L(...) __VA_ARGS__
#define HAUX 16
#define GV 3
#define PRIO 0
template <int N> struct IC { static constexpr int value = N; };
template <int A, int B, class F> __device__ __forceinline__ void static_for(F&& f) {
  if constexpr (A < B) { f(IC<A>{}); static_for<A + 1, B>(f); }
}
#define WAIT_V(n) asm volatile("s_waitcnt vmcnt(" #n ")" ::: "memory")
#define WAIT_L0() asm volatile("s_waitcnt lgkmcnt(0)" ::: "memory")
#define BAR() do { asm volatile("" ::: "memory"); __builtin_amdgcn_s_barrier(); asm volatile("" ::: "memory"); } while (0)

DEVI void glds16(const char* sbase, unsigned voff, unsigned ldsaddr) {
  asm volatile("s_mov_b32 m0, %2\n\ts_nop 0\n\tglobal_load_lds_dwordx4 %0, %1" ::"v"(voff), "s"(sbase), "s"(ldsaddr) : "memory");
}
DEVI void glds_1(const char* sb, unsigned v0, unsigned la) { glds16(sb, v0, la); }
DEVI void glds_2(const char* sb, unsigned v0, unsigned v1, unsigned la) {
  asm volatile("s_mov_b32 m0, %3\n\ts_nop 0\n\tglobal_load_lds_dwordx4 %0, %2\n\tglobal_load_lds_dwordx4 %1, %2 offset:1024"
               ::"v"(v0), "v"(v1), "s"(sb), "s"(la) : "memory");
}
DEVI void glds_4(const char* sb, unsigned v0, unsigned v1, unsigned v2, unsigned v3, unsigned la) {
  asm volatile("s_mov_b32 m0, %5\n\ts_nop 0\n\tglobal_load_lds_dwordx4 %0, %4\n\tglobal_load_lds_dwordx4 %1, %4 offset:1024\n\t"
               "global_load_lds_dwordx4 %2, %4 offset:2048\n\tglobal_load_lds_dwordx4 %3, %4 offset:3072"
               ::"v"(v0), "v"(v1), "v"(v2), "v"(v3), "s"(sb), "s"(la) : "memory");
}
DEVI void glds_2q(const char* sb, unsigned v0, unsigned v1, unsigned v2, unsigned la) {
  asm volatile("s_mov_b32 m0, %4\n\ts_nop 0\n\tglobal_load_lds_dwordx4 %0, %3\n\tglobal_load_lds_dwordx4 %1, %3 offset:1024\n\t"
               "s_mov_b64 exec, 0xffff\n\tglobal_load_lds_dwordx4 %2, %3 offset:2048\n\ts_mov_b64 exec, -1"
               ::"v"(v0), "v"(v1), "v"(v2), "s"(sb), "s"(la) : "memory");
}
DEVI void glds_2m(const char* sb, unsigned v0, unsigned v1, unsigned la, unsigned long long m0_, unsigned long long m1_) {
  asm volatile("s_mov_b32 m0, %3\n\ts_mov_b64 exec, %4\n\tglobal_load_lds_dwordx4 %0, %2\n\t"
               "s_mov_b64 exec, %5\n\tglobal_load_lds_dwordx4 %1, %2 offset:1024\n\ts_mov_b64 exec, -1"
               ::"v"(v0), "v"(v1), "s"(sb), "s"(la), "s"(m0_), "s"(m1_) : "memory");
}
DEVI unsigned ldsa(const LAS char* p) { return (unsigned)(size_t)p; }
DEVI bf16x8 lds_b128(const LAS char* p) { return *(const LAS bf16x8*)p; }
DEVI bf16x8 lds_tr2(const LAS char* p0, const LAS char* p1) {
  bf16x4 a = __builtin_amdgcn_ds_read_tr16_b64_v4i16((LAS bf16x4*)p0);
  bf16x4 b = __builtin_amdgcn_ds_read_tr16_b64_v4i16((LAS bf16x4*)p1);
  return __builtin_shufflevector(a, b, 0, 1, 2, 3, 4, 5, 6, 7);
}
DEVI f32x4 mfma16(bf16x8 a, bf16x8 b, f32x4 c) { return __builtin_amdgcn_mfma_f32_16x16x32_bf16(a, b, c, 0, 0, 0); }

constexpr int K1_WR = 16 * 448 + 32;
constexpr int K1_WS = 14464;
constexpr int NCNT = 1024;

__global__ __launch_bounds__(256) void k0_prep(const bf16_t* __restrict__ Wqkv, char* __restrict__ Wp, unsigned* __restrict__ cnt) {
  STAMPK(0, 0);
  const int u = blockIdx.x, hg = u / 18, sl = u - hg * 18;
  char* dst = Wp + (size_t)u * K1_WS;
  u32x4 v[4];
#pragma unroll
  for (int i = 0; i < 4; ++i) {
    const int ci = i * 256 + threadIdx.x;
    const int reg = ci >= 450, cj = ci - reg * 450;
    const int rr = cj / 28;
    int ch = cj - rr * 28; if (ch > 26) ch = 26;
    const int seg = ch / 9, cc = ch - seg * 9;
    const int gcol = (seg == 2 ? 0 : (seg + 1) * D_) + hg * 72 + cc * 8;
    const int rc = rr > 15 ? 15 : rr;
    const int k = sl * 32 + (rc >> 3) * 16 + reg * 8 + (rc & 7);
    v[i] = *(const u32x4*)(Wqkv + (size_t)k * (3 * D_) + gcol);
  }
  if (u < NCNT / 256) cnt[(u * 256 + threadIdx.x) * 32] = 0u;
#pragma unroll
  for (int i = 0; i < 4; ++i) {
    const int ci = i * 256 + threadIdx.x;
    if (ci < 904) *(u32x4*)(dst + ci * 16) = v[i];
  }
  STAMPEND(0, 1);
}

constexpr int K1_XT = 80 * 128;
constexpr int K1_WBASE = 4 * K1_XT;
constexpr int K1_AUX = 156928;
constexpr int K1_LDS = K1_AUX + 768;
constexpr int K1_QS = 440;
constexpr int K1_OB = 35200, K1_OS = 160;
constexpr int K2_WR = 9216 + 128, K2_WS = K2_WR + 9216, K2_WBASE = 45568;

DEVI float dot2bf(unsigned a, unsigned b, float c) {
  typedef __bf16 bf2v __attribute__((ext_vector_type(2)));
  return __builtin_amdgcn_fdot2_f32_bf16(__builtin_bit_cast(bf2v, a), __builtin_bit_cast(bf2v, b), c, false);
}

template <int NJ, int J0>
DEVI void k2_gemm(LAS char* lds, f32x4 (&acc)[5], int lane, int wm, int wk, const char* Wo, const unsigned (&woff)[3], unsigned wdst,
                  const char* OB, int obase, unsigned* __restrict__ cnt, int tid, int wid) {
  const int g = lane >> 4, m = lane & 15, q4 = (lane >> 2) & 3, p4 = lane & 3;
  const int xa = (wk * 4 + g) * 512 + (wm * 16 + m) * 16;
  const int wa = K2_WBASE + (g & 1) * K2_WR + ((wk * 2 + (g >> 1)) * 8 + q4) * 288 + J0 * 32 + p4 * 8;
  auto rdW = [&](bf16x8 (&wf)[NJ], int slot) {
#pragma unroll
    for (int j = 0; j < NJ; ++j) wf[j] = lds_tr2(lds + wa + slot * K2_WS + j * 32, lds + wa + slot * K2_WS + j * 32 + 4 * 288);
  };
  bf16x8 w0[NJ], w1[NJ], w2[NJ];
  rdW(w0, 2); rdW(w1, 3); rdW(w2, 4);
  WAIT_L0(); BAR();
#pragma unroll
  for (int t = 6; t < 9; ++t) glds_2q(Wo + (size_t)t * 64 * D_ * 2, woff[0], woff[1], woff[2], wdst + (t - 4) * K2_WS);
  if (wid == 0) {
    unsigned spins = 0;
    while ((unsigned)__builtin_amdgcn_readfirstlane(__hip_atomic_load(cnt, __ATOMIC_RELAXED, __HIP_MEMORY_SCOPE_AGENT)) < 8u) {
      __builtin_amdgcn_s_sleep(1);
      if (++spins > (1u << 22)) break;
    }
  }
  BAR();
  STAMPK(1, 5);
  {
    const __amdgpu_buffer_rsrc_t rso = __builtin_amdgcn_make_buffer_rsrc((void*)OB, 0, S_ * D_ * 2, 0x00020000);
    u32x4 ov[5];
#pragma unroll
    for (int n = 0; n < 5; ++n) ov[n] = __builtin_amdgcn_raw_buffer_load_b128(rso, obase + ((n < 4 || tid < 256) ? n * 512 + tid : tid) * 16, 0, 16);
#pragma unroll
    for (int n = 0; n < 5; ++n) if (n < 4 || tid < 256) *(LAS u32x4*)(lds + (n * 512 + tid) * 16) = ov[n];
  }
  WAIT_V(0); WAIT_L0(); BAR();
  STAMPK(1, 6);
  bf16x8 xf[9];
#pragma unroll
  for (int t = 0; t < 9; ++t) xf[t] = lds_b128(lds + xa + t * 4096);
#pragma unroll
  for (int j = 0; j < NJ; ++j) acc[j] = mfma16(w0[j], xf[0], (f32x4){0, 0, 0, 0});
  rdW(w0, 5);
#pragma unroll
  for (int j = 0; j < NJ; ++j) acc[j] = mfma16(w1[j], xf[1], acc[j]);
  rdW(w1, 0);
#pragma unroll
  for (int j = 0; j < NJ; ++j) acc[j] = mfma16(w2[j], xf[2], acc[j]);
  rdW(w2, 1);
#pragma unroll
  for (int j = 0; j < NJ; ++j) acc[j] = mfma16(w0[j], xf[3], acc[j]);
  rdW(w0, 2);
#pragma unroll
  for (int j = 0; j < NJ; ++j) acc[j] = mfma16(w1[j], xf[4], acc[j]);
  rdW(w1, 3);
#pragma unroll
  for (int j = 0; j < NJ; ++j) acc[j] = mfma16(w2[j], xf[5], acc[j]);
  rdW(w2, 4);
#pragma unroll
  for (int j = 0; j < NJ; ++j) acc[j] = mfma16(w0[j], xf[6], acc[j]);
#pragma unroll
  for (int j = 0; j < NJ; ++j) acc[j] = mfma16(w1[j], xf[7], acc[j]);
#pragma unroll
  for (int j = 0; j < NJ; ++j) acc[j] = mfma16(w2[j], xf[8], acc[j]);
  WAIT_L0(); BAR();
  LAS float* rt = (LAS float*)lds;
#pragma unroll
  for (int j = 0; j < NJ; ++j) *(LAS f32x4*)(rt + (wk * 32 + wm * 16 + m) * 148 + (J0 + j) * 16 + g * 4) = acc[j];
}

__global__ __launch_bounds__(512, 2) void k1_qkv_attn(const bf16_t* __restrict__ X, const char* __restrict__ Wp, char* __restrict__ OB,
                                                       const bf16_t* __restrict__ Wo, bf16_t* __restrict__ Y, f32x2* __restrict__ stats,
                                                       unsigned* __restrict__ cnt, const bf16_t* __restrict__ Wg, const bf16_t* __restrict__ Wu,
                                                       float* __restrict__ csum) {
  __shared__ __attribute__((aligned(16))) char lds_[K1_LDS];
  LAS char* lds = (LAS char*)lds_;
  const unsigned lbase = ldsa(lds);
  const int tid = threadIdx.x, lane = tid & 63, wid = __builtin_amdgcn_readfirstlane(tid >> 6);
  const int bid = blockIdx.x, xcd = bid & 7, idx = bid >> 3;
  const int tt = (xcd >> 1) * 8 + (idx & 7), hg = (xcd & 1) * 4 + (idx >> 3);
  const int t0 = tt * 64 - 16;
  STAMPK(1, 0);

  unsigned xoff[2], woff[2];
#pragma unroll
  for (int i = 0; i < 2; ++i) {
    int ci = i * 64 + lane; if (ci > 79) ci = 79;
    ci += wid * 80;
    const int row = ci >> 3, c = (ci & 7) ^ ((row >> 1) & 7);
    int grow = t0 + row; if (grow < 0) grow = 0;
    xoff[i] = (unsigned)((grow * D_ + c * 8) * 2 + 1024 - i * 1024);
  }
  woff[0] = (unsigned)((wid * 113 + lane) * 16); woff[1] = woff[0];
  const unsigned wdst = lbase + K1_WBASE + wid * 1808;
  const unsigned long long xmask = wid == 0 ? ~0xffull : ~0ull;
  auto stageX = [&](int kt) {
    glds_2m((const char*)X + kt * 128 - 1024, xoff[0], xoff[1], lbase + (kt & 3) * K1_XT + wid * 1280, xmask, 0xffffull);
  };
  auto stageW = [&](int s) {
    glds_2m(Wp + (size_t)(hg * 18 + s) * K1_WS, woff[0], woff[1], wdst + (s & 7) * K1_WS, ~0ull, 0x1ffffffffffffull);
  };
  auto stageT = [&](int t) { stageW(2 * t); stageX(t); stageW(2 * t + 1); };
  stageT(0); stageT(1);
  if (tid >= 64 && tid < 64 + 32) {
    const int c = tid - 64;
    *(LAS u32x4*)(lds + (c >> 3) * K1_XT + (c & 7) * 16) = (u32x4){0x3f803f80u, 0x3f803f80u, 0x3f803f80u, 0x3f803f80u};
  }
  const int th = hg >> 2, ct = hg & 3;
  unsigned w2off[3];
#pragma unroll
  for (int i = 0; i < 3; ++i) {
    int ci = i * 64 + lane; if (ci > 143) ci = 143;
    const int rr = ci / 18, c = ci - rr * 18;
    w2off[i] = (unsigned)(((wid * 8 + rr) * D_ + ct * 144 + c * 8) * 2 - i * 1024);
  }
  const unsigned w2dst = lbase + K2_WBASE + (wid & 1) * K2_WR + (wid >> 1) * 2304;
  auto stageB = [&](int kt) { glds_2q((const char*)Wo + (size_t)kt * 64 * D_ * 2, w2off[0], w2off[1], w2off[2], w2dst + ((kt + 2) % 6) * K2_WS); };

  const int g = lane >> 4, m = lane & 15, q4 = (lane >> 2) & 3, p4 = lane & 3;
  const int xa0 = m * 128 + ((g ^ (m >> 1)) * 16);
  const int wa0 = K1_WBASE + (g & 1) * K1_WR + ((g >> 1) * 8 + q4) * 448 + (wid < 7 ? wid : 0) * 64 + p4 * 8;
  f32x4 acc[5][2];
#pragma unroll
  for (int i = 0; i < 5; ++i) { acc[i][0] = (f32x4){0, 0, 0, 0}; acc[i][1] = (f32x4){0, 0, 0, 0}; }
  LAS f32x2* ms = (LAS f32x2*)(lds + K1_AUX);
  auto top = [&](int t) {
    __builtin_amdgcn_sched_barrier(0);
    if (t < 6) { asm volatile("s_waitcnt vmcnt(12) lgkmcnt(0)" ::: "memory"); } else { asm volatile("s_waitcnt vmcnt(6) lgkmcnt(0)" ::: "memory"); }
    BAR();
    __builtin_amdgcn_sched_barrier(0);
  };
  auto stg = [&](int t) {
    if (t < 5) stageT(t + 4);
    if (t == 6) { stageB(0); stageB(1); }
    if (t == 7) { stageB(2); stageB(3); }
  };
  WAIT_V(6); WAIT_L0(); BAR();
  STAMPK(1, 1);
  auto rdX = [&](bf16x8 (&xf)[5], int kt, int ks) {
#pragma unroll
    for (int i = 0; i < 5; ++i) xf[i] = lds_b128(lds + ((xa0 ^ (ks * 64)) + (kt & 3) * K1_XT + i * 2048));
  };
  auto mainloop = [&](auto EARLY) {
    bf16x8 xfa[5], xfb[5], wfa[2], wfb[2];
    auto rdW = [&](bf16x8 (&wf)[2], int s) {
#pragma unroll
      for (int j = 0; j < 2; ++j) wf[j] = lds_tr2(lds + (wa0 + (s & 7) * K1_WS + j * 32), lds + (wa0 + (s & 7) * K1_WS + j * 32 + 4 * 448));
    };
    auto step = [&](bf16x8 (&wc)[2], bf16x8 (&xc)[5], bf16x8 (&wn)[2], bf16x8 (&xn)[5], int s) {
#pragma unroll
      for (int i = 0; i < 5; ++i) {
        if (i < 2) wn[i] = lds_tr2(lds + (wa0 + (s & 7) * K1_WS + i * 32), lds + (wa0 + (s & 7) * K1_WS + i * 32 + 4 * 448));
        acc[i][0] = mfma16(wc[0], xc[i], acc[i][0]);
        __builtin_amdgcn_sched_barrier(0);
        xn[i] = lds_b128(lds + ((xa0 ^ ((s & 1) * 64)) + ((s >> 1) & 3) * K1_XT + i * 2048));
        acc[i][1] = mfma16(wc[1], xc[i], acc[i][1]);
        __builtin_amdgcn_sched_barrier(0);
      }
    };
    rdX(xfa, 0, 0); rdW(wfa, 0);
    stageT(2);
    step(wfa, xfa, wfb, xfb, 1);
    stageT(3);
#pragma unroll
    for (int t = 0; t < 8; ++t) {
      top(t);
      if (decltype(EARLY)::value) stg(t);
      step(wfb, xfb, wfa, xfa, 2 * t + 2);
      if (!decltype(EARLY)::value) stg(t);
      step(wfa, xfa, wfb, xfb, 2 * t + 3);
    }
#pragma unroll
    for (int i = 0; i < 5; ++i)
#pragma unroll
      for (int j = 0; j < 2; ++j) acc[i][j] = mfma16(wfb[j], xfb[i], acc[i][j]);
  };
  if (wid < 4) {
    mainloop(IC<1>{});
  } else if (wid < 7) {
    mainloop(IC<0>{});
  } else {
    bf16x8 xfa[5], xfb[5];
    const bf16x8 ones = {0x3f80, 0x3f80, 0x3f80, 0x3f80, 0x3f80, 0x3f80, 0x3f80, 0x3f80};
    auto step = [&](bf16x8 (&xc)[5], bf16x8 (&xn)[5], int s) {
#pragma unroll
      for (int i = 0; i < 5; ++i) {
        acc[i][0] = mfma16(xc[i], xc[i], acc[i][0]);
        xn[i] = lds_b128(lds + ((xa0 ^ ((s & 1) * 64)) + ((s >> 1) & 3) * K1_XT + i * 2048));
        acc[i][1] = mfma16(ones, xc[i], acc[i][1]);
        __builtin_amdgcn_sched_barrier(0);
      }
    };
    rdX(xfa, 0, 0);
    stageT(2);
    step(xfa, xfb, 1);
    stageT(3);
#pragma unroll
    for (int t = 0; t < 8; ++t) {
      top(t);
      step(xfb, xfa, 2 * t + 2);
      stg(t);
      step(xfa, xfb, 2 * t + 3);
    }
#pragma unroll
    for (int i = 0; i < 5; ++i) { acc[i][0] = mfma16(xfb[i], xfb[i], acc[i][0]); acc[i][1] = mfma16(ones, xfb[i], acc[i][1]); }
#pragma unroll
    for (int i = 0; i < 5; ++i) {
      const float q = (m & 2) ? ((m & 1) ? acc[i][0][3] : acc[i][0][2]) : ((m & 1) ? acc[i][0][1] : acc[i][0][0]);
      const float mean = acc[i][1][0] * (1.0f / D_);
      const float var = fmaxf(q * (1.0f / D_) - mean * mean, 0.f);
      if (g == (m >> 2)) ms[i * 16 + m] = (f32x2){mean, rsqrtf(var + LN_EPS)};
    }
  }
  WAIT_L0(); BAR();
  STAMPK(1, 2);
  stageB(4); stageB(5);
  const bool docs = csum != nullptr && bid < 144;
  u32x4 cv[9];
  if (docs) {
    const bf16_t* Wc = (bid < 72 ? Wg : Wu) + (size_t)((tid >> 3) * 9) * F_ + (bid % 72) * 64 + (tid & 7) * 8;
#pragma unroll
    for (int i = 0; i < 9; ++i) cv[i] = *(const u32x4*)(Wc + (size_t)i * F_);
  }
  if (wid < 7) {
    f32x4 cs[2];
#pragma unroll
    for (int j = 0; j < 2; ++j)
#pragma unroll
      for (int r = 0; r < 4; ++r) cs[j][r] = __shfl(acc[0][j][r], lane & 48);
#pragma unroll
    for (int i = 0; i < 5; ++i) {
      const f32x2 mr = ms[i * 16 + m];
#pragma unroll
      for (int j = 0; j < 2; ++j) {
        const int col = (wid * 2 + j) * 16 + g * 4;
        const f32x4 v = (acc[i][j] - cs[j] * mr.x) * mr.y;
        if (col < 216) *(LAS u32x2*)(lds + (i * 16 + m) * K1_QS + col * 2) = (u32x2){pk2(v[0], v[1]), pk2(v[2], v[3])};
      }
    }
  }
  WAIT_L0(); BAR();
  STAMPK(1, 3);
  {
    const int half = lane & 1, hh = wid & 3, tok = (wid >> 2) * 32 + (lane >> 1);
    const int row = tok + 16;
    const int gpos = t0 + row;
    const LAS unsigned* bp = (const LAS unsigned*)(lds + row * K1_QS + hh * 36 + half * 20);
    unsigned qv[5];
#pragma unroll
    for (int d = 0; d < 4; ++d) qv[d] = bp[72 + d];
    qv[4] = half ? 0u : bp[72 + 4];
    float lg[8];
#pragma unroll
    for (int w = 0; w < 8; ++w) {
      const LAS unsigned* kp = bp - w * (2 * K1_QS / 4);
      float s = 0.f;
#pragma unroll
      for (int d = 0; d < 5; ++d) s = dot2bf(qv[d], kp[d], s);
      s += __shfl_xor(s, 1);
      lg[w] = (gpos - 2 * w >= 0) ? s * (0.23570226039551584f * 1.4426950408889634f) : -1e30f;
    }
    float mx = lg[0];
#pragma unroll
    for (int w = 1; w < 8; ++w) mx = fmaxf(mx, lg[w]);
    float den = 0.f;
#pragma unroll
    for (int w = 0; w < 8; ++w) { lg[w] = (gpos - 2 * w >= 0) ? __builtin_amdgcn_exp2f(lg[w] - mx) : 0.f; den += lg[w]; }
    const float inv = __builtin_amdgcn_rcpf(den);
    float ov[10];
#pragma unroll
    for (int d = 0; d < 10; ++d) ov[d] = 0.f;
#pragma unroll
    for (int w = 0; w < 8; ++w) {
      const LAS unsigned* vp = bp - w * (2 * K1_QS / 4) + 36;
      const float pw = lg[w] * inv;
#pragma unroll
      for (int d = 0; d < 5; ++d) { const unsigned vv = vp[d]; ov[2 * d] += pw * bflo(vv); ov[2 * d + 1] += pw * bfhi(vv); }
    }
    LAS unsigned* op = (LAS unsigned*)(lds + K1_OB + tok * K1_OS + hh * 36 + half * 20);
#pragma unroll
    for (int d = 0; d < 5; ++d) if (d < 4 || !half) op[d] = pk2(ov[2 * d], ov[2 * d + 1]);
  }
  WAIT_L0(); BAR();
  {
    const __amdgpu_buffer_rsrc_t rso = __builtin_amdgcn_make_buffer_rsrc((void*)OB, 0, S_ * D_ * 2, 0x00020000);
#pragma unroll
    for (int i = 0; i < 2; ++i) {
      const int ci = i * 512 + tid;
      if (ci < 576) {
        const int r = ci & 31, q = ci >> 5, hf = q / 9, cc = q - hf * 9;
        const u32x4 v = *(const LAS u32x4*)(lds + K1_OB + (hf * 32 + r) * K1_OS + cc * 16);
        __builtin_amdgcn_raw_buffer_store_b128(v, rso, (((tt * 2 + hf) * 72 + hg * 9 + cc) * 32 + r) * 16, 0, 16);
      }
    }
  }
  const int prow = (tid >> 3) & 31, sub = tid & 7;
  const size_t gofs = (size_t)(tt * 64 + th * 32 + prow) * D_ + ct * 144;
  u32x4 rv[3];
#pragma unroll
  for (int c = 0; c < 3; ++c) { const int ch = c * 8 + sub; rv[c] = *(const u32x4*)(X + gofs + (ch < 18 ? ch : 0) * 8); }
  WAIT_V(0); BAR();
  if (tid == 0) __hip_atomic_fetch_add(cnt + tt * 32, 1u, __ATOMIC_RELAXED, __HIP_MEMORY_SCOPE_AGENT);
  STAMPK(1, 4);
  if (docs) {
    float cs8[8] = {0, 0, 0, 0, 0, 0, 0, 0};
#pragma unroll
    for (int i = 0; i < 9; ++i)
#pragma unroll
      for (int e = 0; e < 4; ++e) { cs8[2 * e] += bflo(cv[i][e]); cs8[2 * e + 1] += bfhi(cv[i][e]); }
#pragma unroll
    for (int e = 0; e < 8; ++e) { float t = cs8[e]; t += __shfl_xor(t, 8); t += __shfl_xor(t, 16); t += __shfl_xor(t, 32); cs8[e] = t; }
    if (lane < 8) {
      LAS f32x4* cp = (LAS f32x4*)(lds + 40960 + (wid * 64 + lane * 8) * 4);
      cp[0] = (f32x4){cs8[0], cs8[1], cs8[2], cs8[3]}; cp[1] = (f32x4){cs8[4], cs8[5], cs8[6], cs8[7]};
    }
  }
  {
    f32x4 acc2[5];
    const int wm = wid & 1, wk = (wid >> 1) & 1;
    const int obase = (tt * 2 + th) * 72 * 32 * 16;
    if (wid < 4) k2_gemm<5, 0>(lds, acc2, lane, wm, wk, (const char*)Wo, w2off, w2dst, OB, obase, cnt + tt * 32, tid, wid);
    else k2_gemm<4, 5>(lds, acc2, lane, wm, wk, (const char*)Wo, w2off, w2dst, OB, obase, cnt + tt * 32, tid, wid);
  }
  WAIT_L0(); BAR();
  STAMPK(1, 7);
  if (docs && tid >= 448) {
    const LAS float* cp = (const LAS float*)(lds + 40960) + lane;
    float t = 0.f;
#pragma unroll
    for (int w = 0; w < 8; ++w) t += cp[w * 64];
    csum[(bid < 72 ? 0 : F_) + (bid % 72) * 64 + lane] = t;
  }
  if (tid < 256) {
    const LAS float* rt = (const LAS float*)lds;
    float xv[3][8];
    float s = 0.f;
#pragma unroll
    for (int c = 0; c < 3; ++c) {
      const int ch = c * 8 + sub;
      const bool ok = ch < 18;
      const LAS float* p0 = rt + prow * 148 + (ok ? ch : 0) * 8;
      const f32x4 a0 = *(const LAS f32x4*)p0 + *(const LAS f32x4*)(p0 + 32 * 148);
      const f32x4 a1 = *(const LAS f32x4*)(p0 + 4) + *(const LAS f32x4*)(p0 + 32 * 148 + 4);
      xv[c][0] = rbf(bflo(rv[c].x) + rbf(a0[0])); xv[c][1] = rbf(bfhi(rv[c].x) + rbf(a0[1]));
      xv[c][2] = rbf(bflo(rv[c].y) + rbf(a0[2])); xv[c][3] = rbf(bfhi(rv[c].y) + rbf(a0[3]));
      xv[c][4] = rbf(bflo(rv[c].z) + rbf(a1[0])); xv[c][5] = rbf(bfhi(rv[c].z) + rbf(a1[1]));
      xv[c][6] = rbf(bflo(rv[c].w) + rbf(a1[2])); xv[c][7] = rbf(bfhi(rv[c].w) + rbf(a1[3]));
      if (ok) {
        *(u32x4*)(Y + gofs + ch * 8) = (u32x4){pk2(xv[c][0], xv[c][1]), pk2(xv[c][2], xv[c][3]), pk2(xv[c][4], xv[c][5]), pk2(xv[c][6], xv[c][7])};
#pragma unroll
        for (int e = 0; e < 8; ++e) s += xv[c][e];
      }
    }
    s += __shfl_xor(s, 1); s += __shfl_xor(s, 2); s += __shfl_xor(s, 4);
    const float mean = s * (1.0f / 144.0f);
    float q = 0.f;
#pragma unroll
    for (int c = 0; c < 3; ++c)
      if (c * 8 + sub < 18) {
#pragma unroll
        for (int e = 0; e < 8; ++e) { const float d = xv[c][e] - mean; q += d * d; }
      }
    q += __shfl_xor(q, 1); q += __shfl_xor(q, 2); q += __shfl_xor(q, 4);
    if (sub == 0) stats[(tt * 64 + th * 32 + prow) * 4 + ct] = (f32x2){mean, q};
  }
  STAMPEND(1, 8);
}

constexpr int K3_XS = 32768, K3_UOFF = 9216 + 128, K3_WS = K3_UOFF + 9216;
constexpr int K3_WBASE = 2 * K3_XS, K3_AUX = K3_WBASE + 4 * K3_WS;
constexpr int K3_CS = K3_AUX + 8192;
constexpr int K3_DUMMY = K3_CS + 2048;
constexpr int K3_LDS = K3_DUMMY + 4096;

template <int NJ, int J0>
DEVI void k3_body(LAS char* lds, const char* X, const char* wsrc, bf16_t* __restrict__ H, const unsigned (&xoff)[4], const unsigned (&woff)[3],
                  unsigned xdst, unsigned wdst, int lane, int wr, int pm, int pf, const char* Wd, unsigned pfoff) {
  auto stageX = [&](int t) { glds_4(X + t * 128, xoff[0], xoff[1], xoff[2], xoff[3], xdst + (t & 1) * K3_XS); };
  auto stageW = [&](int s) { glds_2q(wsrc + (size_t)s * 32 * F_ * 2, woff[0], woff[1], woff[2], wdst + (s & 3) * K3_WS); };
  const int tk = lane & 31, h = lane >> 5, q4 = (lane >> 2) & 3, p4 = lane & 3, isup = (lane >> 4) & 1;
  const int xa = (wr * 64 + tk) * 128 + ((h ^ ((tk >> 1) & 7)) * 16);
  const int wa = K3_WBASE + isup * K3_UOFF + (h * 8 + q4) * 288 + J0 * 32 + p4 * 8;
  auto rdW = [&](int wo, int kk, int j) -> bf16x8 {
    const LAS char* p = lds + (wa + wo + kk * (16 * 288) + j * 32);
    return lds_tr2(p, p + 4 * 288);
  };
  auto rdX = [&](int xo, int k4, int i) -> bf16x8 {
    return lds_b128(lds + ((xa ^ (k4 * 32)) + xo + i * 4096));
  };
  f32x16 acc[2][NJ];
  f32x16 zero16;
#pragma unroll
  for (int r = 0; r < 16; ++r) zero16[r] = 0.f;
  WAIT_V(10); BAR();
  float rs[2];
  {
    bf16x8 mf[2];
#pragma unroll
    for (int i = 0; i < 2; ++i) {
      const LAS f32x4* sp = (const LAS f32x4*)(lds + K3_AUX + (wr * 64 + i * 32 + tk) * 32);
      const f32x4 s01 = sp[0], s23 = sp[1];
      const float mean = (s01[0] + s01[2] + s23[0] + s23[2]) * 0.25f;
      const float d0 = s01[0] - mean, d1 = s01[2] - mean, d2 = s23[0] - mean, d3 = s23[2] - mean;
      const float m2 = (s01[1] + s01[3]) + (s23[1] + s23[3]) + 144.0f * ((d0 * d0 + d1 * d1) + (d2 * d2 + d3 * d3));
      rs[i] = m2 * (1.0f / D_) + LN_EPS;
      const float m0 = rbf(-mean), m1 = rbf(-mean - m0), m2_ = (-mean - m0) - m1;
      u32x4 w = {pk2(m0, m0), pk2(m0, m1), pk2(m1, m2_), 0u};
      if (h) w = (u32x4){0u, 0u, 0u, 0u};
      mf[i] = __builtin_bit_cast(bf16x8, w);
    }
#pragma unroll
    for (int j = 0; j < NJ; ++j) {
      const float c = *(const LAS float*)(lds + K3_CS + (isup * 144 + (J0 + j) * 16 + (lane & 15)) * 4);
      const float c0 = rbf(c), c1 = rbf(c - c0), c2 = (c - c0) - c1;
      const u32x4 w = {pk2(c0, c1), pk2(c2, c0), pk2(c1, c0), 0u};
      const bf16x8 cf = __builtin_bit_cast(bf16x8, w);
#pragma unroll
      for (int i = 0; i < 2; ++i) acc[i][j] = __builtin_amdgcn_mfma_f32_32x32x16_bf16(cf, mf[i], zero16, 0, 0, 0);
    }
  }
  bf16x8 xfa[2], xfb[2], xfc[2], wf[4];
  WAIT_V(3); BAR();
  STAMP(1);
  xfa[0] = rdX(0, 0, 0); xfa[1] = rdX(0, 0, 1);
  wf[0] = rdW(0, 0, 0); wf[1] = rdW(0, 0, 1);
  stageX(1); stageW(2);

  if (PRIO == 1 && NJ == 5) __builtin_amdgcn_s_setprio(1);
  if (PRIO == 2 && NJ == 4) __builtin_amdgcn_s_setprio(1);
#pragma unroll 1
  for (int t = 0; t < 9; ++t) {
    const int tp = t & 1;
    const int wo = tp * (2 * K3_WS), won = (1 - tp) * (2 * K3_WS);
    const int xo = tp * K3_XS, xon = (1 - tp) * K3_XS;
#pragma unroll
    for (int k4 = 0; k4 < 4; ++k4) {
      if (k4 == 0) {
        if (t < 8) { WAIT_V(7); }
        ABL_B(BAR());
      }
      if (k4 == 2) {
        if (t < 7) { WAIT_V(3); } else if (t == 7) { WAIT_V(0); }
        WAIT_L0();
        ABL_B(BAR());
        if (t == 7) {
          glds_4(Wd, pfoff, pfoff, pfoff, pfoff, ldsa(lds) + K3_DUMMY);
          glds_2(Wd + 3136, pfoff, pfoff, ldsa(lds) + K3_DUMMY);
        }
      }
      __builtin_amdgcn_sched_barrier(0);
#pragma unroll
      for (int j = 0; j < NJ; ++j) {
        const int n = k4 * NJ + j;
        if (j == 0) {
          if (k4 == (NJ == 4 ? 0 : 1) && t < 8) stageW(2 * t + 3);
          if (k4 == (NJ == 4 ? 2 : 3) && t < 7) { stageW(2 * t + 4); stageX(t + 2); }
        }
        {
          const int n2 = n + 2, k42 = n2 / NJ, j2 = n2 % NJ;
          if (k42 < 4) { ABL_L(wf[n2 & 3] = rdW(wo + (k42 >> 1) * K3_WS, k42 & 1, j2);) }
          else { ABL_L(wf[n2 & 3] = rdW(won, 0, j2);) }
        }
        if (j == 1) {
          if (k4 == 0) { ABL_L(xfb[0] = rdX(xo, 1, 0); xfb[1] = rdX(xo, 1, 1);) }
          if (k4 == 1) { ABL_L(xfa[0] = rdX(xo, 2, 0); xfa[1] = rdX(xo, 2, 1);) }
          if (k4 == 3) { ABL_L(xfa[0] = rdX(xon, 0, 0); xfa[1] = rdX(xon, 0, 1);) }
        }
        if (j == 2 && k4 == 1) { ABL_L(xfc[0] = rdX(xo, 3, 0); xfc[1] = rdX(xo, 3, 1);) }
#pragma unroll
        for (int i = 0; i < 2; ++i)
          acc[i][j] = __builtin_amdgcn_mfma_f32_32x32x16_bf16(wf[n & 3], k4 == 1 ? xfb[i] : (k4 == 3 ? xfc[i] : xfa[i]), acc[i][j], 0, 0, 0);
        __builtin_amdgcn_sched_barrier(0);
      }
    }
  }
  STAMP(2);
  const __amdgpu_buffer_rsrc_t rsh = __builtin_amdgcn_make_buffer_rsrc((void*)H, 0, S_ * F_ * 2, 0x00020000);
  const int hofs = (((pf * 9 + J0) * 2 + h) * S_ + pm * 256 + wr * 64 + tk) * 16;
#pragma unroll
  for (int i = 0; i < 2; ++i) {
    const float c1 = -1.4426950408889634f * __builtin_amdgcn_rsqf(rs[i]);
    const f32x2 c1v = {c1, c1}, inv = {rs[i], rs[i]};
#pragma unroll
    for (int j = 0; j < NJ; ++j) {
      u32x4 hw;
#pragma unroll
      for (int r = 0; r < 4; ++r) {
        const f32x2 ag = {acc[i][j][2 * r], acc[i][j][2 * r + 1]}, au = {acc[i][j][8 + 2 * r], acc[i][j][8 + 2 * r + 1]};
        const f32x2 z = ag * c1v;
        const f32x2 e = {__builtin_amdgcn_exp2f(z.x), __builtin_amdgcn_exp2f(z.y)};
        const f32x2 d = e * inv + inv;
        const f32x2 rc = {__builtin_amdgcn_rcpf(d.x), __builtin_amdgcn_rcpf(d.y)};
        const f32x2 hv = (ag * au) * rc;
        hw[r] = pk2(hv.x, hv.y);
      }
      __builtin_amdgcn_raw_buffer_store_b128(hw, rsh, hofs + j * (2 * S_ * 16) + i * 512, 0, HAUX);
    }
  }
  STAMP(3);
}

__global__ __launch_bounds__(512, 2) void k3_gateup(const bf16_t* __restrict__ X, const bf16_t* __restrict__ Wg, const bf16_t* __restrict__ Wu,
                                                     const float* __restrict__ csum, const f32x2* __restrict__ stats, bf16_t* __restrict__ H,
                                                     const bf16_t* __restrict__ Wd) {
  __shared__ __attribute__((aligned(16))) char lds_[K3_LDS];
  LAS char* lds = (LAS char*)lds_;
  const unsigned lbase = ldsa(lds);
  const int tid = threadIdx.x, lane = tid & 63, wid = __builtin_amdgcn_readfirstlane(tid >> 6);
  const int bid = blockIdx.x, xcd = bid & 7, idx = bid >> 3;
  const int pm = (xcd >> 2) * 4 + (idx & 3), pf = (xcd & 3) * 8 + (idx >> 2);
  const int wr = wid & 3, wc = wid >> 2;
  STAMP(0);
  glds16((const char*)(stats + pm * 256 * 4), (unsigned)(tid * 16), lbase + K3_AUX + wid * 1024);
  {
    const int l36 = lane < 36 ? lane : 35;
    if (lane < 36) glds16((const char*)(csum + (wid & 1) * F_ + pf * 144), (unsigned)(l36 * 16), lbase + K3_CS + (wid & 1) * 576);
  }
  unsigned xoff[4], woff[3];
#pragma unroll
  for (int i = 0; i < 4; ++i) {
    const int row = (wid * 4 + i) * 8 + (lane >> 3);
    const int c = (lane & 7) ^ ((row >> 1) & 7);
    xoff[i] = (unsigned)(((pm * 256 + row) * D_ + c * 8) * 2 - i * 1024);
  }
  const unsigned xdst = lbase + wid * 4096;
  glds_4((const char*)X, xoff[0], xoff[1], xoff[2], xoff[3], xdst);
#pragma unroll
  for (int i = 0; i < 3; ++i) {
    int ci = i * 64 + lane; if (ci > 143) ci = 143;
    const int kk = ci / 18, cp = ci - kk * 18;
    woff[i] = (unsigned)((((wid & 3) * 8 + kk) * F_ + pf * 144 + cp * 8) * 2 - i * 1024);
  }
  const char* wsrc = (const char*)((wid >> 2) ? Wu : Wg);
  const unsigned wdst = lbase + K3_WBASE + (wid >> 2) * K3_UOFF + (wid & 3) * 2304;
  glds_2q(wsrc, woff[0], woff[1], woff[2], wdst);
  glds_2q(wsrc + 32 * F_ * 2, woff[0], woff[1], woff[2], wdst + K3_WS);
  const unsigned pfoff = (unsigned)((xcd & 3) * (1152 * D_ * 2) + (idx * 8 + wid) * 5184 + lane * 16);
  if (wc == 0) k3_body<5, 0>(lds, (const char*)X, wsrc, H, xoff, woff, xdst, wdst, lane, wr, pm, pf, (const char*)Wd, pfoff);
  else k3_body<4, 5>(lds, (const char*)X, wsrc, H, xoff, woff, xdst, wdst, lane, wr, pm, pf, (const char*)Wd, pfoff);
  STAMPEND(3, 4);
}

constexpr int K4_XB = 16384, K4_STG = K4_XB + 18432, K4_LDS = 4 * K4_STG;

template <int NJ, int J0>
DEVI void k4_body(LAS char* lds, const char* X, const char* W, unsigned xoff, const unsigned (&woff)[3], unsigned sdst, unsigned wdst,
                  int lane, int wm, int split, int tile, float* __restrict__ P, unsigned* __restrict__ cnt) {
  auto stage = [&](int kt, int buf) {
    glds_2(X + (size_t)kt * (8 * S_ * 16), xoff, xoff, sdst + buf * K4_STG);
    glds_2q(W + (size_t)kt * 64 * D_ * 2, woff[0], woff[1], woff[2], wdst + buf * K4_STG);
  };
  const int g = lane >> 4, m = lane & 15, q4 = (lane >> 2) & 3, p4 = lane & 3;
  const int xa = g * 2048 + (wm * 32 + m) * 16;
  const int wa = K4_XB + ((g >> 1) * 16 + (g & 1) * 4 + q4) * 288 + J0 * 32 + p4 * 8;
  f32x4 acc[2][NJ];
#pragma unroll
  for (int i = 0; i < 2; ++i)
#pragma unroll
    for (int j = 0; j < NJ; ++j) acc[i][j] = (f32x4){0, 0, 0, 0};
  bf16x8 xa_[2], wa_[NJ], xb_[2], wb_[NJ];
  auto rd = [&](bf16x8 (&xf)[2], bf16x8 (&wf)[NJ], const LAS char* sb, int ks) {
#pragma unroll
    for (int i = 0; i < 2; ++i) xf[i] = lds_b128(sb + xa + ks * 8192 + i * 256);
#pragma unroll
    for (int j = 0; j < NJ; ++j) wf[j] = lds_tr2(sb + wa + ks * (32 * 288) + j * 32, sb + wa + ks * (32 * 288) + j * 32 + 8 * 288);
  };
  auto step = [&](bf16x8 (&xc)[2], bf16x8 (&wc)[NJ], bf16x8 (&xn)[2], bf16x8 (&wn)[NJ], const LAS char* sb, int ks) {
#pragma unroll
    for (int j = 0; j < NJ; ++j) {
      if (j < 2) xn[j] = lds_b128(sb + xa + ks * 8192 + j * 256);
      wn[j] = lds_tr2(sb + wa + ks * (32 * 288) + j * 32, sb + wa + ks * (32 * 288) + j * 32 + 8 * 288);
#pragma unroll
      for (int i = 0; i < 2; ++i) acc[i][j] = mfma16(wc[j], xc[i], acc[i][j]);
      __builtin_amdgcn_sched_barrier(0);
    }
  };
  WAIT_V(5); BAR();
  STAMPK(4, 1);
  rd(xa_, wa_, lds, 0);
  stage(2, 2);
  step(xa_, wa_, xb_, wb_, lds, 1);
  stage(3, 3);
  auto iter = [&](auto INFL, auto MORE, int t) {
    asm volatile("s_waitcnt vmcnt(%0) lgkmcnt(0)" ::"n"(decltype(INFL)::value * 5) : "memory");
    BAR();
    __builtin_amdgcn_sched_barrier(0);
    const LAS char* sn = lds + ((t + 1) & 3) * K4_STG;
    if (decltype(MORE)::value && NJ == 4) stage(t + 4, t & 3);
    step(xb_, wb_, xa_, wa_, sn, 0);
    if (decltype(MORE)::value && NJ == 5) stage(t + 4, t & 3);
    step(xa_, wa_, xb_, wb_, sn, 1);
  };
#pragma unroll 1
  for (int t = 0; t < 14; ++t) iter(IC<2>{}, IC<1>{}, t);
  iter(IC<2>{}, IC<0>{}, 14);
  iter(IC<1>{}, IC<0>{}, 15);
  iter(IC<0>{}, IC<0>{}, 16);
#pragma unroll
  for (int j = 0; j < NJ; ++j)
#pragma unroll
    for (int i = 0; i < 2; ++i) acc[i][j] = mfma16(wb_[j], xb_[i], acc[i][j]);
  STAMPK(4, 2);
  const __amdgpu_buffer_rsrc_t rs = __builtin_amdgcn_make_buffer_rsrc((void*)P, 0, 64 * 16 * 18 * 1024, 0x00020000);
  if (wm != split) {
    const int sb = ((tile * 4 + wm) * 4 + split) * 18 * 1024 + lane * 16;
#pragma unroll
    for (int i = 0; i < 2; ++i)
#pragma unroll
      for (int j = 0; j < NJ; ++j)
        __builtin_amdgcn_raw_buffer_store_b128(__builtin_bit_cast(u32x4, acc[i][j]), rs, sb + (i * 9 + J0 + j) * 1024, 0, 16);
    WAIT_V(0);
    if (lane == 0) __hip_atomic_fetch_add(cnt + (tile * 4 + wm) * 32, 1u, __ATOMIC_RELAXED, __HIP_MEMORY_SCOPE_AGENT);
  } else {
    LAS f32x4* own = (LAS f32x4*)(lds + 2 * K4_STG);
#pragma unroll
    for (int i = 0; i < 2; ++i)
#pragma unroll
      for (int j = 0; j < NJ; ++j) own[(i * 9 + J0 + j) * 64 + lane] = acc[i][j];
    if (NJ == 5) {
      unsigned spins = 0;
      while ((unsigned)__builtin_amdgcn_readfirstlane(__hip_atomic_load(cnt + (tile * 4 + split) * 32, __ATOMIC_RELAXED, __HIP_MEMORY_SCOPE_AGENT)) < 6u) {
        __builtin_amdgcn_s_sleep(1);
        if (++spins > (1u << 22)) break;
      }
    }
  }
}

__global__ __launch_bounds__(512, 2) void k4_down(const bf16_t* __restrict__ X, const bf16_t* __restrict__ W, const bf16_t* __restrict__ R,
                                                   bf16_t* __restrict__ Y, unsigned* __restrict__ cnt, float* __restrict__ P) {
  __shared__ __attribute__((aligned(16))) char lds_[K4_LDS];
  LAS char* lds = (LAS char*)lds_;
  const unsigned lbase = ldsa(lds);
  const int tid = threadIdx.x, lane = tid & 63, wid = __builtin_amdgcn_readfirstlane(tid >> 6);
  const int bid = blockIdx.x, xcd = bid & 7, idx = bid >> 3;
  const int split = xcd & 3;
  const int m0 = (xcd >> 2) * 1024 + (idx >> 2) * 128, n0 = (idx & 3) * 144, k0 = split * 1152;
  const int wm = wid & 3, wn = wid >> 2;
  STAMPK(4, 0);
  const unsigned xoff = (unsigned)((((k0 >> 3) + wid) * S_ + m0 + lane) * 16);
  unsigned woff[3];
#pragma unroll
  for (int i = 0; i < 3; ++i) {
    int ci = i * 64 + lane; if (ci > 143) ci = 143;
    const int rr = ci / 18, c = ci - rr * 18;
    woff[i] = (unsigned)(((k0 + wid * 8 + rr) * D_ + n0 + c * 8) * 2 - i * 1024);
  }
  const unsigned sdst = lbase + wid * 2048, wdst = lbase + K4_XB + wid * 2304;
#pragma unroll
  for (int s = 0; s < 2; ++s) {
    glds_2((const char*)X + (size_t)s * (8 * S_ * 16), xoff, xoff, sdst + s * K4_STG);
    glds_2q((const char*)W + (size_t)s * 64 * D_ * 2, woff[0], woff[1], woff[2], wdst + s * K4_STG);
  }
  const int tile = (m0 >> 7) * 4 + (idx & 3);
  if (wn == 0) k4_body<5, 0>(lds, (const char*)X, (const char*)W, xoff, woff, sdst, wdst, lane, wm, split, tile, P, cnt);
  else k4_body<4, 5>(lds, (const char*)X, (const char*)W, xoff, woff, sdst, wdst, lane, wm, split, tile, P, cnt);
  const int row = (tid >> 3) & 31, sub = tid & 7;
  const size_t gofs = (size_t)(m0 + split * 32 + row) * D_ + n0;
  u32x4 rv[3];
#pragma unroll
  for (int c = 0; c < 3; ++c) { const int ch = c * 8 + sub; rv[c] = *(const u32x4*)(R + gofs + (ch < 18 ? ch : 0) * 8); }
  WAIT_L0(); BAR();
  STAMPK(4, 3);
  const int g = lane >> 4, m = lane & 15;
  LAS f32x4* own = (LAS f32x4*)(lds + 2 * K4_STG);
  LAS float* rt = (LAS float*)lds;
  {
    const __amdgpu_buffer_rsrc_t rs = __builtin_amdgcn_make_buffer_rsrc((void*)P, 0, 64 * 16 * 18 * 1024, 0x00020000);
    u32x4 pv[3][3];
#pragma unroll
    for (int n = 0; n < 3; ++n) {
      const int f = wid + 8 * n < 18 ? wid + 8 * n : wid;
#pragma unroll
      for (int sx = 0; sx < 3; ++sx) {
        const int src = sx + (sx >= split);
        pv[n][sx] = __builtin_amdgcn_raw_buffer_load_b128(rs, (((tile * 4 + split) * 4 + src) * 18 + f) * 1024 + lane * 16, 0, 16);
      }
    }
#pragma unroll
    for (int n = 0; n < 3; ++n) {
      const int f = wid + 8 * n;
      if (f < 18) {
        const f32x4 o = own[f * 64 + lane];
        const f32x4 p0 = __builtin_bit_cast(f32x4, pv[n][0]), p1 = __builtin_bit_cast(f32x4, pv[n][1]), p2 = __builtin_bit_cast(f32x4, pv[n][2]);
        const f32x4 s0 = split == 0 ? o : p0, s1 = split == 0 ? p0 : (split == 1 ? o : p1), s2 = split <= 1 ? p1 : (split == 2 ? o : p2), s3 = split == 3 ? o : p2;
        const f32x4 v = ((s0 + s1) + s2) + s3;
        const int i = f / 9, j = f - i * 9;
        *(LAS f32x4*)(rt + (i * 16 + m) * 148 + j * 16 + g * 4) = v;
      }
    }
  }
  WAIT_L0(); BAR();
  if (tid < 256) {
#pragma unroll
    for (int c = 0; c < 3; ++c) {
      const int ch = c * 8 + sub;
      if (ch < 18) {
        const LAS float* p0 = rt + row * 148 + ch * 8;
        const f32x4 a0 = *(const LAS f32x4*)p0, a1 = *(const LAS f32x4*)(p0 + 4);
        *(u32x4*)(Y + gofs + ch * 8) = (u32x4){pk2(bflo(rv[c].x) + rbf(a0[0]), bfhi(rv[c].x) + rbf(a0[1])), pk2(bflo(rv[c].y) + rbf(a0[2]), bfhi(rv[c].y) + rbf(a0[3])),
                                               pk2(bflo(rv[c].z) + rbf(a1[0]), bfhi(rv[c].z) + rbf(a1[1])), pk2(bflo(rv[c].w) + rbf(a1[2]), bfhi(rv[c].w) + rbf(a1[3]))};
      }
    }
  }
  STAMPEND(4, 4);
}

extern "C" void kernel_launch(void* const* d_in, const int* in_sizes, int n_in,
                              void* d_out, int out_size, void* d_ws, size_t ws_size,
                              hipStream_t stream) {
  const bf16_t* x0 = (const bf16_t*)d_in[0];
  const bf16_t* wqkv = (const bf16_t*)d_in[1];
  const bf16_t* wo = (const bf16_t*)d_in[2];
  const bf16_t* wg = (const bf16_t*)d_in[4];
  const bf16_t* wu = (const bf16_t*)d_in[5];
  const bf16_t* wd = (const bf16_t*)d_in[6];
  bf16_t* out = (bf16_t*)d_out;
  char* ws = (char*)d_ws;
  constexpr size_t XB = (size_t)S_ * D_ * 2, HB = (size_t)S_ * F_ * 2, PB = (size_t)4 * S_ * D_ * 4;
  size_t off = 0;
  float* csum = (float*)(ws + off); off += 65536;
  unsigned* cnt = (unsigned*)(ws + off); off += NCNT * 128;
  char* wp = ws + off; off += (size_t)144 * K1_WS;
  const bf16_t* xin = x0;
  k0_prep<<<144, 256, 0, stream>>>(wqkv, wp, cnt);
  for (int l = 0; l < 2; ++l) {
    char* o = ws + off; off += XB;
    bf16_t* x1 = (bf16_t*)(ws + off); off += XB;
    f32x2* stats = (f32x2*)(ws + off); off += 65536;
    bf16_t* h = (bf16_t*)(ws + off); off += HB;
    float* P = (float*)(ws + off); off += PB;
    bf16_t* x2 = (l == 1) ? out : (bf16_t*)(ws + off); off += XB;
    k1_qkv_attn<<<256, 512, 0, stream>>>(xin, wp, o, wo, x1, stats, cnt + (l * 512 + 256) * 32, wg, wu, l == 0 ? csum : nullptr);
    k3_gateup<<<256, 512, 0, stream>>>(x1, wg, wu, csum, stats, h, wd);
    k4_down<<<256, 512, 0, stream>>>(h, wd, x1, x2, cnt + (l * 512) * 32, P);
    xin = x2;
  }
}
```

```cpp
#include <hip/hip_runtime.h>
#include <hip/hip_bf16.h>
#include <stdint.h>

typedef unsigned short bf16_t;
typedef short bf16x8 __attribute__((ext_vector_type(8)));
typedef short bf16x4 __attribute__((ext_vector_type(4)));
typedef float f32x4 __attribute__((ext_vector_type(4)));
typedef float f32x2 __attribute__((ext_vector_type(2)));
typedef float f32x16 __attribute__((ext_vector_type(16)));
typedef unsigned u32x4 __attribute__((ext_vector_type(4)));
typedef unsigned u32x2 __attribute__((ext_vector_type(2)));
#define LAS __attribute__((address_space(3)))
#define DEVI __device__ __forceinline__

constexpr int S_ = 2048, D_ = 576, F_ = 4608, NH_ = 32, DH_ = 18;
constexpr float LN_EPS = 1e-5f;

DEVI float bf2f(unsigned b) { return __uint_as_float(b << 16); }
DEVI float bflo(unsigned w) { return __uint_as_float(w << 16); }
DEVI float bfhi(unsigned w) { return __uint_as_float(w & 0xffff0000u); }
DEVI unsigned pk2(float lo, float hi) {
  typedef __bf16 bf2v __attribute__((ext_vector_type(2)));
  bf2v v; v.x = (__bf16)lo; v.y = (__bf16)hi;
  return __builtin_bit_cast(unsigned, v);
}
template <int CTRL> DEVI float dpp(float x) { return __builtin_bit_cast(float, __builtin_amdgcn_update_dpp(0, __builtin_bit_cast(int, x), CTRL, 0xf, 0xf, true)); }
DEVI float sum8(float x) {
  x += dpp<0xB1>(x); x += dpp<0x4E>(x); x += dpp<0x141>(x); return x;
}
DEVI float rbf(float x) { return (float)((__bf16)x); }

#define RSTAMP(i)
#define RDUMP(n)
#define STAMPW(kid, i, cond)
#define STAMPK(kid, i)
#define STAMPEND(kid, i)
#define STAMP(i) STAMPK(3, i)
#define ABL_G(x) x
#define ABL_B(x) x
#define ABL_L(...) __VA_ARGS__
#define HAUX 16
#define GV 3
#define PRIO 0
template <int N> struct IC { static constexpr int value = N; };
template <int A, int B, class F> __device__ __forceinline__ void static_for(F&& f) {
  if constexpr (A < B) { f(IC<A>{}); static_for<A + 1, B>(f); }
}
#define WAIT_V(n) asm volatile("s_waitcnt vmcnt(" #n ")" ::: "memory")
#define WAIT_L0() asm volatile("s_waitcnt lgkmcnt(0)" ::: "memory")
#define BAR() do { asm volatile("" ::: "memory"); __builtin_amdgcn_s_barrier(); asm volatile("" ::: "memory"); } while (0)

DEVI void glds16(const char* sbase, unsigned voff, unsigned ldsaddr) {
  asm volatile("s_mov_b32 m0, %2\n\ts_nop 0\n\tglobal_load_lds_dwordx4 %0, %1" ::"v"(voff), "s"(sbase), "s"(ldsaddr) : "memory");
}
DEVI void glds_1(const char* sb, unsigned v0, unsigned la) { glds16(sb, v0, la); }
DEVI void glds_2(const char* sb, unsigned v0, unsigned v1, unsigned la) {
  asm volatile("s_mov_b32 m0, %3\n\ts_nop 0\n\tglobal_load_lds_dwordx4 %0, %2\n\tglobal_load_lds_dwordx4 %1, %2 offset:1024"
               ::"v"(v0), "v"(v1), "s"(sb), "s"(la) : "memory");
}
DEVI void glds_4(const char* sb, unsigned v0, unsigned v1, unsigned v2, unsigned v3, unsigned la) {
  asm volatile("s_mov_b32 m0, %5\n\ts_nop 0\n\tglobal_load_lds_dwordx4 %0, %4\n\tglobal_load_lds_dwordx4 %1, %4 offset:1024\n\t"
               "global_load_lds_dwordx4 %2, %4 offset:2048\n\tglobal_load_lds_dwordx4 %3, %4 offset:3072"
               ::"v"(v0), "v"(v1), "v"(v2), "v"(v3), "s"(sb), "s"(la) : "memory");
}
DEVI void glds_2q(const char* sb, unsigned v0, unsigned v1, unsigned v2, unsigned la) {
  asm volatile("s_mov_b32 m0, %4\n\ts_nop 0\n\tglobal_load_lds_dwordx4 %0, %3\n\tglobal_load_lds_dwordx4 %1, %3 offset:1024\n\t"
               "s_mov_b64 exec, 0xffff\n\tglobal_load_lds_dwordx4 %2, %3 offset:2048\n\ts_mov_b64 exec, -1"
               ::"v"(v0), "v"(v1), "v"(v2), "s"(sb), "s"(la) : "memory");
}
DEVI void glds_2m(const char* sb, unsigned v0, unsigned v1, unsigned la, unsigned long long m0_, unsigned long long m1_) {
  asm volatile("s_mov_b32 m0, %3\n\ts_mov_b64 exec, %4\n\tglobal_load_lds_dwordx4 %0, %2\n\t"
               "s_mov_b64 exec, %5\n\tglobal_load_lds_dwordx4 %1, %2 offset:1024\n\ts_mov_b64 exec, -1"
               ::"v"(v0), "v"(v1), "s"(sb), "s"(la), "s"(m0_), "s"(m1_) : "memory");
}
DEVI unsigned ldsa(const LAS char* p) { return (unsigned)(size_t)p; }
DEVI bf16x8 lds_b128(const LAS char* p) { return *(const LAS bf16x8*)p; }
DEVI bf16x8 lds_tr2(const LAS char* p0, const LAS char* p1) {
  bf16x4 a = __builtin_amdgcn_ds_read_tr16_b64_v4i16((LAS bf16x4*)p0);
  bf16x4 b = __builtin_amdgcn_ds_read_tr16_b64_v4i16((LAS bf16x4*)p1);
  return __builtin_shufflevector(a, b, 0, 1, 2, 3, 4, 5, 6, 7);
}
DEVI f32x4 mfma16(bf16x8 a, bf16x8 b, f32x4 c) { return __builtin_amdgcn_mfma_f32_16x16x32_bf16(a, b, c, 0, 0, 0); }

constexpr int K1_WR = 16 * 448 + 32;
constexpr int K1_WS = 14464;
constexpr int NCNT = 1024;

__global__ __launch_bounds__(256) void k0_prep(const bf16_t* __restrict__ Wqkv, char* __restrict__ Wp, unsigned* __restrict__ cnt) {
  STAMPK(0, 0);
  const int u = blockIdx.x, hg = u / 18, sl = u - hg * 18;
  char* dst = Wp + (size_t)u * K1_WS;
  u32x4 v[4];
#pragma unroll
  for (int i = 0; i < 4; ++i) {
    const int ci = i * 256 + threadIdx.x;
    const int reg = ci >= 450, cj = ci - reg * 450;
    const int rr = cj / 28;
    int ch = cj - rr * 28; if (ch > 26) ch = 26;
    const int seg = ch / 9, cc = ch - seg * 9;
    const int gcol = (seg == 2 ? 0 : (seg + 1) * D_) + hg * 72 + cc * 8;
    const int rc = rr > 15 ? 15 : rr;
    const int k = sl * 32 + (rc >> 3) * 16 + reg * 8 + (rc & 7);
    v[i] = *(const u32x4*)(Wqkv + (size_t)k * (3 * D_) + gcol);
  }
  if (u < NCNT / 256) cnt[(u * 256 + threadIdx.x) * 32] = 0u;
#pragma unroll
  for (int i = 0; i < 4; ++i) {
    const int ci = i * 256 + threadIdx.x;
    if (ci < 904) *(u32x4*)(dst + ci * 16) = v[i];
  }
  STAMPEND(0, 1);
}

constexpr int K1_XT = 80 * 128;
constexpr int K1_WBASE = 4 * K1_XT;
constexpr int K1_AUX = 156928;
constexpr int K1_LDS = K1_AUX + 768;
constexpr int K1_QS = 440;
constexpr int K1_OB = 35200, K1_OS = 160;
constexpr int K2_WR = 9216 + 128, K2_WS = K2_WR + 9216, K2_WBASE = 45568;

DEVI float dot2bf(unsigned a, unsigned b, float c) {
  typedef __bf16 bf2v __attribute__((ext_vector_type(2)));
  return __builtin_amdgcn_fdot2_f32_bf16(__builtin_bit_cast(bf2v, a), __builtin_bit_cast(bf2v, b), c, false);
}

constexpr int K2_RT = 44032;

template <int NJ, int J0>
DEVI void k2_gemm(LAS char* lds, int lane, int wm, int wk, const char* Wo, const unsigned (&woff)[3], unsigned wdst,
                  const char* OB, int obase, unsigned* __restrict__ cnt, int tid, int wid) {
  const int g = lane >> 4, m = lane & 15, q4 = (lane >> 2) & 3, p4 = lane & 3;
  const int wa = K2_WBASE + (g & 1) * K2_WR + ((wk * 2 + (g >> 1)) * 8 + q4) * 288 + J0 * 32 + p4 * 8;
  auto rdW = [&](bf16x8 (&wf)[NJ], int slot) {
#pragma unroll
    for (int j = 0; j < NJ; ++j) wf[j] = lds_tr2(lds + wa + slot * K2_WS + j * 32, lds + wa + slot * K2_WS + j * 32 + 4 * 288);
  };
  bf16x8 w[6][NJ];
  rdW(w[0], 2); rdW(w[1], 3); rdW(w[2], 4); rdW(w[3], 5);
  RSTAMP(8);
  WAIT_V(0); WAIT_L0();
  RSTAMP(9);
  BAR();
  if (tid == 0) __hip_atomic_fetch_add(cnt, 1u, __ATOMIC_RELAXED, __HIP_MEMORY_SCOPE_AGENT);
  RSTAMP(10);
#pragma unroll
  for (int t = 6; t < 9; ++t) glds_2q(Wo + (size_t)t * 64 * D_ * 2, woff[0], woff[1], woff[2], wdst + (t - 4) * K2_WS);
  rdW(w[4], 0); rdW(w[5], 1);
  RSTAMP(11);
  if (wid == 0) {
    unsigned spins = 0;
    while ((unsigned)__builtin_amdgcn_readfirstlane(__hip_atomic_load(cnt, __ATOMIC_RELAXED, __HIP_MEMORY_SCOPE_AGENT)) < 8u) {
      __builtin_amdgcn_s_sleep(1);
      if (++spins > (1u << 22)) break;
    }
  }
  WAIT_V(0); WAIT_L0(); BAR();
  RSTAMP(12);
  const __amdgpu_buffer_rsrc_t rso = __builtin_amdgcn_make_buffer_rsrc((void*)OB, 0, S_ * D_ * 2, 0x00020000);
  u32x4 xf[9];
#pragma unroll
  for (int t = 0; t < 9; ++t) xf[t] = __builtin_amdgcn_raw_buffer_load_b128(rso, obase + (wk * 4 + g) * 512 + (wm * 16 + m) * 16 + t * 4096, 0, 16);
  bf16x8 w6[NJ], w7[NJ], w8[NJ];
  rdW(w6, 2); rdW(w7, 3); rdW(w8, 4);
  RSTAMP(13);
  f32x4 acc[NJ];
#pragma unroll
  for (int j = 0; j < NJ; ++j) acc[j] = (f32x4){0, 0, 0, 0};
#pragma unroll
  for (int t = 0; t < 6; ++t)
#pragma unroll
    for (int j = 0; j < NJ; ++j) acc[j] = mfma16(w[t][j], __builtin_bit_cast(bf16x8, xf[t]), acc[j]);
#pragma unroll
  for (int j = 0; j < NJ; ++j) acc[j] = mfma16(w6[j], __builtin_bit_cast(bf16x8, xf[6]), acc[j]);
#pragma unroll
  for (int j = 0; j < NJ; ++j) acc[j] = mfma16(w7[j], __builtin_bit_cast(bf16x8, xf[7]), acc[j]);
#pragma unroll
  for (int j = 0; j < NJ; ++j) acc[j] = mfma16(w8[j], __builtin_bit_cast(bf16x8, xf[8]), acc[j]);
  RSTAMP(14);
  LAS float* rt = (LAS float*)(lds + K2_RT);
#pragma unroll
  for (int j = 0; j < NJ; ++j) *(LAS f32x4*)(rt + (wk * 32 + wm * 16 + m) * 148 + (J0 + j) * 16 + g * 4) = acc[j];
}

__global__ __launch_bounds__(512, 2) void k1_qkv_attn(const bf16_t* __restrict__ X, const char* __restrict__ Wp, char* __restrict__ OB,
                                                       const bf16_t* __restrict__ Wo, bf16_t* __restrict__ Y, f32x2* __restrict__ stats,
                                                       unsigned* __restrict__ cnt, const bf16_t* __restrict__ Wg, const bf16_t* __restrict__ Wu,
                                                       float* __restrict__ csum) {
  __shared__ __attribute__((aligned(16))) char lds_[K1_LDS];
  LAS char* lds = (LAS char*)lds_;
  const unsigned lbase = ldsa(lds);
  const int tid = threadIdx.x, lane = tid & 63, wid = __builtin_amdgcn_readfirstlane(tid >> 6);
  const int bid = blockIdx.x, xcd = bid & 7, idx = bid >> 3;
  const int tt = (xcd >> 1) * 8 + (idx & 7), hg = (xcd & 1) * 4 + (idx >> 3);
  const int t0 = tt * 64 - 16;
  STAMPK(1, 0);

  unsigned xoff[2], woff[2];
#pragma unroll
  for (int i = 0; i < 2; ++i) {
    int ci = i * 64 + lane; if (ci > 79) ci = 79;
    ci += wid * 80;
    const int row = ci >> 3, c = (ci & 7) ^ ((row >> 1) & 7);
    int grow = t0 + row; if (grow < 0) grow = 0;
    xoff[i] = (unsigned)((grow * D_ + c * 8) * 2 + 1024 - i * 1024);
  }
  woff[0] = (unsigned)((wid * 113 + lane) * 16); woff[1] = woff[0];
  const unsigned wdst = lbase + K1_WBASE + wid * 1808;
  const unsigned long long xmask = wid == 0 ? ~0xffull : ~0ull;
  auto stageX = [&](int kt) {
    glds_2m((const char*)X + kt * 128 - 1024, xoff[0], xoff[1], lbase + (kt & 3) * K1_XT + wid * 1280, xmask, 0xffffull);
  };
  auto stageW = [&](int s) {
    glds_2m(Wp + (size_t)(hg * 18 + s) * K1_WS, woff[0], woff[1], wdst + (s & 7) * K1_WS, ~0ull, 0x1ffffffffffffull);
  };
  auto stageT = [&](int t) { stageW(2 * t); stageX(t); stageW(2 * t + 1); };
  stageT(0); stageT(1);
  if (tid >= 64 && tid < 64 + 32) {
    const int c = tid - 64;
    *(LAS u32x4*)(lds + (c >> 3) * K1_XT + (c & 7) * 16) = (u32x4){0x3f803f80u, 0x3f803f80u, 0x3f803f80u, 0x3f803f80u};
  }
  const int th = hg >> 2, ct = hg & 3;
  unsigned w2off[3];
#pragma unroll
  for (int i = 0; i < 3; ++i) {
    int ci = i * 64 + lane; if (ci > 143) ci = 143;
    const int rr = ci / 18, c = ci - rr * 18;
    w2off[i] = (unsigned)(((wid * 8 + rr) * D_ + ct * 144 + c * 8) * 2 - i * 1024);
  }
  const unsigned w2dst = lbase + K2_WBASE + (wid & 1) * K2_WR + (wid >> 1) * 2304;
  auto stageB = [&](int kt) { glds_2q((const char*)Wo + (size_t)kt * 64 * D_ * 2, w2off[0], w2off[1], w2off[2], w2dst + ((kt + 2) % 6) * K2_WS); };

  const int g = lane >> 4, m = lane & 15, q4 = (lane >> 2) & 3, p4 = lane & 3;
  const int xa0 = m * 128 + ((g ^ (m >> 1)) * 16);
  const int wa0 = K1_WBASE + (g & 1) * K1_WR + ((g >> 1) * 8 + q4) * 448 + (wid < 7 ? wid : 0) * 64 + p4 * 8;
  f32x4 acc[5][2];
#pragma unroll
  for (int i = 0; i < 5; ++i) { acc[i][0] = (f32x4){0, 0, 0, 0}; acc[i][1] = (f32x4){0, 0, 0, 0}; }
  LAS f32x2* ms = (LAS f32x2*)(lds + K1_AUX);
  auto top = [&](int t) {
    __builtin_amdgcn_sched_barrier(0);
    if (t < 6) { asm volatile("s_waitcnt vmcnt(12) lgkmcnt(0)" ::: "memory"); } else { asm volatile("s_waitcnt vmcnt(6) lgkmcnt(0)" ::: "memory"); }
    BAR();
    __builtin_amdgcn_sched_barrier(0);
  };
  auto stg = [&](int t) {
    if (t < 5) stageT(t + 4);
    if (t == 6) { stageB(0); stageB(1); }
    if (t == 7) { stageB(2); stageB(3); }
  };
  WAIT_V(6); WAIT_L0(); BAR();
  RSTAMP(0);
  auto rdX = [&](bf16x8 (&xf)[5], int kt, int ks) {
#pragma unroll
    for (int i = 0; i < 5; ++i) xf[i] = lds_b128(lds + ((xa0 ^ (ks * 64)) + (kt & 3) * K1_XT + i * 2048));
  };
  auto mainloop = [&](auto EARLY) {
    bf16x8 xfa[5], xfb[5], wfa[2], wfb[2];
    auto rdW = [&](bf16x8 (&wf)[2], int s) {
#pragma unroll
      for (int j = 0; j < 2; ++j) wf[j] = lds_tr2(lds + (wa0 + (s & 7) * K1_WS + j * 32), lds + (wa0 + (s & 7) * K1_WS + j * 32 + 4 * 448));
    };
    auto step = [&](bf16x8 (&wc)[2], bf16x8 (&xc)[5], bf16x8 (&wn)[2], bf16x8 (&xn)[5], int s) {
#pragma unroll
      for (int i = 0; i < 5; ++i) {
        if (i < 2) wn[i] = lds_tr2(lds + (wa0 + (s & 7) * K1_WS + i * 32), lds + (wa0 + (s & 7) * K1_WS + i * 32 + 4 * 448));
        acc[i][0] = mfma16(wc[0], xc[i], acc[i][0]);
        __builtin_amdgcn_sched_barrier(0);
        xn[i] = lds_b128(lds + ((xa0 ^ ((s & 1) * 64)) + ((s >> 1) & 3) * K1_XT + i * 2048));
        acc[i][1] = mfma16(wc[1], xc[i], acc[i][1]);
        __builtin_amdgcn_sched_barrier(0);
      }
    };
    rdX(xfa, 0, 0); rdW(wfa, 0);
    stageT(2);
    step(wfa, xfa, wfb, xfb, 1);
    stageT(3);
#pragma unroll
    for (int t = 0; t < 8; ++t) {
      top(t);
      if (decltype(EARLY)::value) stg(t);
      step(wfb, xfb, wfa, xfa, 2 * t + 2);
      if (!decltype(EARLY)::value) stg(t);
      step(wfa, xfa, wfb, xfb, 2 * t + 3);
    }
#pragma unroll
    for (int i = 0; i < 5; ++i)
#pragma unroll
      for (int j = 0; j < 2; ++j) acc[i][j] = mfma16(wfb[j], xfb[i], acc[i][j]);
  };
  if (wid < 4) {
    mainloop(IC<1>{});
  } else if (wid < 7) {
    mainloop(IC<0>{});
  } else {
    bf16x8 xfa[5], xfb[5];
    const bf16x8 ones = {0x3f80, 0x3f80, 0x3f80, 0x3f80, 0x3f80, 0x3f80, 0x3f80, 0x3f80};
    auto step = [&](bf16x8 (&xc)[5], bf16x8 (&xn)[5], int s) {
#pragma unroll
      for (int i = 0; i < 5; ++i) {
        acc[i][0] = mfma16(xc[i], xc[i], acc[i][0]);
        xn[i] = lds_b128(lds + ((xa0 ^ ((s & 1) * 64)) + ((s >> 1) & 3) * K1_XT + i * 2048));
        acc[i][1] = mfma16(ones, xc[i], acc[i][1]);
        __builtin_amdgcn_sched_barrier(0);
      }
    };
    rdX(xfa, 0, 0);
    stageT(2);
    step(xfa, xfb, 1);
    stageT(3);
#pragma unroll
    for (int t = 0; t < 8; ++t) {
      top(t);
      step(xfb, xfa, 2 * t + 2);
      stg(t);
      step(xfa, xfb, 2 * t + 3);
    }
#pragma unroll
    for (int i = 0; i < 5; ++i) { acc[i][0] = mfma16(xfb[i], xfb[i], acc[i][0]); acc[i][1] = mfma16(ones, xfb[i], acc[i][1]); }
#pragma unroll
    for (int i = 0; i < 5; ++i) {
      const float q = (m & 2) ? ((m & 1) ? acc[i][0][3] : acc[i][0][2]) : ((m & 1) ? acc[i][0][1] : acc[i][0][0]);
      const float mean = acc[i][1][0] * (1.0f / D_);
      const float var = fmaxf(q * (1.0f / D_) - mean * mean, 0.f);
      if (g == (m >> 2)) ms[i * 16 + m] = (f32x2){mean, __builtin_amdgcn_rsqf(var + LN_EPS)};
    }
  }
  RSTAMP(1);
  WAIT_L0(); BAR();
  RSTAMP(2);
  stageB(4); stageB(5);
  const bool docs = csum != nullptr && bid < 144;
  u32x4 cv[9];
  if (docs) {
    const bf16_t* Wc = (bid < 72 ? Wg : Wu) + (size_t)((tid >> 3) * 9) * F_ + (bid % 72) * 64 + (tid & 7) * 8;
#pragma unroll
    for (int i = 0; i < 9; ++i) cv[i] = *(const u32x4*)(Wc + (size_t)i * F_);
  }
  RSTAMP(3);
  if (wid < 7) {
    f32x2 mr[5];
#pragma unroll
    for (int i = 0; i < 5; ++i) mr[i] = ms[i * 16 + m];
    f32x4 cs[2];
#pragma unroll
    for (int j = 0; j < 2; ++j)
#pragma unroll
      for (int r = 0; r < 4; ++r) cs[j][r] = dpp<0x150>(acc[0][j][r]);
    LAS char* qp = lds + m * K1_QS + (wid * 32 + g * 4) * 2;
    const bool ok1 = wid * 32 + 16 + g * 4 < 216;
#pragma unroll
    for (int i = 0; i < 5; ++i) {
      const float nm = -mr[i].x * mr[i].y;
#pragma unroll
      for (int j = 0; j < 2; ++j) {
        const f32x4 v = acc[i][j] * mr[i].y + cs[j] * nm;
        if (j == 0 || ok1) *(LAS u32x2*)(qp + i * 16 * K1_QS + j * 32) = (u32x2){pk2(v[0], v[1]), pk2(v[2], v[3])};
      }
    }
  }
  RSTAMP(4);
  WAIT_L0(); BAR();
  RSTAMP(5);
  {
    const int half = lane & 1, hh = wid & 3, tok = (wid >> 2) * 32 + (lane >> 1);
    const int row = tok + 16;
    const int gpos = t0 + row;
    const LAS unsigned* bp = (const LAS unsigned*)(lds + row * K1_QS + hh * 36 + half * 20);
    unsigned qv[5], kv[8][5], vv[8][5];
#pragma unroll
    for (int d = 0; d < 5; ++d) qv[d] = bp[72 + (d < 4 ? d : (half ? 0 : 4))];
#pragma unroll
    for (int w = 0; w < 8; ++w)
#pragma unroll
      for (int d = 0; d < 5; ++d) kv[w][d] = bp[d - w * (2 * K1_QS / 4)];
#pragma unroll
    for (int w = 0; w < 8; ++w)
#pragma unroll
      for (int d = 0; d < 5; ++d) vv[w][d] = bp[36 + d - w * (2 * K1_QS / 4)];
    __builtin_amdgcn_sched_barrier(0);
    if (half) qv[4] = 0u;
    float lg[8];
#pragma unroll
    for (int w = 0; w < 8; ++w) {
      float s = 0.f;
#pragma unroll
      for (int d = 0; d < 5; ++d) s = dot2bf(qv[d], kv[w][d], s);
      s += dpp<0xB1>(s);
      lg[w] = (gpos - 2 * w >= 0) ? s * (0.23570226039551584f * 1.4426950408889634f) : -1e30f;
    }
    float mx = lg[0];
#pragma unroll
    for (int w = 1; w < 8; ++w) mx = fmaxf(mx, lg[w]);
    float den = 0.f;
#pragma unroll
    for (int w = 0; w < 8; ++w) { lg[w] = __builtin_amdgcn_exp2f(lg[w] - mx); den += lg[w]; }
    const float inv = __builtin_amdgcn_rcpf(den);
    f32x2 ov2[5];
#pragma unroll
    for (int d = 0; d < 5; ++d) ov2[d] = (f32x2){0.f, 0.f};
#pragma unroll
    for (int w = 0; w < 8; ++w) {
      const f32x2 pw = {lg[w], lg[w]};
#pragma unroll
      for (int d = 0; d < 5; ++d) ov2[d] += pw * (f32x2){bflo(vv[w][d]), bfhi(vv[w][d])};
    }
    float ov[10];
#pragma unroll
    for (int d = 0; d < 5; ++d) { ov[2 * d] = ov2[d].x * inv; ov[2 * d + 1] = ov2[d].y * inv; }
    LAS unsigned* op = (LAS unsigned*)(lds + K1_OB + tok * K1_OS + hh * 36 + half * 20);
#pragma unroll
    for (int d = 0; d < 5; ++d) if (d < 4 || !half) op[d] = pk2(ov[2 * d], ov[2 * d + 1]);
  }
  RSTAMP(6);
  asm volatile("s_waitcnt vmcnt(6) lgkmcnt(0)" ::: "memory");
  BAR();
  RSTAMP(7);
  {
    const __amdgpu_buffer_rsrc_t rso = __builtin_amdgcn_make_buffer_rsrc((void*)OB, 0, S_ * D_ * 2, 0x00020000);
#pragma unroll
    for (int i = 0; i < 2; ++i) {
      const int ci = i * 512 + tid;
      if (ci < 576) {
        const int r = ci & 31, q = ci >> 5, hf = q / 9, cc = q - hf * 9;
        const u32x4 v = *(const LAS u32x4*)(lds + K1_OB + (hf * 32 + r) * K1_OS + cc * 16);
        __builtin_amdgcn_raw_buffer_store_b128(v, rso, (((tt * 2 + hf) * 72 + hg * 9 + cc) * 32 + r) * 16, 0, 16);
      }
    }
  }
  const int prow = tid >> 4, sub = tid & 15;
  const size_t gofs = (size_t)(tt * 64 + th * 32 + prow) * D_ + ct * 144;
  u32x4 rv[2];
  rv[0] = *(const u32x4*)(X + gofs + sub * 8);
  rv[1] = *(const u32x4*)(X + gofs + (sub < 2 ? 16 + sub : sub) * 8);
  if (docs) {
    float cs8[8] = {0, 0, 0, 0, 0, 0, 0, 0};
#pragma unroll
    for (int i = 0; i < 9; ++i)
#pragma unroll
      for (int e = 0; e < 4; ++e) { cs8[2 * e] += bflo(cv[i][e]); cs8[2 * e + 1] += bfhi(cv[i][e]); }
#pragma unroll
    for (int e = 0; e < 8; ++e) { float t = cs8[e]; t += __shfl_xor(t, 8); t += __shfl_xor(t, 16); t += __shfl_xor(t, 32); cs8[e] = t; }
    if (lane < 8) {
      LAS f32x4* cp = (LAS f32x4*)(lds + (wid * 64 + lane * 8) * 4);
      cp[0] = (f32x4){cs8[0], cs8[1], cs8[2], cs8[3]}; cp[1] = (f32x4){cs8[4], cs8[5], cs8[6], cs8[7]};
    }
  }
  {
    const int wm = wid & 1, wk = (wid >> 1) & 1;
    const int obase = (tt * 2 + th) * 72 * 32 * 16;
    if (wid < 4) k2_gemm<5, 0>(lds, lane, wm, wk, (const char*)Wo, w2off, w2dst, OB, obase, cnt + tt * 32, tid, wid);
    else k2_gemm<4, 5>(lds, lane, wm, wk, (const char*)Wo, w2off, w2dst, OB, obase, cnt + tt * 32, tid, wid);
  }
  WAIT_L0(); BAR();
  RSTAMP(15);
  if (docs && tid >= 448) {
    const LAS float* cp = (const LAS float*)lds + lane;
    float t = 0.f;
#pragma unroll
    for (int w = 0; w < 8; ++w) t += cp[w * 64];
    csum[(bid < 72 ? 0 : F_) + (bid % 72) * 64 + lane] = t;
  }
  {
    const LAS float* rt = (const LAS float*)(lds + K2_RT);
    float xv[2][8];
    float s = 0.f;
#pragma unroll
    for (int c = 0; c < 2; ++c) {
      const bool ok = c == 0 || sub < 2;
      const int ch = ok ? c * 16 + sub : sub;
      const LAS float* p0 = rt + prow * 148 + ch * 8;
      const f32x4 a0 = *(const LAS f32x4*)p0 + *(const LAS f32x4*)(p0 + 32 * 148);
      const f32x4 a1 = *(const LAS f32x4*)(p0 + 4) + *(const LAS f32x4*)(p0 + 32 * 148 + 4);
      const u32x4 y = {pk2(a0[0], a0[1]), pk2(a0[2], a0[3]), pk2(a1[0], a1[1]), pk2(a1[2], a1[3])};
      u32x4 xo;
#pragma unroll
      for (int e = 0; e < 4; ++e) {
        xo[e] = pk2(bflo(rv[c][e]) + bflo(y[e]), bfhi(rv[c][e]) + bfhi(y[e]));
        xv[c][2 * e] = ok ? bflo(xo[e]) : 0.f; xv[c][2 * e + 1] = ok ? bfhi(xo[e]) : 0.f;
      }
      if (ok) *(u32x4*)(Y + gofs + ch * 8) = xo;
#pragma unroll
      for (int e = 0; e < 8; ++e) s += xv[c][e];
    }
    s = sum8(s); s += dpp<0x140>(s);
    const float mean = s * (1.0f / 144.0f);
    float q = 0.f;
#pragma unroll
    for (int c = 0; c < 2; ++c)
#pragma unroll
      for (int e = 0; e < 8; ++e) { const float d = xv[c][e] - mean; q += (c == 0 || sub < 2) ? d * d : 0.f; }
    q = sum8(q); q += dpp<0x140>(q);
    if (sub == 0) stats[(tt * 64 + th * 32 + prow) * 4 + ct] = (f32x2){mean, q};
  }
  RSTAMP(16);
  STAMPEND(1, 1);
  RSTAMP(17);
  RDUMP(18);
}

constexpr int K3_XS = 32768, K3_UOFF = 9216 + 128, K3_WS = K3_UOFF + 9216;
constexpr int K3_WBASE = 2 * K3_XS, K3_AUX = K3_WBASE + 4 * K3_WS;
constexpr int K3_CS = K3_AUX + 8192;
constexpr int K3_DUMMY = K3_CS + 2048;
constexpr int K3_LDS = K3_DUMMY + 4096;

template <int NJ, int J0>
DEVI void k3_body(LAS char* lds, const char* X, const char* wsrc, bf16_t* __restrict__ H, const unsigned (&xoff)[4], const unsigned (&woff)[3],
                  unsigned xdst, unsigned wdst, int lane, int wr, int pm, int pf, const char* Wd, unsigned pfoff) {
  auto stageX = [&](int t) { glds_4(X + t * 128, xoff[0], xoff[1], xoff[2], xoff[3], xdst + (t & 1) * K3_XS); };
  auto stageW = [&](int s) { glds_2q(wsrc + (size_t)s * 32 * F_ * 2, woff[0], woff[1], woff[2], wdst + (s & 3) * K3_WS); };
  const int tk = lane & 31, h = lane >> 5, q4 = (lane >> 2) & 3, p4 = lane & 3, isup = (lane >> 4) & 1;
  const int xa = (wr * 64 + tk) * 128 + ((h ^ ((tk >> 1) & 7)) * 16);
  const int wa = K3_WBASE + isup * K3_UOFF + (h * 8 + q4) * 288 + J0 * 32 + p4 * 8;
  auto rdW = [&](int wo, int kk, int j) -> bf16x8 {
    const LAS char* p = lds + (wa + wo + kk * (16 * 288) + j * 32);
    return lds_tr2(p, p + 4 * 288);
  };
  auto rdX = [&](int xo, int k4, int i) -> bf16x8 {
    return lds_b128(lds + ((xa ^ (k4 * 32)) + xo + i * 4096));
  };
  f32x16 acc[2][NJ];
#pragma unroll
  for (int i = 0; i < 2; ++i)
#pragma unroll
    for (int j = 0; j < NJ; ++j)
#pragma unroll
      for (int r = 0; r < 16; ++r) acc[i][j][r] = 0.f;
  auto correction = [&]() {
    bf16x8 mf[2];
#pragma unroll
    for (int i = 0; i < 2; ++i) {
      const LAS f32x4* sp = (const LAS f32x4*)(lds + K3_AUX + (wr * 64 + i * 32 + tk) * 32);
      const f32x4 s01 = sp[0], s23 = sp[1];
      const float mean = (s01[0] + s01[2] + s23[0] + s23[2]) * 0.25f;
      const float m0 = rbf(-mean), m1 = rbf(-mean - m0), m2_ = (-mean - m0) - m1;
      u32x4 w = {pk2(m0, m0), pk2(m0, m1), pk2(m1, m2_), 0u};
      if (h) w = (u32x4){0u, 0u, 0u, 0u};
      mf[i] = __builtin_bit_cast(bf16x8, w);
    }
#pragma unroll
    for (int j = 0; j < NJ; ++j) {
      const float c = *(const LAS float*)(lds + K3_CS + (isup * 144 + (J0 + j) * 16 + (lane & 15)) * 4);
      const float c0 = rbf(c), c1 = rbf(c - c0), c2 = (c - c0) - c1;
      const u32x4 w = {pk2(c0, c1), pk2(c2, c0), pk2(c1, c0), 0u};
      const bf16x8 cf = __builtin_bit_cast(bf16x8, w);
#pragma unroll
      for (int i = 0; i < 2; ++i) acc[i][j] = __builtin_amdgcn_mfma_f32_32x32x16_bf16(cf, mf[i], acc[i][j], 0, 0, 0);
    }
  };
  WAIT_V(10); BAR();
  correction();
  bf16x8 xfa[2], xfb[2], xfc[2], wf[4];
  WAIT_V(3); BAR();
  STAMP(1);
  xfa[0] = rdX(0, 0, 0); xfa[1] = rdX(0, 0, 1);
  wf[0] = rdW(0, 0, 0); wf[1] = rdW(0, 0, 1);
  stageX(1); stageW(2);

  if (PRIO == 1 && NJ == 5) __builtin_amdgcn_s_setprio(1);
  if (PRIO == 2 && NJ == 4) __builtin_amdgcn_s_setprio(1);
#pragma unroll 1
  for (int t = 0; t < 9; ++t) {
    const int tp = t & 1;
    const int wo = tp * (2 * K3_WS), won = (1 - tp) * (2 * K3_WS);
    const int xo = tp * K3_XS, xon = (1 - tp) * K3_XS;
#pragma unroll
    for (int k4 = 0; k4 < 4; ++k4) {
      if (k4 == 0) {
        if (t < 8) { WAIT_V(7); }
        ABL_B(BAR());
      }
      if (k4 == 2) {
        if (t < 7) { WAIT_V(3); } else if (t == 7) { WAIT_V(0); }
        WAIT_L0();
        ABL_B(BAR());
        if (t == 7) {
          glds_4(Wd, pfoff, pfoff, pfoff, pfoff, ldsa(lds) + K3_DUMMY);
          glds_2(Wd + 3136, pfoff, pfoff, ldsa(lds) + K3_DUMMY);
        }
      }
      __builtin_amdgcn_sched_barrier(0);
#pragma unroll
      for (int j = 0; j < NJ; ++j) {
        const int n = k4 * NJ + j;
        if (j == 0) {
          if (k4 == (NJ == 4 ? 0 : 1) && t < 8) stageW(2 * t + 3);
          if (k4 == (NJ == 4 ? 2 : 3) && t < 7) { stageW(2 * t + 4); stageX(t + 2); }
        }
        {
          const int n2 = n + 2, k42 = n2 / NJ, j2 = n2 % NJ;
          if (k42 < 4) { ABL_L(wf[n2 & 3] = rdW(wo + (k42 >> 1) * K3_WS, k42 & 1, j2);) }
          else { ABL_L(wf[n2 & 3] = rdW(won, 0, j2);) }
        }
        if (j == 1) {
          if (k4 == 0) { ABL_L(xfb[0] = rdX(xo, 1, 0); xfb[1] = rdX(xo, 1, 1);) }
          if (k4 == 1) { ABL_L(xfa[0] = rdX(xo, 2, 0); xfa[1] = rdX(xo, 2, 1);) }
          if (k4 == 3) { ABL_L(xfa[0] = rdX(xon, 0, 0); xfa[1] = rdX(xon, 0, 1);) }
        }
        if (j == 2 && k4 == 1) { ABL_L(xfc[0] = rdX(xo, 3, 0); xfc[1] = rdX(xo, 3, 1);) }
#pragma unroll
        for (int i = 0; i < 2; ++i)
          acc[i][j] = __builtin_amdgcn_mfma_f32_32x32x16_bf16(wf[n & 3], k4 == 1 ? xfb[i] : (k4 == 3 ? xfc[i] : xfa[i]), acc[i][j], 0, 0, 0);
        __builtin_amdgcn_sched_barrier(0);
      }
    }
  }
  STAMP(2);
  float rs[2];
#pragma unroll
  for (int i = 0; i < 2; ++i) {
    const LAS f32x4* sp = (const LAS f32x4*)(lds + K3_AUX + (wr * 64 + i * 32 + tk) * 32);
    const f32x4 s01 = sp[0], s23 = sp[1];
    const float mean = (s01[0] + s01[2] + s23[0] + s23[2]) * 0.25f;
    const float d0 = s01[0] - mean, d1 = s01[2] - mean, d2 = s23[0] - mean, d3 = s23[2] - mean;
    const float m2 = (s01[1] + s01[3]) + (s23[1] + s23[3]) + 144.0f * ((d0 * d0 + d1 * d1) + (d2 * d2 + d3 * d3));
    rs[i] = m2 * (1.0f / D_) + LN_EPS;
  }
  const __amdgpu_buffer_rsrc_t rsh = __builtin_amdgcn_make_buffer_rsrc((void*)H, 0, S_ * F_ * 2, 0x00020000);
  const int hofs = (((pf * 9 + J0) * 2 + h) * S_ + pm * 256 + wr * 64 + tk) * 16;
#pragma unroll
  for (int i = 0; i < 2; ++i) {
    const float c1 = -1.4426950408889634f * __builtin_amdgcn_rsqf(rs[i]);
    const f32x2 c1v = {c1, c1}, inv = {rs[i], rs[i]};
#pragma unroll
    for (int j = 0; j < NJ; ++j) {
      u32x4 hw;
#pragma unroll
      for (int r = 0; r < 4; ++r) {
        const f32x2 ag = {acc[i][j][2 * r], acc[i][j][2 * r + 1]}, au = {acc[i][j][8 + 2 * r], acc[i][j][8 + 2 * r + 1]};
        const f32x2 z = ag * c1v;
        const f32x2 e = {__builtin_amdgcn_exp2f(z.x), __builtin_amdgcn_exp2f(z.y)};
        const f32x2 d = e * inv + inv;
        const f32x2 rc = {__builtin_amdgcn_rcpf(d.x), __builtin_amdgcn_rcpf(d.y)};
        const f32x2 hv = (ag * au) * rc;
        hw[r] = pk2(hv.x, hv.y);
      }
      __builtin_amdgcn_raw_buffer_store_b128(hw, rsh, hofs + j * (2 * S_ * 16) + i * 512, 0, HAUX);
    }
  }
  STAMP(3);
}

__global__ __launch_bounds__(512, 2) void k3_gateup(const bf16_t* __restrict__ X, const bf16_t* __restrict__ Wg, const bf16_t* __restrict__ Wu,
                                                     const float* __restrict__ csum, const f32x2* __restrict__ stats, bf16_t* __restrict__ H,
                                                     const bf16_t* __restrict__ Wd) {
  __shared__ __attribute__((aligned(16))) char lds_[K3_LDS];
  LAS char* lds = (LAS char*)lds_;
  const unsigned lbase = ldsa(lds);
  const int tid = threadIdx.x, lane = tid & 63, wid = __builtin_amdgcn_readfirstlane(tid >> 6);
  const int bid = blockIdx.x, xcd = bid & 7, idx = bid >> 3;
  const int pm = (xcd >> 2) * 4 + (idx & 3), pf = (xcd & 3) * 8 + (idx >> 2);
  const int wr = wid & 3, wc = wid >> 2;
  STAMP(0);
  glds16((const char*)(stats + pm * 256 * 4), (unsigned)(tid * 16), lbase + K3_AUX + wid * 1024);
  {
    const int l36 = lane < 36 ? lane : 35;
    if (lane < 36) glds16((const char*)(csum + (wid & 1) * F_ + pf * 144), (unsigned)(l36 * 16), lbase + K3_CS + (wid & 1) * 576);
  }
  unsigned xoff[4], woff[3];
#pragma unroll
  for (int i = 0; i < 4; ++i) {
    const int row = (wid * 4 + i) * 8 + (lane >> 3);
    const int c = (lane & 7) ^ ((row >> 1) & 7);
    xoff[i] = (unsigned)(((pm * 256 + row) * D_ + c * 8) * 2 - i * 1024);
  }
  const unsigned xdst = lbase + wid * 4096;
  glds_4((const char*)X, xoff[0], xoff[1], xoff[2], xoff[3], xdst);
#pragma unroll
  for (int i = 0; i < 3; ++i) {
    int ci = i * 64 + lane; if (ci > 143) ci = 143;
    const int kk = ci / 18, cp = ci - kk * 18;
    woff[i] = (unsigned)((((wid & 3) * 8 + kk) * F_ + pf * 144 + cp * 8) * 2 - i * 1024);
  }
  const char* wsrc = (const char*)((wid >> 2) ? Wu : Wg);
  const unsigned wdst = lbase + K3_WBASE + (wid >> 2) * K3_UOFF + (wid & 3) * 2304;
  glds_2q(wsrc, woff[0], woff[1], woff[2], wdst);
  glds_2q(wsrc + 32 * F_ * 2, woff[0], woff[1], woff[2], wdst + K3_WS);
  const unsigned pfoff = (unsigned)((xcd & 3) * (1152 * D_ * 2) + (idx * 8 + wid) * 5184 + lane * 16);
  if (wc == 0) k3_body<5, 0>(lds, (const char*)X, wsrc, H, xoff, woff, xdst, wdst, lane, wr, pm, pf, (const char*)Wd, pfoff);
  else k3_body<4, 5>(lds, (const char*)X, wsrc, H, xoff, woff, xdst, wdst, lane, wr, pm, pf, (const char*)Wd, pfoff);
  STAMPEND(3, 4);
}

constexpr int K4_XB = 16384, K4_STG = K4_XB + 18432, K4_LDS = 4 * K4_STG;

template <int NJ, int J0>
DEVI void k4_body(LAS char* lds, const char* X, const char* W, unsigned xoff, const unsigned (&woff)[3], unsigned sdst, unsigned wdst,
                  int lane, int wm, int split, int tile, float* __restrict__ P, unsigned* __restrict__ cnt) {
  auto stage = [&](int kt, int buf) {
    glds_2(X + (size_t)kt * (8 * S_ * 16), xoff, xoff, sdst + buf * K4_STG);
    glds_2q(W + (size_t)kt * 64 * D_ * 2, woff[0], woff[1], woff[2], wdst + buf * K4_STG);
  };
  const int g = lane >> 4, m = lane & 15, q4 = (lane >> 2) & 3, p4 = lane & 3;
  const int xa = g * 2048 + (wm * 32 + m) * 16;
  const int wa = K4_XB + ((g >> 1) * 16 + (g & 1) * 4 + q4) * 288 + J0 * 32 + p4 * 8;
  f32x4 acc[2][NJ];
#pragma unroll
  for (int i = 0; i < 2; ++i)
#pragma unroll
    for (int j = 0; j < NJ; ++j) acc[i][j] = (f32x4){0, 0, 0, 0};
  bf16x8 xa_[2], wa_[NJ], xb_[2], wb_[NJ];
  auto rd = [&](bf16x8 (&xf)[2], bf16x8 (&wf)[NJ], const LAS char* sb, int ks) {
#pragma unroll
    for (int i = 0; i < 2; ++i) xf[i] = lds_b128(sb + xa + ks * 8192 + i * 256);
#pragma unroll
    for (int j = 0; j < NJ; ++j) wf[j] = lds_tr2(sb + wa + ks * (32 * 288) + j * 32, sb + wa + ks * (32 * 288) + j * 32 + 8 * 288);
  };
  auto step = [&](bf16x8 (&xc)[2], bf16x8 (&wc)[NJ], bf16x8 (&xn)[2], bf16x8 (&wn)[NJ], const LAS char* sb, int ks) {
#pragma unroll
    for (int j = 0; j < NJ; ++j) {
      if (j < 2) xn[j] = lds_b128(sb + xa + ks * 8192 + j * 256);
      wn[j] = lds_tr2(sb + wa + ks * (32 * 288) + j * 32, sb + wa + ks * (32 * 288) + j * 32 + 8 * 288);
#pragma unroll
      for (int i = 0; i < 2; ++i) acc[i][j] = mfma16(wc[j], xc[i], acc[i][j]);
      __builtin_amdgcn_sched_barrier(0);
    }
  };
  WAIT_V(5); BAR();
  STAMPK(4, 1);
  rd(xa_, wa_, lds, 0);
  stage(2, 2);
  step(xa_, wa_, xb_, wb_, lds, 1);
  stage(3, 3);
  auto iter = [&](auto INFL, auto MORE, int t) {
    asm volatile("s_waitcnt vmcnt(%0) lgkmcnt(0)" ::"n"(decltype(INFL)::value * 5) : "memory");
    BAR();
    __builtin_amdgcn_sched_barrier(0);
    const LAS char* sn = lds + ((t + 1) & 3) * K4_STG;
    if (decltype(MORE)::value && NJ == 4) stage(t + 4, t & 3);
    step(xb_, wb_, xa_, wa_, sn, 0);
    if (decltype(MORE)::value && NJ == 5) stage(t + 4, t & 3);
    step(xa_, wa_, xb_, wb_, sn, 1);
  };
#pragma unroll 1
  for (int t = 0; t < 14; ++t) iter(IC<2>{}, IC<1>{}, t);
  iter(IC<2>{}, IC<0>{}, 14);
  iter(IC<1>{}, IC<0>{}, 15);
  iter(IC<0>{}, IC<0>{}, 16);
#pragma unroll
  for (int j = 0; j < NJ; ++j)
#pragma unroll
    for (int i = 0; i < 2; ++i) acc[i][j] = mfma16(wb_[j], xb_[i], acc[i][j]);
  STAMPK(4, 2);
  const __amdgpu_buffer_rsrc_t rs = __builtin_amdgcn_make_buffer_rsrc((void*)P, 0, 64 * 16 * 9 * 1024, 0x00020000);
  if (wm != split) {
    const int sb = ((tile * 4 + wm) * 4 + split) * 9 * 1024 + lane * 16;
#pragma unroll
    for (int j = 0; j < NJ; ++j) {
      const u32x4 v = {pk2(acc[0][j][0], acc[0][j][1]), pk2(acc[0][j][2], acc[0][j][3]), pk2(acc[1][j][0], acc[1][j][1]), pk2(acc[1][j][2], acc[1][j][3])};
      __builtin_amdgcn_raw_buffer_store_b128(v, rs, sb + (J0 + j) * 1024, 0, 16);
    }
    WAIT_V(0);
    if (lane == 0) __hip_atomic_fetch_add(cnt + (tile * 4 + wm) * 32, 1u, __ATOMIC_RELAXED, __HIP_MEMORY_SCOPE_AGENT);
  } else {
    LAS f32x4* own = (LAS f32x4*)(lds + 2 * K4_STG);
#pragma unroll
    for (int i = 0; i < 2; ++i)
#pragma unroll
      for (int j = 0; j < NJ; ++j) own[(i * 9 + J0 + j) * 64 + lane] = acc[i][j];
    if (NJ == 5) {
      unsigned spins = 0;
      while ((unsigned)__builtin_amdgcn_readfirstlane(__hip_atomic_load(cnt + (tile * 4 + split) * 32, __ATOMIC_RELAXED, __HIP_MEMORY_SCOPE_AGENT)) < 6u) {
        __builtin_amdgcn_s_sleep(1);
        if (++spins > (1u << 22)) break;
      }
    }
  }
}

__global__ __launch_bounds__(512, 2) void k4_down(const bf16_t* __restrict__ X, const bf16_t* __restrict__ W, const bf16_t* __restrict__ R,
                                                   bf16_t* __restrict__ Y, unsigned* __restrict__ cnt, float* __restrict__ P) {
  __shared__ __attribute__((aligned(16))) char lds_[K4_LDS];
  LAS char* lds = (LAS char*)lds_;
  const unsigned lbase = ldsa(lds);
  const int tid = threadIdx.x, lane = tid & 63, wid = __builtin_amdgcn_readfirstlane(tid >> 6);
  const int bid = blockIdx.x, xcd = bid & 7, idx = bid >> 3;
  const int split = xcd & 3;
  const int m0 = (xcd >> 2) * 1024 + (idx >> 2) * 128, n0 = (idx & 3) * 144, k0 = split * 1152;
  const int wm = wid & 3, wn = wid >> 2;
  STAMPK(4, 0);
  const unsigned xoff = (unsigned)((((k0 >> 3) + wid) * S_ + m0 + lane) * 16);
  unsigned woff[3];
#pragma unroll
  for (int i = 0; i < 3; ++i) {
    int ci = i * 64 + lane; if (ci > 143) ci = 143;
    const int rr = ci / 18, c = ci - rr * 18;
    woff[i] = (unsigned)(((k0 + wid * 8 + rr) * D_ + n0 + c * 8) * 2 - i * 1024);
  }
  const unsigned sdst = lbase + wid * 2048, wdst = lbase + K4_XB + wid * 2304;
#pragma unroll
  for (int s = 0; s < 2; ++s) {
    glds_2((const char*)X + (size_t)s * (8 * S_ * 16), xoff, xoff, sdst + s * K4_STG);
    glds_2q((const char*)W + (size_t)s * 64 * D_ * 2, woff[0], woff[1], woff[2], wdst + s * K4_STG);
  }
  const int tile = (m0 >> 7) * 4 + (idx & 3);
  if (wn == 0) k4_body<5, 0>(lds, (const char*)X, (const char*)W, xoff, woff, sdst, wdst, lane, wm, split, tile, P, cnt);
  else k4_body<4, 5>(lds, (const char*)X, (const char*)W, xoff, woff, sdst, wdst, lane, wm, split, tile, P, cnt);
  const int row = (tid >> 3) & 31, sub = tid & 7;
  const size_t gofs = (size_t)(m0 + split * 32 + row) * D_ + n0;
  u32x4 rv[3];
#pragma unroll
  for (int c = 0; c < 3; ++c) { const int ch = c * 8 + sub; rv[c] = *(const u32x4*)(R + gofs + (ch < 18 ? ch : 0) * 8); }
  WAIT_L0(); BAR();
  STAMPK(4, 3);
  const int g = lane >> 4, m = lane & 15;
  LAS f32x4* own = (LAS f32x4*)(lds + 2 * K4_STG);
  LAS float* rt = (LAS float*)lds;
  {
    const __amdgpu_buffer_rsrc_t rs = __builtin_amdgcn_make_buffer_rsrc((void*)P, 0, 64 * 16 * 9 * 1024, 0x00020000);
    u32x4 pv[2][3];
#pragma unroll
    for (int n = 0; n < 2; ++n) {
      const int j = wid + 8 * n < 9 ? wid + 8 * n : wid;
#pragma unroll
      for (int sx = 0; sx < 3; ++sx) {
        const int src = sx + (sx >= split);
        pv[n][sx] = __builtin_amdgcn_raw_buffer_load_b128(rs, (((tile * 4 + split) * 4 + src) * 9 + j) * 1024 + lane * 16, 0, 16);
      }
    }
#pragma unroll
    for (int n = 0; n < 2; ++n) {
      const int j = wid + 8 * n;
      if (j < 9) {
#pragma unroll
        for (int i = 0; i < 2; ++i) {
          f32x4 v = own[(i * 9 + j) * 64 + lane];
#pragma unroll
          for (int sx = 0; sx < 3; ++sx)
            v += (f32x4){bflo(pv[n][sx][2 * i]), bfhi(pv[n][sx][2 * i]), bflo(pv[n][sx][2 * i + 1]), bfhi(pv[n][sx][2 * i + 1])};
          *(LAS f32x4*)(rt + (i * 16 + m) * 148 + j * 16 + g * 4) = v;
        }
      }
    }
  }
  WAIT_L0(); BAR();
  if (tid < 256) {
#pragma unroll
    for (int c = 0; c < 3; ++c) {
      const int ch = c * 8 + sub;
      if (ch < 18) {
        const LAS float* p0 = rt + row * 148 + ch * 8;
        const f32x4 a0 = *(const LAS f32x4*)p0, a1 = *(const LAS f32x4*)(p0 + 4);
        *(u32x4*)(Y + gofs + ch * 8) = (u32x4){pk2(bflo(rv[c].x) + rbf(a0[0]), bfhi(rv[c].x) + rbf(a0[1])), pk2(bflo(rv[c].y) + rbf(a0[2]), bfhi(rv[c].y) + rbf(a0[3])),
                                               pk2(bflo(rv[c].z) + rbf(a1[0]), bfhi(rv[c].z) + rbf(a1[1])), pk2(bflo(rv[c].w) + rbf(a1[2]), bfhi(rv[c].w) + rbf(a1[3]))};
      }
    }
  }
  STAMPEND(4, 4);
}

extern "C" void kernel_launch(void* const* d_in, const int* in_sizes, int n_in,
                              void* d_out, int out_size, void* d_ws, size_t ws_size,
                              hipStream_t stream) {
  const bf16_t* x0 = (const bf16_t*)d_in[0];
  const bf16_t* wqkv = (const bf16_t*)d_in[1];
  const bf16_t* wo = (const bf16_t*)d_in[2];
  const bf16_t* wg = (const bf16_t*)d_in[4];
  const bf16_t* wu = (const bf16_t*)d_in[5];
  const bf16_t* wd = (const bf16_t*)d_in[6];
  bf16_t* out = (bf16_t*)d_out;
  char* ws = (char*)d_ws;
  constexpr size_t XB = (size_t)S_ * D_ * 2, HB = (size_t)S_ * F_ * 2, PB = (size_t)4 * S_ * D_ * 4;
  size_t off = 0;
  float* csum = (float*)(ws + off); off += 65536;
  unsigned* cnt = (unsigned*)(ws + off); off += NCNT * 128;
  char* wp = ws + off; off += (size_t)144 * K1_WS;
  const bf16_t* xin = x0;
  k0_prep<<<144, 256, 0, stream>>>(wqkv, wp, cnt);
  char* o = ws + off; off += XB;
  bf16_t* x1 = (bf16_t*)(ws + off); off += XB;
  f32x2* stats = (f32x2*)(ws + off); off += 65536;
  bf16_t* xm = (bf16_t*)(ws + off); off += XB;
  bf16_t* h = (bf16_t*)(ws + off); off += HB;
  float* P = (float*)(ws + off); off += PB;
  for (int l = 0; l < 2; ++l) {
    bf16_t* x2 = (l == 1) ? out : xm;
    k1_qkv_attn<<<256, 512, 0, stream>>>(xin, wp, o, wo, x1, stats, cnt + (l * 512 + 256) * 32, wg, wu, l == 0 ? csum : nullptr);
    k3_gateup<<<256, 512, 0, stream>>>(x1, wg, wu, csum, stats, h, wd);
    k4_down<<<256, 512, 0, stream>>>(h, wd, x1, x2, cnt + (l * 512) * 32, P);
    xin = x2;
  }
}
```

```cpp
#include <hip/hip_runtime.h>
#include <hip/hip_bf16.h>
#include <stdint.h>

typedef unsigned short bf16_t;
typedef short bf16x8 __attribute__((ext_vector_type(8)));
typedef short bf16x4 __attribute__((ext_vector_type(4)));
typedef float f32x4 __attribute__((ext_vector_type(4)));
typedef float f32x2 __attribute__((ext_vector_type(2)));
typedef float f32x16 __attribute__((ext_vector_type(16)));
typedef unsigned u32x4 __attribute__((ext_vector_type(4)));
typedef unsigned u32x2 __attribute__((ext_vector_type(2)));
#define LAS __attribute__((address_space(3)))
#define DEVI __device__ __forceinline__

constexpr int S_ = 2048, D_ = 576, F_ = 4608, NH_ = 32, DH_ = 18;
constexpr float LN_EPS = 1e-5f;

DEVI float bf2f(unsigned b) { return __uint_as_float(b << 16); }
DEVI float bflo(unsigned w) { return __uint_as_float(w << 16); }
DEVI float bfhi(unsigned w) { return __uint_as_float(w & 0xffff0000u); }
DEVI unsigned pk2(float lo, float hi) {
  typedef __bf16 bf2v __attribute__((ext_vector_type(2)));
  bf2v v; v.x = (__bf16)lo; v.y = (__bf16)hi;
  return __builtin_bit_cast(unsigned, v);
}
template <int CTRL> DEVI float dpp(float x) { return __builtin_bit_cast(float, __builtin_amdgcn_update_dpp(0, __builtin_bit_cast(int, x), CTRL, 0xf, 0xf, true)); }
DEVI float sum8(float x) {
  x += dpp<0xB1>(x); x += dpp<0x4E>(x); x += dpp<0x141>(x); return x;
}
DEVI float rbf(float x) { return (float)((__bf16)x); }

#define RSTAMP(i)
#define RDUMP(n)
#define STAMPW(kid, i, cond)
#define STAMPK(kid, i)
#define STAMPEND(kid, i)
#define STAMP(i) STAMPK(3, i)
#define ABL_G(x) x
#define ABL_B(x) x
#define ABL_L(...) __VA_ARGS__
#define HAUX 16
#define GV 3
#define PRIO 0
template <int N> struct IC { static constexpr int value = N; };
template <int A, int B, class F> __device__ __forceinline__ void static_for(F&& f) {
  if constexpr (A < B) { f(IC<A>{}); static_for<A + 1, B>(f); }
}
#define WAIT_V(n) asm volatile("s_waitcnt vmcnt(" #n ")" ::: "memory")
#define WAIT_L0() asm volatile("s_waitcnt lgkmcnt(0)" ::: "memory")
#define BAR() do { asm volatile("" ::: "memory"); __builtin_amdgcn_s_barrier(); asm volatile("" ::: "memory"); } while (0)

DEVI void glds16(const char* sbase, unsigned voff, unsigned ldsaddr) {
  asm volatile("s_mov_b32 m0, %2\n\ts_nop 0\n\tglobal_load_lds_dwordx4 %0, %1" ::"v"(voff), "s"(sbase), "s"(ldsaddr) : "memory");
}
DEVI void glds_1(const char* sb, unsigned v0, unsigned la) { glds16(sb, v0, la); }
DEVI void glds_2(const char* sb, unsigned v0, unsigned v1, unsigned la) {
  asm volatile("s_mov_b32 m0, %3\n\ts_nop 0\n\tglobal_load_lds_dwordx4 %0, %2\n\tglobal_load_lds_dwordx4 %1, %2 offset:1024"
               ::"v"(v0), "v"(v1), "s"(sb), "s"(la) : "memory");
}
DEVI void glds_4(const char* sb, unsigned v0, unsigned v1, unsigned v2, unsigned v3, unsigned la) {
  asm volatile("s_mov_b32 m0, %5\n\ts_nop 0\n\tglobal_load_lds_dwordx4 %0, %4\n\tglobal_load_lds_dwordx4 %1, %4 offset:1024\n\t"
               "global_load_lds_dwordx4 %2, %4 offset:2048\n\tglobal_load_lds_dwordx4 %3, %4 offset:3072"
               ::"v"(v0), "v"(v1), "v"(v2), "v"(v3), "s"(sb), "s"(la) : "memory");
}
DEVI void glds_2q(const char* sb, unsigned v0, unsigned v1, unsigned v2, unsigned la) {
  asm volatile("s_mov_b32 m0, %4\n\ts_nop 0\n\tglobal_load_lds_dwordx4 %0, %3\n\tglobal_load_lds_dwordx4 %1, %3 offset:1024\n\t"
               "s_mov_b64 exec, 0xffff\n\tglobal_load_lds_dwordx4 %2, %3 offset:2048\n\ts_mov_b64 exec, -1"
               ::"v"(v0), "v"(v1), "v"(v2), "s"(sb), "s"(la) : "memory");
}
DEVI void glds_2m(const char* sb, unsigned v0, unsigned v1, unsigned la, unsigned long long m0_, unsigned long long m1_) {
  asm volatile("s_mov_b32 m0, %3\n\ts_mov_b64 exec, %4\n\tglobal_load_lds_dwordx4 %0, %2\n\t"
               "s_mov_b64 exec, %5\n\tglobal_load_lds_dwordx4 %1, %2 offset:1024\n\ts_mov_b64 exec, -1"
               ::"v"(v0), "v"(v1), "s"(sb), "s"(la), "s"(m0_), "s"(m1_) : "memory");
}
DEVI unsigned ldsa(const LAS char* p) { return (unsigned)(size_t)p; }
DEVI bf16x8 lds_b128(const LAS char* p) { return *(const LAS bf16x8*)p; }
DEVI bf16x8 lds_tr2(const LAS char* p0, const LAS char* p1) {
  bf16x4 a = __builtin_amdgcn_ds_read_tr16_b64_v4i16((LAS bf16x4*)p0);
  bf16x4 b = __builtin_amdgcn_ds_read_tr16_b64_v4i16((LAS bf16x4*)p1);
  return __builtin_shufflevector(a, b, 0, 1, 2, 3, 4, 5, 6, 7);
}
DEVI f32x4 mfma16(bf16x8 a, bf16x8 b, f32x4 c) { return __builtin_amdgcn_mfma_f32_16x16x32_bf16(a, b, c, 0, 0, 0); }

constexpr int K1_WR = 16 * 448 + 32;
constexpr int K1_WS = 14464;
constexpr int NCNT = 1024;

__global__ __launch_bounds__(256) void k0_prep(const bf16_t* __restrict__ Wqkv, char* __restrict__ Wp, unsigned* __restrict__ cnt,
                                                const bf16_t* __restrict__ Wg, const bf16_t* __restrict__ Wu, float* __restrict__ csum,
                                                const bf16_t* __restrict__ X, const bf16_t* __restrict__ Wo) {
  __shared__ float red[32][64 + 1];
  STAMPK(0, 0);
  const int b = blockIdx.x;
  if (b < 144) {
    const int u = b, hg = u / 18, sl = u - hg * 18;
    char* dst = Wp + (size_t)u * K1_WS;
    u32x4 v[4];
#pragma unroll
    for (int i = 0; i < 4; ++i) {
      const int ci = i * 256 + threadIdx.x;
      const int reg = ci >= 450, cj = ci - reg * 450;
      const int rr = cj / 28;
      int ch = cj - rr * 28; if (ch > 26) ch = 26;
      const int seg = ch / 9, cc = ch - seg * 9;
      const int gcol = (seg == 2 ? 0 : (seg + 1) * D_) + hg * 72 + cc * 8;
      const int rc = rr > 15 ? 15 : rr;
      const int k = sl * 32 + (rc >> 3) * 16 + reg * 8 + (rc & 7);
      v[i] = *(const u32x4*)(Wqkv + (size_t)k * (3 * D_) + gcol);
    }
    if (u < NCNT / 256) cnt[(u * 256 + threadIdx.x) * 32] = 0u;
#pragma unroll
    for (int i = 0; i < 4; ++i) {
      const int ci = i * 256 + threadIdx.x;
      if (ci < 904) *(u32x4*)(dst + ci * 16) = v[i];
    }
  } else if (b < 144 + 432) {
    const int u = b - 144, kq = u / 144, cb = u - kq * 144;
    const bf16_t* W = (cb < 72) ? Wg : Wu;
    const int col0 = (cb % 72) * 64;
    const int ch = threadIdx.x & 7, rg = threadIdx.x >> 3;
    float s[8] = {0, 0, 0, 0, 0, 0, 0, 0};
#pragma unroll
    for (int i = 0; i < 6; ++i) {
      const u32x4 v = *(const u32x4*)(W + (size_t)(kq * 192 + rg * 6 + i) * F_ + col0 + ch * 8);
      s[0] += bflo(v.x); s[1] += bfhi(v.x); s[2] += bflo(v.y); s[3] += bfhi(v.y);
      s[4] += bflo(v.z); s[5] += bfhi(v.z); s[6] += bflo(v.w); s[7] += bfhi(v.w);
    }
#pragma unroll
    for (int j = 0; j < 8; ++j) red[rg][ch * 8 + j] = s[j];
    __syncthreads();
    if (threadIdx.x < 64) {
      float t = 0.f;
#pragma unroll
      for (int r = 0; r < 32; ++r) t += red[r][threadIdx.x];
      csum[kq * 2 * F_ + (cb < 72 ? 0 : F_) + col0 + threadIdx.x] = t;
    }
  } else {
    const int u = b - 576;
    const char* src = u < 144 ? (const char*)X + (size_t)u * 16384 : (const char*)Wo + (size_t)(u - 144) * 16384;
    const int lim = u < 144 ? 16384 : (D_ * D_ * 2 - (u - 144) * 16384 < 16384 ? D_ * D_ * 2 - (u - 144) * 16384 : 16384);
#pragma unroll
    for (int i = 0; i < 4; ++i) {
      const int o = (i * 256 + threadIdx.x) * 16;
      if (o < lim) { const u32x4 v = *(const u32x4*)(src + o); asm volatile("" ::"v"(v)); }
    }
  }
  STAMPEND(0, 1);
}

constexpr int K1_XT = 80 * 128;
constexpr int K1_WBASE = 4 * K1_XT;
constexpr int K1_AUX = 156928;
constexpr int K1_LDS = K1_AUX + 768;
constexpr int K1_QS = 440;
constexpr int K1_OB = 35200, K1_OS = 160;
constexpr int K2_WR = 9216 + 128, K2_WS = K2_WR + 9216, K2_WBASE = 45568;

DEVI float dot2bf(unsigned a, unsigned b, float c) {
  typedef __bf16 bf2v __attribute__((ext_vector_type(2)));
  return __builtin_amdgcn_fdot2_f32_bf16(__builtin_bit_cast(bf2v, a), __builtin_bit_cast(bf2v, b), c, false);
}

constexpr int K2_RT = 44032;

template <int NJ, int J0>
DEVI void k2_gemm(LAS char* lds, int lane, int wm, int wk, const char* Wo, const unsigned (&woff)[3], unsigned wdst,
                  const char* OB, int obase, unsigned* __restrict__ cnt, int tid, int wid) {
  const int g = lane >> 4, m = lane & 15, q4 = (lane >> 2) & 3, p4 = lane & 3;
  const int wa = K2_WBASE + (g & 1) * K2_WR + ((wk * 2 + (g >> 1)) * 8 + q4) * 288 + J0 * 32 + p4 * 8;
  auto rdW = [&](bf16x8 (&wf)[NJ], int slot) {
#pragma unroll
    for (int j = 0; j < NJ; ++j) wf[j] = lds_tr2(lds + wa + slot * K2_WS + j * 32, lds + wa + slot * K2_WS + j * 32 + 4 * 288);
  };
  bf16x8 w[6][NJ];
  rdW(w[0], 2); rdW(w[1], 3); rdW(w[2], 4); rdW(w[3], 5);
  RSTAMP(8);
  WAIT_V(0); WAIT_L0();
  RSTAMP(9);
  BAR();
  if (tid == 0) __hip_atomic_fetch_add(cnt, 1u, __ATOMIC_RELAXED, __HIP_MEMORY_SCOPE_AGENT);
  RSTAMP(10);
#pragma unroll
  for (int t = 6; t < 9; ++t) glds_2q(Wo + (size_t)t * 64 * D_ * 2, woff[0], woff[1], woff[2], wdst + (t - 4) * K2_WS);
  rdW(w[4], 0); rdW(w[5], 1);
  RSTAMP(11);
  if (wid == 0) {
    unsigned spins = 0;
    while ((unsigned)__builtin_amdgcn_readfirstlane(__hip_atomic_load(cnt, __ATOMIC_RELAXED, __HIP_MEMORY_SCOPE_AGENT)) < 8u) {
      __builtin_amdgcn_s_sleep(1);
      if (++spins > (1u << 22)) break;
    }
  }
  WAIT_V(0); WAIT_L0(); BAR();
  RSTAMP(12);
  const __amdgpu_buffer_rsrc_t rso = __builtin_amdgcn_make_buffer_rsrc((void*)OB, 0, S_ * D_ * 2, 0x00020000);
  u32x4 xf[9];
#pragma unroll
  for (int t = 0; t < 9; ++t) xf[t] = __builtin_amdgcn_raw_buffer_load_b128(rso, obase + (wk * 4 + g) * 512 + (wm * 16 + m) * 16 + t * 4096, 0, 16);
  bf16x8 w6[NJ], w7[NJ], w8[NJ];
  rdW(w6, 2); rdW(w7, 3); rdW(w8, 4);
  RSTAMP(13);
  f32x4 acc[NJ];
#pragma unroll
  for (int j = 0; j < NJ; ++j) acc[j] = (f32x4){0, 0, 0, 0};
#pragma unroll
  for (int t = 0; t < 6; ++t)
#pragma unroll
    for (int j = 0; j < NJ; ++j) acc[j] = mfma16(w[t][j], __builtin_bit_cast(bf16x8, xf[t]), acc[j]);
#pragma unroll
  for (int j = 0; j < NJ; ++j) acc[j] = mfma16(w6[j], __builtin_bit_cast(bf16x8, xf[6]), acc[j]);
#pragma unroll
  for (int j = 0; j < NJ; ++j) acc[j] = mfma16(w7[j], __builtin_bit_cast(bf16x8, xf[7]), acc[j]);
#pragma unroll
  for (int j = 0; j < NJ; ++j) acc[j] = mfma16(w8[j], __builtin_bit_cast(bf16x8, xf[8]), acc[j]);
  RSTAMP(14);
  LAS float* rt = (LAS float*)(lds + K2_RT);
#pragma unroll
  for (int j = 0; j < NJ; ++j) *(LAS f32x4*)(rt + (wk * 32 + wm * 16 + m) * 148 + (J0 + j) * 16 + g * 4) = acc[j];
}

__global__ __launch_bounds__(512, 2) void k1_qkv_attn(const bf16_t* __restrict__ X, const char* __restrict__ Wp, char* __restrict__ OB,
                                                       const bf16_t* __restrict__ Wo, bf16_t* __restrict__ Y, f32x2* __restrict__ stats,
                                                       unsigned* __restrict__ cnt) {
  __shared__ __attribute__((aligned(16))) char lds_[K1_LDS];
  LAS char* lds = (LAS char*)lds_;
  const unsigned lbase = ldsa(lds);
  const int tid = threadIdx.x, lane = tid & 63, wid = __builtin_amdgcn_readfirstlane(tid >> 6);
  const int bid = blockIdx.x, xcd = bid & 7, idx = bid >> 3;
  const int tt = (xcd >> 1) * 8 + (idx & 7), hg = (xcd & 1) * 4 + (idx >> 3);
  const int t0 = tt * 64 - 16;
  STAMPK(1, 0);

  unsigned xoff[2], woff[2];
#pragma unroll
  for (int i = 0; i < 2; ++i) {
    int ci = i * 64 + lane; if (ci > 79) ci = 79;
    ci += wid * 80;
    const int row = ci >> 3, c = (ci & 7) ^ ((row >> 1) & 7);
    int grow = t0 + row; if (grow < 0) grow = 0;
    xoff[i] = (unsigned)((grow * D_ + c * 8) * 2 + 1024 - i * 1024);
  }
  woff[0] = (unsigned)((wid * 113 + lane) * 16); woff[1] = woff[0];
  const unsigned wdst = lbase + K1_WBASE + wid * 1808;
  const unsigned long long xmask = wid == 0 ? ~0xffull : ~0ull;
  auto stageX = [&](int kt) {
    glds_2m((const char*)X + kt * 128 - 1024, xoff[0], xoff[1], lbase + (kt & 3) * K1_XT + wid * 1280, xmask, 0xffffull);
  };
  auto stageW = [&](int s) {
    glds_2m(Wp + (size_t)(hg * 18 + s) * K1_WS, woff[0], woff[1], wdst + (s & 7) * K1_WS, ~0ull, 0x1ffffffffffffull);
  };
  auto stageT = [&](int t) { stageW(2 * t); stageX(t); stageW(2 * t + 1); };
  stageT(0); stageT(1);
  if (tid >= 64 && tid < 64 + 32) {
    const int c = tid - 64;
    *(LAS u32x4*)(lds + (c >> 3) * K1_XT + (c & 7) * 16) = (u32x4){0x3f803f80u, 0x3f803f80u, 0x3f803f80u, 0x3f803f80u};
  }
  const int th = hg >> 2, ct = hg & 3;
  unsigned w2off[3];
#pragma unroll
  for (int i = 0; i < 3; ++i) {
    int ci = i * 64 + lane; if (ci > 143) ci = 143;
    const int rr = ci / 18, c = ci - rr * 18;
    w2off[i] = (unsigned)(((wid * 8 + rr) * D_ + ct * 144 + c * 8) * 2 - i * 1024);
  }
  const unsigned w2dst = lbase + K2_WBASE + (wid & 1) * K2_WR + (wid >> 1) * 2304;
  auto stageB = [&](int kt) { glds_2q((const char*)Wo + (size_t)kt * 64 * D_ * 2, w2off[0], w2off[1], w2off[2], w2dst + ((kt + 2) % 6) * K2_WS); };

  const int g = lane >> 4, m = lane & 15, q4 = (lane >> 2) & 3, p4 = lane & 3;
  const int xa0 = m * 128 + ((g ^ (m >> 1)) * 16);
  const int wa0 = K1_WBASE + (g & 1) * K1_WR + ((g >> 1) * 8 + q4) * 448 + (wid < 7 ? wid : 0) * 64 + p4 * 8;
  f32x4 acc[5][2];
#pragma unroll
  for (int i = 0; i < 5; ++i) { acc[i][0] = (f32x4){0, 0, 0, 0}; acc[i][1] = (f32x4){0, 0, 0, 0}; }
  LAS f32x2* ms = (LAS f32x2*)(lds + K1_AUX);
  auto top = [&](int t) {
    __builtin_amdgcn_sched_barrier(0);
    if (t < 6) { asm volatile("s_waitcnt vmcnt(12) lgkmcnt(0)" ::: "memory"); } else { asm volatile("s_waitcnt vmcnt(6) lgkmcnt(0)" ::: "memory"); }
    BAR();
    __builtin_amdgcn_sched_barrier(0);
  };
  auto stg = [&](int t) {
    if (t < 5) stageT(t + 4);
    if (t == 6) { stageB(0); stageB(1); }
    if (t == 7) { stageB(2); stageB(3); }
  };
  WAIT_V(6); WAIT_L0(); BAR();
  RSTAMP(0);
  auto rdX = [&](bf16x8 (&xf)[5], int kt, int ks) {
#pragma unroll
    for (int i = 0; i < 5; ++i) xf[i] = lds_b128(lds + ((xa0 ^ (ks * 64)) + (kt & 3) * K1_XT + i * 2048));
  };
  auto mainloop = [&](auto EARLY) {
    bf16x8 xfa[5], xfb[5], wfa[2], wfb[2];
    auto rdW = [&](bf16x8 (&wf)[2], int s) {
#pragma unroll
      for (int j = 0; j < 2; ++j) wf[j] = lds_tr2(lds + (wa0 + (s & 7) * K1_WS + j * 32), lds + (wa0 + (s & 7) * K1_WS + j * 32 + 4 * 448));
    };
    auto step = [&](bf16x8 (&wc)[2], bf16x8 (&xc)[5], bf16x8 (&wn)[2], bf16x8 (&xn)[5], int s) {
#pragma unroll
      for (int i = 0; i < 5; ++i) {
        if (i < 2) wn[i] = lds_tr2(lds + (wa0 + (s & 7) * K1_WS + i * 32), lds + (wa0 + (s & 7) * K1_WS + i * 32 + 4 * 448));
        acc[i][0] = mfma16(wc[0], xc[i], acc[i][0]);
        __builtin_amdgcn_sched_barrier(0);
        xn[i] = lds_b128(lds + ((xa0 ^ ((s & 1) * 64)) + ((s >> 1) & 3) * K1_XT + i * 2048));
        acc[i][1] = mfma16(wc[1], xc[i], acc[i][1]);
        __builtin_amdgcn_sched_barrier(0);
      }
    };
    rdX(xfa, 0, 0); rdW(wfa, 0);
    stageT(2);
    step(wfa, xfa, wfb, xfb, 1);
    stageT(3);
#pragma unroll
    for (int t = 0; t < 8; ++t) {
      top(t);
      if (decltype(EARLY)::value) stg(t);
      step(wfb, xfb, wfa, xfa, 2 * t + 2);
      if (!decltype(EARLY)::value) stg(t);
      step(wfa, xfa, wfb, xfb, 2 * t + 3);
    }
#pragma unroll
    for (int i = 0; i < 5; ++i)
#pragma unroll
      for (int j = 0; j < 2; ++j) acc[i][j] = mfma16(wfb[j], xfb[i], acc[i][j]);
  };
  if (wid < 4) {
    mainloop(IC<1>{});
  } else if (wid < 7) {
    mainloop(IC<0>{});
  } else {
    bf16x8 xfa[5], xfb[5];
    const bf16x8 ones = {0x3f80, 0x3f80, 0x3f80, 0x3f80, 0x3f80, 0x3f80, 0x3f80, 0x3f80};
    auto step = [&](bf16x8 (&xc)[5], bf16x8 (&xn)[5], int s) {
#pragma unroll
      for (int i = 0; i < 5; ++i) {
        acc[i][0] = mfma16(xc[i], xc[i], acc[i][0]);
        xn[i] = lds_b128(lds + ((xa0 ^ ((s & 1) * 64)) + ((s >> 1) & 3) * K1_XT + i * 2048));
        acc[i][1] = mfma16(ones, xc[i], acc[i][1]);
        __builtin_amdgcn_sched_barrier(0);
      }
    };
    rdX(xfa, 0, 0);
    stageT(2);
    step(xfa, xfb, 1);
    stageT(3);
#pragma unroll
    for (int t = 0; t < 8; ++t) {
      top(t);
      step(xfb, xfa, 2 * t + 2);
      stg(t);
      step(xfa, xfb, 2 * t + 3);
    }
#pragma unroll
    for (int i = 0; i < 5; ++i) { acc[i][0] = mfma16(xfb[i], xfb[i], acc[i][0]); acc[i][1] = mfma16(ones, xfb[i], acc[i][1]); }
#pragma unroll
    for (int i = 0; i < 5; ++i) {
      const float q = (m & 2) ? ((m & 1) ? acc[i][0][3] : acc[i][0][2]) : ((m & 1) ? acc[i][0][1] : acc[i][0][0]);
      const float mean = acc[i][1][0] * (1.0f / D_);
      const float var = fmaxf(q * (1.0f / D_) - mean * mean, 0.f);
      if (g == (m >> 2)) ms[i * 16 + m] = (f32x2){mean, __builtin_amdgcn_rsqf(var + LN_EPS)};
    }
  }
  RSTAMP(1);
  WAIT_L0(); BAR();
  RSTAMP(2);
  stageB(4); stageB(5);
  RSTAMP(3);
  if (wid < 7) {
    f32x2 mr[5];
#pragma unroll
    for (int i = 0; i < 5; ++i) mr[i] = ms[i * 16 + m];
    f32x4 cs[2];
#pragma unroll
    for (int j = 0; j < 2; ++j)
#pragma unroll
      for (int r = 0; r < 4; ++r) cs[j][r] = dpp<0x150>(acc[0][j][r]);
    LAS char* qp = lds + m * K1_QS + (wid * 32 + g * 4) * 2;
    const bool ok1 = wid * 32 + 16 + g * 4 < 216;
#pragma unroll
    for (int i = 0; i < 5; ++i) {
      const float nm = -mr[i].x * mr[i].y;
#pragma unroll
      for (int j = 0; j < 2; ++j) {
        const f32x4 v = acc[i][j] * mr[i].y + cs[j] * nm;
        if (j == 0 || ok1) *(LAS u32x2*)(qp + i * 16 * K1_QS + j * 32) = (u32x2){pk2(v[0], v[1]), pk2(v[2], v[3])};
      }
    }
  }
  RSTAMP(4);
  WAIT_L0(); BAR();
  RSTAMP(5);
  {
    const int half = lane & 1, hh = wid & 3, tok = (wid >> 2) * 32 + (lane >> 1);
    const int row = tok + 16;
    const int gpos = t0 + row;
    const LAS unsigned* bp = (const LAS unsigned*)(lds + row * K1_QS + hh * 36 + half * 20);
    unsigned qv[5], kv[8][5], vv[8][5];
#pragma unroll
    for (int d = 0; d < 5; ++d) qv[d] = bp[72 + (d < 4 ? d : (half ? 0 : 4))];
#pragma unroll
    for (int w = 0; w < 8; ++w)
#pragma unroll
      for (int d = 0; d < 5; ++d) kv[w][d] = bp[d - w * (2 * K1_QS / 4)];
#pragma unroll
    for (int w = 0; w < 8; ++w)
#pragma unroll
      for (int d = 0; d < 5; ++d) vv[w][d] = bp[36 + d - w * (2 * K1_QS / 4)];
    __builtin_amdgcn_sched_barrier(0);
    if (half) qv[4] = 0u;
    float lg[8];
#pragma unroll
    for (int w = 0; w < 8; ++w) {
      float s = 0.f;
#pragma unroll
      for (int d = 0; d < 5; ++d) s = dot2bf(qv[d], kv[w][d], s);
      s += dpp<0xB1>(s);
      lg[w] = (gpos - 2 * w >= 0) ? s * (0.23570226039551584f * 1.4426950408889634f) : -1e30f;
    }
    float mx = lg[0];
#pragma unroll
    for (int w = 1; w < 8; ++w) mx = fmaxf(mx, lg[w]);
    float den = 0.f;
#pragma unroll
    for (int w = 0; w < 8; ++w) { lg[w] = __builtin_amdgcn_exp2f(lg[w] - mx); den += lg[w]; }
    const float inv = __builtin_amdgcn_rcpf(den);
    f32x2 ov2[5];
#pragma unroll
    for (int d = 0; d < 5; ++d) ov2[d] = (f32x2){0.f, 0.f};
#pragma unroll
    for (int w = 0; w < 8; ++w) {
      const f32x2 pw = {lg[w], lg[w]};
#pragma unroll
      for (int d = 0; d < 5; ++d) ov2[d] += pw * (f32x2){bflo(vv[w][d]), bfhi(vv[w][d])};
    }
    float ov[10];
#pragma unroll
    for (int d = 0; d < 5; ++d) { ov[2 * d] = ov2[d].x * inv; ov[2 * d + 1] = ov2[d].y * inv; }
    LAS unsigned* op = (LAS unsigned*)(lds + K1_OB + tok * K1_OS + hh * 36 + half * 20);
#pragma unroll
    for (int d = 0; d < 5; ++d) if (d < 4 || !half) op[d] = pk2(ov[2 * d], ov[2 * d + 1]);
  }
  RSTAMP(6);
  asm volatile("s_waitcnt vmcnt(6) lgkmcnt(0)" ::: "memory");
  BAR();
  RSTAMP(7);
  {
    const __amdgpu_buffer_rsrc_t rso = __builtin_amdgcn_make_buffer_rsrc((void*)OB, 0, S_ * D_ * 2, 0x00020000);
#pragma unroll
    for (int i = 0; i < 2; ++i) {
      const int ci = i * 512 + tid;
      if (ci < 576) {
        const int r = ci & 31, q = ci >> 5, hf = q / 9, cc = q - hf * 9;
        const u32x4 v = *(const LAS u32x4*)(lds + K1_OB + (hf * 32 + r) * K1_OS + cc * 16);
        __builtin_amdgcn_raw_buffer_store_b128(v, rso, (((tt * 2 + hf) * 72 + hg * 9 + cc) * 32 + r) * 16, 0, 16);
      }
    }
  }
  const int prow = tid >> 4, sub = tid & 15;
  const size_t gofs = (size_t)(tt * 64 + th * 32 + prow) * D_ + ct * 144;
  u32x4 rv[2];
  rv[0] = *(const u32x4*)(X + gofs + sub * 8);
  rv[1] = *(const u32x4*)(X + gofs + (sub < 2 ? 16 + sub : sub) * 8);
  {
    const int wm = wid & 1, wk = (wid >> 1) & 1;
    const int obase = (tt * 2 + th) * 72 * 32 * 16;
    if (wid < 4) k2_gemm<5, 0>(lds, lane, wm, wk, (const char*)Wo, w2off, w2dst, OB, obase, cnt + tt * 32, tid, wid);
    else k2_gemm<4, 5>(lds, lane, wm, wk, (const char*)Wo, w2off, w2dst, OB, obase, cnt + tt * 32, tid, wid);
  }
  WAIT_L0(); BAR();
  RSTAMP(15);
  {
    const LAS float* rt = (const LAS float*)(lds + K2_RT);
    float xv[2][8];
    float s = 0.f;
#pragma unroll
    for (int c = 0; c < 2; ++c) {
      const bool ok = c == 0 || sub < 2;
      const int ch = ok ? c * 16 + sub : sub;
      const LAS float* p0 = rt + prow * 148 + ch * 8;
      const f32x4 a0 = *(const LAS f32x4*)p0 + *(const LAS f32x4*)(p0 + 32 * 148);
      const f32x4 a1 = *(const LAS f32x4*)(p0 + 4) + *(const LAS f32x4*)(p0 + 32 * 148 + 4);
      const u32x4 y = {pk2(a0[0], a0[1]), pk2(a0[2], a0[3]), pk2(a1[0], a1[1]), pk2(a1[2], a1[3])};
      u32x4 xo;
#pragma unroll
      for (int e = 0; e < 4; ++e) {
        xo[e] = pk2(bflo(rv[c][e]) + bflo(y[e]), bfhi(rv[c][e]) + bfhi(y[e]));
        xv[c][2 * e] = ok ? bflo(xo[e]) : 0.f; xv[c][2 * e + 1] = ok ? bfhi(xo[e]) : 0.f;
      }
      if (ok) *(u32x4*)(Y + gofs + ch * 8) = xo;
#pragma unroll
      for (int e = 0; e < 8; ++e) s += xv[c][e];
    }
    s = sum8(s); s += dpp<0x140>(s);
    const float mean = s * (1.0f / 144.0f);
    float q = 0.f;
#pragma unroll
    for (int c = 0; c < 2; ++c)
#pragma unroll
      for (int e = 0; e < 8; ++e) { const float d = xv[c][e] - mean; q += (c == 0 || sub < 2) ? d * d : 0.f; }
    q = sum8(q); q += dpp<0x140>(q);
    if (sub == 0) stats[(tt * 64 + th * 32 + prow) * 4 + ct] = (f32x2){mean, q};
  }
  RSTAMP(16);
  STAMPEND(1, 1);
  RSTAMP(17);
  RDUMP(18);
}

constexpr int K3_XS = 32768, K3_UOFF = 9216 + 128, K3_WS = K3_UOFF + 9216;
constexpr int K3_WBASE = 2 * K3_XS, K3_AUX = K3_WBASE + 4 * K3_WS;
constexpr int K3_CS = K3_AUX + 8192;
constexpr int K3_DUMMY = K3_CS + 3584;
constexpr int K3_LDS = K3_DUMMY + 4096;

template <int NJ, int J0>
DEVI void k3_body(LAS char* lds, const char* X, const char* wsrc, bf16_t* __restrict__ H, const unsigned (&xoff)[4], const unsigned (&woff)[3],
                  unsigned xdst, unsigned wdst, int lane, int wr, int pm, int pf, const char* Wd, unsigned pfoff) {
  auto stageX = [&](int t) { glds_4(X + t * 128, xoff[0], xoff[1], xoff[2], xoff[3], xdst + (t & 1) * K3_XS); };
  auto stageW = [&](int s) { glds_2q(wsrc + (size_t)s * 32 * F_ * 2, woff[0], woff[1], woff[2], wdst + (s & 3) * K3_WS); };
  const int tk = lane & 31, h = lane >> 5, q4 = (lane >> 2) & 3, p4 = lane & 3, isup = (lane >> 4) & 1;
  const int xa = (wr * 64 + tk) * 128 + ((h ^ ((tk >> 1) & 7)) * 16);
  const int wa = K3_WBASE + isup * K3_UOFF + (h * 8 + q4) * 288 + J0 * 32 + p4 * 8;
  auto rdW = [&](int wo, int kk, int j) -> bf16x8 {
    const LAS char* p = lds + (wa + wo + kk * (16 * 288) + j * 32);
    return lds_tr2(p, p + 4 * 288);
  };
  auto rdX = [&](int xo, int k4, int i) -> bf16x8 {
    return lds_b128(lds + ((xa ^ (k4 * 32)) + xo + i * 4096));
  };
  f32x16 acc[2][NJ];
#pragma unroll
  for (int i = 0; i < 2; ++i)
#pragma unroll
    for (int j = 0; j < NJ; ++j)
#pragma unroll
      for (int r = 0; r < 16; ++r) acc[i][j][r] = 0.f;
  auto correction = [&]() {
    bf16x8 mf[2];
#pragma unroll
    for (int i = 0; i < 2; ++i) {
      const LAS f32x4* sp = (const LAS f32x4*)(lds + K3_AUX + (wr * 64 + i * 32 + tk) * 32);
      const f32x4 s01 = sp[0], s23 = sp[1];
      const float mean = (s01[0] + s01[2] + s23[0] + s23[2]) * 0.25f;
      const float m0 = rbf(-mean), m1 = rbf(-mean - m0), m2_ = (-mean - m0) - m1;
      u32x4 w = {pk2(m0, m0), pk2(m0, m1), pk2(m1, m2_), 0u};
      if (h) w = (u32x4){0u, 0u, 0u, 0u};
      mf[i] = __builtin_bit_cast(bf16x8, w);
    }
#pragma unroll
    for (int j = 0; j < NJ; ++j) {
      const LAS float* cp = (const LAS float*)(lds + K3_CS) + isup * 144 + (J0 + j) * 16 + (lane & 15);
      const float c = (cp[0] + cp[288]) + cp[576];
      const float c0 = rbf(c), c1 = rbf(c - c0), c2 = (c - c0) - c1;
      const u32x4 w = {pk2(c0, c1), pk2(c2, c0), pk2(c1, c0), 0u};
      const bf16x8 cf = __builtin_bit_cast(bf16x8, w);
#pragma unroll
      for (int i = 0; i < 2; ++i) acc[i][j] = __builtin_amdgcn_mfma_f32_32x32x16_bf16(cf, mf[i], acc[i][j], 0, 0, 0);
    }
  };
  WAIT_V(10); BAR();
  correction();
  bf16x8 xfa[2], xfb[2], xfc[2], wf[4];
  WAIT_V(3); BAR();
  STAMP(1);
  xfa[0] = rdX(0, 0, 0); xfa[1] = rdX(0, 0, 1);
  wf[0] = rdW(0, 0, 0); wf[1] = rdW(0, 0, 1);
  stageX(1); stageW(2);

  if (PRIO == 1 && NJ == 5) __builtin_amdgcn_s_setprio(1);
  if (PRIO == 2 && NJ == 4) __builtin_amdgcn_s_setprio(1);
#pragma unroll 1
  for (int t = 0; t < 9; ++t) {
    const int tp = t & 1;
    const int wo = tp * (2 * K3_WS), won = (1 - tp) * (2 * K3_WS);
    const int xo = tp * K3_XS, xon = (1 - tp) * K3_XS;
#pragma unroll
    for (int k4 = 0; k4 < 4; ++k4) {
      if (k4 == 0) {
        if (t < 8) { WAIT_V(7); }
        ABL_B(BAR());
      }
      if (k4 == 2) {
        if (t < 7) { WAIT_V(3); } else if (t == 7) { WAIT_V(0); }
        WAIT_L0();
        ABL_B(BAR());
        if (t == 7) {
          glds_4(Wd, pfoff, pfoff, pfoff, pfoff, ldsa(lds) + K3_DUMMY);
          glds_2(Wd + 3136, pfoff, pfoff, ldsa(lds) + K3_DUMMY);
        }
      }
      __builtin_amdgcn_sched_barrier(0);
#pragma unroll
      for (int j = 0; j < NJ; ++j) {
        const int n = k4 * NJ + j;
        if (j == 0) {
          if (k4 == (NJ == 4 ? 0 : 1) && t < 8) stageW(2 * t + 3);
          if (k4 == (NJ == 4 ? 2 : 3) && t < 7) { stageW(2 * t + 4); stageX(t + 2); }
        }
        {
          const int n2 = n + 2, k42 = n2 / NJ, j2 = n2 % NJ;
          if (k42 < 4) { ABL_L(wf[n2 & 3] = rdW(wo + (k42 >> 1) * K3_WS, k42 & 1, j2);) }
          else { ABL_L(wf[n2 & 3] = rdW(won, 0, j2);) }
        }
        if (j == 1) {
          if (k4 == 0) { ABL_L(xfb[0] = rdX(xo, 1, 0); xfb[1] = rdX(xo, 1, 1);) }
          if (k4 == 1) { ABL_L(xfa[0] = rdX(xo, 2, 0); xfa[1] = rdX(xo, 2, 1);) }
          if (k4 == 3) { ABL_L(xfa[0] = rdX(xon, 0, 0); xfa[1] = rdX(xon, 0, 1);) }
        }
        if (j == 2 && k4 == 1) { ABL_L(xfc[0] = rdX(xo, 3, 0); xfc[1] = rdX(xo, 3, 1);) }
#pragma unroll
        for (int i = 0; i < 2; ++i)
          acc[i][j] = __builtin_amdgcn_mfma_f32_32x32x16_bf16(wf[n & 3], k4 == 1 ? xfb[i] : (k4 == 3 ? xfc[i] : xfa[i]), acc[i][j], 0, 0, 0);
        __builtin_amdgcn_sched_barrier(0);
      }
    }
  }
  STAMP(2);
  float rs[2];
#pragma unroll
  for (int i = 0; i < 2; ++i) {
    const LAS f32x4* sp = (const LAS f32x4*)(lds + K3_AUX + (wr * 64 + i * 32 + tk) * 32);
    const f32x4 s01 = sp[0], s23 = sp[1];
    const float mean = (s01[0] + s01[2] + s23[0] + s23[2]) * 0.25f;
    const float d0 = s01[0] - mean, d1 = s01[2] - mean, d2 = s23[0] - mean, d3 = s23[2] - mean;
    const float m2 = (s01[1] + s01[3]) + (s23[1] + s23[3]) + 144.0f * ((d0 * d0 + d1 * d1) + (d2 * d2 + d3 * d3));
    rs[i] = m2 * (1.0f / D_) + LN_EPS;
  }
  const __amdgpu_buffer_rsrc_t rsh = __builtin_amdgcn_make_buffer_rsrc((void*)H, 0, S_ * F_ * 2, 0x00020000);
  const int hofs = (((pf * 9 + J0) * 2 + h) * S_ + pm * 256 + wr * 64 + tk) * 16;
#pragma unroll
  for (int i = 0; i < 2; ++i) {
    const float c1 = -1.4426950408889634f * __builtin_amdgcn_rsqf(rs[i]);
    const f32x2 c1v = {c1, c1}, inv = {rs[i], rs[i]};
#pragma unroll
    for (int j = 0; j < NJ; ++j) {
      u32x4 hw;
#pragma unroll
      for (int r = 0; r < 4; ++r) {
        const f32x2 ag = {acc[i][j][2 * r], acc[i][j][2 * r + 1]}, au = {acc[i][j][8 + 2 * r], acc[i][j][8 + 2 * r + 1]};
        const f32x2 z = ag * c1v;
        const f32x2 e = {__builtin_amdgcn_exp2f(z.x), __builtin_amdgcn_exp2f(z.y)};
        const f32x2 d = e * inv + inv;
        const f32x2 rc = {__builtin_amdgcn_rcpf(d.x), __builtin_amdgcn_rcpf(d.y)};
        const f32x2 hv = (ag * au) * rc;
        hw[r] = pk2(hv.x, hv.y);
      }
      __builtin_amdgcn_raw_buffer_store_b128(hw, rsh, hofs + j * (2 * S_ * 16) + i * 512, 0, HAUX);
    }
  }
  STAMP(3);
}

__global__ __launch_bounds__(512, 2) void k3_gateup(const bf16_t* __restrict__ X, const bf16_t* __restrict__ Wg, const bf16_t* __restrict__ Wu,
                                                     const float* __restrict__ csum, const f32x2* __restrict__ stats, bf16_t* __restrict__ H,
                                                     const bf16_t* __restrict__ Wd) {
  __shared__ __attribute__((aligned(16))) char lds_[K3_LDS];
  LAS char* lds = (LAS char*)lds_;
  const unsigned lbase = ldsa(lds);
  const int tid = threadIdx.x, lane = tid & 63, wid = __builtin_amdgcn_readfirstlane(tid >> 6);
  const int bid = blockIdx.x, xcd = bid & 7, idx = bid >> 3;
  const int pm = (xcd >> 2) * 4 + (idx & 3), pf = (xcd & 3) * 8 + (idx >> 2);
  const int wr = wid & 3, wc = wid >> 2;
  STAMP(0);
  glds16((const char*)(stats + pm * 256 * 4), (unsigned)(tid * 16), lbase + K3_AUX + wid * 1024);
  {
    const int l36 = lane < 36 ? lane : 35;
    const int cw = wid < 6 ? wid : 0;
    if (lane < 36) glds16((const char*)(csum + cw * F_ + pf * 144), (unsigned)(l36 * 16), lbase + K3_CS + cw * 576);
  }
  unsigned xoff[4], woff[3];
#pragma unroll
  for (int i = 0; i < 4; ++i) {
    const int row = (wid * 4 + i) * 8 + (lane >> 3);
    const int c = (lane & 7) ^ ((row >> 1) & 7);
    xoff[i] = (unsigned)(((pm * 256 + row) * D_ + c * 8) * 2 - i * 1024);
  }
  const unsigned xdst = lbase + wid * 4096;
  glds_4((const char*)X, xoff[0], xoff[1], xoff[2], xoff[3], xdst);
#pragma unroll
  for (int i = 0; i < 3; ++i) {
    int ci = i * 64 + lane; if (ci > 143) ci = 143;
    const int kk = ci / 18, cp = ci - kk * 18;
    woff[i] = (unsigned)((((wid & 3) * 8 + kk) * F_ + pf * 144 + cp * 8) * 2 - i * 1024);
  }
  const char* wsrc = (const char*)((wid >> 2) ? Wu : Wg);
  const unsigned wdst = lbase + K3_WBASE + (wid >> 2) * K3_UOFF + (wid & 3) * 2304;
  glds_2q(wsrc, woff[0], woff[1], woff[2], wdst);
  glds_2q(wsrc + 32 * F_ * 2, woff[0], woff[1], woff[2], wdst + K3_WS);
  const unsigned pfoff = (unsigned)((xcd & 3) * (1152 * D_ * 2) + (idx * 8 + wid) * 5184 + lane * 16);
  if (wc == 0) k3_body<5, 0>(lds, (const char*)X, wsrc, H, xoff, woff, xdst, wdst, lane, wr, pm, pf, (const char*)Wd, pfoff);
  else k3_body<4, 5>(lds, (const char*)X, wsrc, H, xoff, woff, xdst, wdst, lane, wr, pm, pf, (const char*)Wd, pfoff);
  STAMPEND(3, 4);
}

constexpr int K4_XB = 16384, K4_STG = K4_XB + 18432, K4_LDS = 4 * K4_STG;

template <int NJ, int J0>
DEVI void k4_body(LAS char* lds, const char* X, const char* W, unsigned xoff, const unsigned (&woff)[3], unsigned sdst, unsigned wdst,
                  int lane, int wm, int split, int tile, float* __restrict__ P, unsigned* __restrict__ cnt) {
  auto stage = [&](int kt, int buf) {
    glds_2(X + (size_t)kt * (8 * S_ * 16), xoff, xoff, sdst + buf * K4_STG);
    glds_2q(W + (size_t)kt * 64 * D_ * 2, woff[0], woff[1], woff[2], wdst + buf * K4_STG);
  };
  const int g = lane >> 4, m = lane & 15, q4 = (lane >> 2) & 3, p4 = lane & 3;
  const int xa = g * 2048 + (wm * 32 + m) * 16;
  const int wa = K4_XB + ((g >> 1) * 16 + (g & 1) * 4 + q4) * 288 + J0 * 32 + p4 * 8;
  f32x4 acc[2][NJ];
#pragma unroll
  for (int i = 0; i < 2; ++i)
#pragma unroll
    for (int j = 0; j < NJ; ++j) acc[i][j] = (f32x4){0, 0, 0, 0};
  bf16x8 xa_[2], wa_[NJ], xb_[2], wb_[NJ];
  auto rd = [&](bf16x8 (&xf)[2], bf16x8 (&wf)[NJ], const LAS char* sb, int ks) {
#pragma unroll
    for (int i = 0; i < 2; ++i) xf[i] = lds_b128(sb + xa + ks * 8192 + i * 256);
#pragma unroll
    for (int j = 0; j < NJ; ++j) wf[j] = lds_tr2(sb + wa + ks * (32 * 288) + j * 32, sb + wa + ks * (32 * 288) + j * 32 + 8 * 288);
  };
  auto step = [&](bf16x8 (&xc)[2], bf16x8 (&wc)[NJ], bf16x8 (&xn)[2], bf16x8 (&wn)[NJ], const LAS char* sb, int ks) {
#pragma unroll
    for (int j = 0; j < NJ; ++j) {
      if (j < 2) xn[j] = lds_b128(sb + xa + ks * 8192 + j * 256);
      wn[j] = lds_tr2(sb + wa + ks * (32 * 288) + j * 32, sb + wa + ks * (32 * 288) + j * 32 + 8 * 288);
#pragma unroll
      for (int i = 0; i < 2; ++i) acc[i][j] = mfma16(wc[j], xc[i], acc[i][j]);
      __builtin_amdgcn_sched_barrier(0);
    }
  };
  WAIT_V(5); BAR();
  STAMPK(4, 1);
  rd(xa_, wa_, lds, 0);
  stage(2, 2);
  step(xa_, wa_, xb_, wb_, lds, 1);
  stage(3, 3);
  auto iter = [&](auto INFL, auto MORE, int t) {
    asm volatile("s_waitcnt vmcnt(%0) lgkmcnt(0)" ::"n"(decltype(INFL)::value * 5) : "memory");
    BAR();
    __builtin_amdgcn_sched_barrier(0);
    const LAS char* sn = lds + ((t + 1) & 3) * K4_STG;
    if (decltype(MORE)::value && NJ == 4) stage(t + 4, t & 3);
    step(xb_, wb_, xa_, wa_, sn, 0);
    if (decltype(MORE)::value && NJ == 5) stage(t + 4, t & 3);
    step(xa_, wa_, xb_, wb_, sn, 1);
  };
#pragma unroll 1
  for (int t = 0; t < 14; ++t) iter(IC<2>{}, IC<1>{}, t);
  iter(IC<2>{}, IC<0>{}, 14);
  iter(IC<1>{}, IC<0>{}, 15);
  iter(IC<0>{}, IC<0>{}, 16);
#pragma unroll
  for (int j = 0; j < NJ; ++j)
#pragma unroll
    for (int i = 0; i < 2; ++i) acc[i][j] = mfma16(wb_[j], xb_[i], acc[i][j]);
  STAMPK(4, 2);
  const __amdgpu_buffer_rsrc_t rs = __builtin_amdgcn_make_buffer_rsrc((void*)P, 0, 64 * 16 * 9 * 1024, 0x00020000);
  if (wm != split) {
    const int sb = ((tile * 4 + wm) * 4 + split) * 9 * 1024 + lane * 16;
#pragma unroll
    for (int j = 0; j < NJ; ++j) {
      const u32x4 v = {pk2(acc[0][j][0], acc[0][j][1]), pk2(acc[0][j][2], acc[0][j][3]), pk2(acc[1][j][0], acc[1][j][1]), pk2(acc[1][j][2], acc[1][j][3])};
      __builtin_amdgcn_raw_buffer_store_b128(v, rs, sb + (J0 + j) * 1024, 0, 16);
    }
    WAIT_V(0);
    if (lane == 0) __hip_atomic_fetch_add(cnt + (tile * 4 + wm) * 32, 1u, __ATOMIC_RELAXED, __HIP_MEMORY_SCOPE_AGENT);
  } else {
    LAS f32x4* own = (LAS f32x4*)(lds + 2 * K4_STG);
#pragma unroll
    for (int i = 0; i < 2; ++i)
#pragma unroll
      for (int j = 0; j < NJ; ++j) own[(i * 9 + J0 + j) * 64 + lane] = acc[i][j];
    if (NJ == 5) {
      unsigned spins = 0;
      while ((unsigned)__builtin_amdgcn_readfirstlane(__hip_atomic_load(cnt + (tile * 4 + split) * 32, __ATOMIC_RELAXED, __HIP_MEMORY_SCOPE_AGENT)) < 6u) {
        __builtin_amdgcn_s_sleep(1);
        if (++spins > (1u << 22)) break;
      }
    }
  }
}

__global__ __launch_bounds__(512, 2) void k4_down(const bf16_t* __restrict__ X, const bf16_t* __restrict__ W, const bf16_t* __restrict__ R,
                                                   bf16_t* __restrict__ Y, unsigned* __restrict__ cnt, float* __restrict__ P) {
  __shared__ __attribute__((aligned(16))) char lds_[K4_LDS];
  LAS char* lds = (LAS char*)lds_;
  const unsigned lbase = ldsa(lds);
  const int tid = threadIdx.x, lane = tid & 63, wid = __builtin_amdgcn_readfirstlane(tid >> 6);
  const int bid = blockIdx.x, xcd = bid & 7, idx = bid >> 3;
  const int split = xcd & 3;
  const int m0 = (xcd >> 2) * 1024 + (idx >> 2) * 128, n0 = (idx & 3) * 144, k0 = split * 1152;
  const int wm = wid & 3, wn = wid >> 2;
  STAMPK(4, 0);
  const unsigned xoff = (unsigned)((((k0 >> 3) + wid) * S_ + m0 + lane) * 16);
  unsigned woff[3];
#pragma unroll
  for (int i = 0; i < 3; ++i) {
    int ci = i * 64 + lane; if (ci > 143) ci = 143;
    const int rr = ci / 18, c = ci - rr * 18;
    woff[i] = (unsigned)(((k0 + wid * 8 + rr) * D_ + n0 + c * 8) * 2 - i * 1024);
  }
  const unsigned sdst = lbase + wid * 2048, wdst = lbase + K4_XB + wid * 2304;
#pragma unroll
  for (int s = 0; s < 2; ++s) {
    glds_2((const char*)X + (size_t)s * (8 * S_ * 16), xoff, xoff, sdst + s * K4_STG);
    glds_2q((const char*)W + (size_t)s * 64 * D_ * 2, woff[0], woff[1], woff[2], wdst + s * K4_STG);
  }
  const int tile = (m0 >> 7) * 4 + (idx & 3);
  if (wn == 0) k4_body<5, 0>(lds, (const char*)X, (const char*)W, xoff, woff, sdst, wdst, lane, wm, split, tile, P, cnt);
  else k4_body<4, 5>(lds, (const char*)X, (const char*)W, xoff, woff, sdst, wdst, lane, wm, split, tile, P, cnt);
  const int row = (tid >> 3) & 31, sub = tid & 7;
  const size_t gofs = (size_t)(m0 + split * 32 + row) * D_ + n0;
  u32x4 rv[3];
#pragma unroll
  for (int c = 0; c < 3; ++c) { const int ch = c * 8 + sub; rv[c] = *(const u32x4*)(R + gofs + (ch < 18 ? ch : 0) * 8); }
  WAIT_L0(); BAR();
  STAMPK(4, 3);
  const int g = lane >> 4, m = lane & 15;
  LAS f32x4* own = (LAS f32x4*)(lds + 2 * K4_STG);
  LAS float* rt = (LAS float*)lds;
  {
    const __amdgpu_buffer_rsrc_t rs = __builtin_amdgcn_make_buffer_rsrc((void*)P, 0, 64 * 16 * 9 * 1024, 0x00020000);
    u32x4 pv[2][3];
#pragma unroll
    for (int n = 0; n < 2; ++n) {
      const int j = wid + 8 * n < 9 ? wid + 8 * n : wid;
#pragma unroll
      for (int sx = 0; sx < 3; ++sx) {
        const int src = sx + (sx >= split);
        pv[n][sx] = __builtin_amdgcn_raw_buffer_load_b128(rs, (((tile * 4 + split) * 4 + src) * 9 + j) * 1024 + lane * 16, 0, 16);
      }
    }
#pragma unroll
    for (int n = 0; n < 2; ++n) {
      const int j = wid + 8 * n;
      if (j < 9) {
#pragma unroll
        for (int i = 0; i < 2; ++i) {
          f32x4 v = own[(i * 9 + j) * 64 + lane];
#pragma unroll
          for (int sx = 0; sx < 3; ++sx)
            v += (f32x4){bflo(pv[n][sx][2 * i]), bfhi(pv[n][sx][2 * i]), bflo(pv[n][sx][2 * i + 1]), bfhi(pv[n][sx][2 * i + 1])};
          *(LAS f32x4*)(rt + (i * 16 + m) * 148 + j * 16 + g * 4) = v;
        }
      }
    }
  }
  WAIT_L0(); BAR();
  if (tid < 256) {
#pragma unroll
    for (int c = 0; c < 3; ++c) {
      const int ch = c * 8 + sub;
      if (ch < 18) {
        const LAS float* p0 = rt + row * 148 + ch * 8;
        const f32x4 a0 = *(const LAS f32x4*)p0, a1 = *(const LAS f32x4*)(p0 + 4);
        *(u32x4*)(Y + gofs + ch * 8) = (u32x4){pk2(bflo(rv[c].x) + rbf(a0[0]), bfhi(rv[c].x) + rbf(a0[1])), pk2(bflo(rv[c].y) + rbf(a0[2]), bfhi(rv[c].y) + rbf(a0[3])),
                                               pk2(bflo(rv[c].z) + rbf(a1[0]), bfhi(rv[c].z) + rbf(a1[1])), pk2(bflo(rv[c].w) + rbf(a1[2]), bfhi(rv[c].w) + rbf(a1[3]))};
      }
    }
  }
  STAMPEND(4, 4);
}

extern "C" void kernel_launch(void* const* d_in, const int* in_sizes, int n_in,
                              void* d_out, int out_size, void* d_ws, size_t ws_size,
                              hipStream_t stream) {
  const bf16_t* x0 = (const bf16_t*)d_in[0];
  const bf16_t* wqkv = (const bf16_t*)d_in[1];
  const bf16_t* wo = (const bf16_t*)d_in[2];
  const bf16_t* wg = (const bf16_t*)d_in[4];
  const bf16_t* wu = (const bf16_t*)d_in[5];
  const bf16_t* wd = (const bf16_t*)d_in[6];
  bf16_t* out = (bf16_t*)d_out;
  char* ws = (char*)d_ws;
  constexpr size_t XB = (size_t)S_ * D_ * 2, HB = (size_t)S_ * F_ * 2, PB = (size_t)4 * S_ * D_ * 4;
  size_t off = 0;
  float* csum = (float*)(ws + off); off += 131072;
  unsigned* cnt = (unsigned*)(ws + off); off += NCNT * 128;
  char* wp = ws + off; off += (size_t)144 * K1_WS;
  const bf16_t* xin = x0;
  k0_prep<<<144 + 432 + 144 + 41, 256, 0, stream>>>(wqkv, wp, cnt, wg, wu, csum, x0, wo);
  char* o = ws + off; off += XB;
  bf16_t* x1 = (bf16_t*)(ws + off); off += XB;
  f32x2* stats = (f32x2*)(ws + off); off += 65536;
  bf16_t* xm = (bf16_t*)(ws + off); off += XB;
  bf16_t* h = (bf16_t*)(ws + off); off += HB;
  float* P = (float*)(ws + off); off += PB;
  for (int l = 0; l < 2; ++l) {
    bf16_t* x2 = (l == 1) ? out : xm;
    k1_qkv_attn<<<256, 512, 0, stream>>>(xin, wp, o, wo, x1, stats, cnt + (l * 512 + 256) * 32);
    k3_gateup<<<256, 512, 0, stream>>>(x1, wg, wu, csum, stats, h, wd);
    k4_down<<<256, 512, 0, stream>>>(h, wd, x1, x2, cnt + (l * 512) * 32, P);
    xin = x2;
  }
}
```

```cpp
#include <hip/hip_runtime.h>
#include <hip/hip_bf16.h>
#include <stdint.h>

typedef unsigned short bf16_t;
typedef short bf16x8 __attribute__((ext_vector_type(8)));
typedef short bf16x4 __attribute__((ext_vector_type(4)));
typedef float f32x4 __attribute__((ext_vector_type(4)));
typedef float f32x2 __attribute__((ext_vector_type(2)));
typedef float f32x16 __attribute__((ext_vector_type(16)));
typedef unsigned u32x4 __attribute__((ext_vector_type(4)));
typedef unsigned u32x2 __attribute__((ext_vector_type(2)));
#define LAS __attribute__((address_space(3)))
#define DEVI __device__ __forceinline__

constexpr int S_ = 2048, D_ = 576, F_ = 4608, NH_ = 32, DH_ = 18;
constexpr float LN_EPS = 1e-5f;

DEVI float bf2f(unsigned b) { return __uint_as_float(b << 16); }
DEVI float bflo(unsigned w) { return __uint_as_float(w << 16); }
DEVI float bfhi(unsigned w) { return __uint_as_float(w & 0xffff0000u); }
DEVI unsigned pk2(float lo, float hi) {
  typedef __bf16 bf2v __attribute__((ext_vector_type(2)));
  bf2v v; v.x = (__bf16)lo; v.y = (__bf16)hi;
  return __builtin_bit_cast(unsigned, v);
}
template <int CTRL> DEVI float dpp(float x) { return __builtin_bit_cast(float, __builtin_amdgcn_update_dpp(0, __builtin_bit_cast(int, x), CTRL, 0xf, 0xf, true)); }
DEVI float sum8(float x) {
  x += dpp<0xB1>(x); x += dpp<0x4E>(x); x += dpp<0x141>(x); return x;
}
DEVI float rbf(float x) { return (float)((__bf16)x); }

#define RSTAMP(i)
#define RDUMP(n)
#define STAMPW(kid, i, cond)
#define STAMPK(kid, i)
#define STAMPEND(kid, i)
#define STAMP(i) STAMPK(3, i)
#define ABL_G(x) x
#define ABL_B(x) x
#define ABL_L(...) __VA_ARGS__
#define HAUX 16
#define GV 3
#define PRIO 0
template <int N> struct IC { static constexpr int value = N; };
template <int A, int B, class F> __device__ __forceinline__ void static_for(F&& f) {
  if constexpr (A < B) { f(IC<A>{}); static_for<A + 1, B>(f); }
}
#define WAIT_V(n) asm volatile("s_waitcnt vmcnt(" #n ")" ::: "memory")
#define WAIT_L0() asm volatile("s_waitcnt lgkmcnt(0)" ::: "memory")
#define BAR() do { asm volatile("" ::: "memory"); __builtin_amdgcn_s_barrier(); asm volatile("" ::: "memory"); } while (0)

DEVI void glds16(const char* sbase, unsigned voff, unsigned ldsaddr) {
  asm volatile("s_mov_b32 m0, %2\n\ts_nop 0\n\tglobal_load_lds_dwordx4 %0, %1" ::"v"(voff), "s"(sbase), "s"(ldsaddr) : "memory");
}
DEVI void glds_1(const char* sb, unsigned v0, unsigned la) { glds16(sb, v0, la); }
DEVI void glds_2(const char* sb, unsigned v0, unsigned v1, unsigned la) {
  asm volatile("s_mov_b32 m0, %3\n\ts_nop 0\n\tglobal_load_lds_dwordx4 %0, %2\n\tglobal_load_lds_dwordx4 %1, %2 offset:1024"
               ::"v"(v0), "v"(v1), "s"(sb), "s"(la) : "memory");
}
DEVI void glds_4(const char* sb, unsigned v0, unsigned v1, unsigned v2, unsigned v3, unsigned la) {
  asm volatile("s_mov_b32 m0, %5\n\ts_nop 0\n\tglobal_load_lds_dwordx4 %0, %4\n\tglobal_load_lds_dwordx4 %1, %4 offset:1024\n\t"
               "global_load_lds_dwordx4 %2, %4 offset:2048\n\tglobal_load_lds_dwordx4 %3, %4 offset:3072"
               ::"v"(v0), "v"(v1), "v"(v2), "v"(v3), "s"(sb), "s"(la) : "memory");
}
DEVI void glds_2q(const char* sb, unsigned v0, unsigned v1, unsigned v2, unsigned la) {
  asm volatile("s_mov_b32 m0, %4\n\ts_nop 0\n\tglobal_load_lds_dwordx4 %0, %3\n\tglobal_load_lds_dwordx4 %1, %3 offset:1024\n\t"
               "s_mov_b64 exec, 0xffff\n\tglobal_load_lds_dwordx4 %2, %3 offset:2048\n\ts_mov_b64 exec, -1"
               ::"v"(v0), "v"(v1), "v"(v2), "s"(sb), "s"(la) : "memory");
}
DEVI void glds_2m(const char* sb, unsigned v0, unsigned v1, unsigned la, unsigned long long m0_, unsigned long long m1_) {
  asm volatile("s_mov_b32 m0, %3\n\ts_mov_b64 exec, %4\n\tglobal_load_lds_dwordx4 %0, %2\n\t"
               "s_mov_b64 exec, %5\n\tglobal_load_lds_dwordx4 %1, %2 offset:1024\n\ts_mov_b64 exec, -1"
               ::"v"(v0), "v"(v1), "s"(sb), "s"(la), "s"(m0_), "s"(m1_) : "memory");
}
DEVI unsigned ldsa(const LAS char* p) { return (unsigned)(size_t)p; }
DEVI bf16x8 lds_b128(const LAS char* p) { return *(const LAS bf16x8*)p; }
DEVI bf16x8 lds_tr2(const LAS char* p0, const LAS char* p1) {
  bf16x4 a = __builtin_amdgcn_ds_read_tr16_b64_v4i16((LAS bf16x4*)p0);
  bf16x4 b = __builtin_amdgcn_ds_read_tr16_b64_v4i16((LAS bf16x4*)p1);
  return __builtin_shufflevector(a, b, 0, 1, 2, 3, 4, 5, 6, 7);
}
DEVI f32x4 mfma16(bf16x8 a, bf16x8 b, f32x4 c) { return __builtin_amdgcn_mfma_f32_16x16x32_bf16(a, b, c, 0, 0, 0); }

constexpr int K1_WR = 16 * 448 + 32;
constexpr int K1_WS = 14464;
constexpr int NCNT = 1024;

__global__ __launch_bounds__(256) void k0_prep(const bf16_t* __restrict__ Wqkv, char* __restrict__ Wp, unsigned* __restrict__ cnt,
                                                const bf16_t* __restrict__ X, const bf16_t* __restrict__ Wo) {
  STAMPK(0, 0);
  const int b = blockIdx.x;
  if (b < 144) {
    const int u = b, hg = u / 18, sl = u - hg * 18;
    char* dst = Wp + (size_t)u * K1_WS;
    u32x4 v[4];
#pragma unroll
    for (int i = 0; i < 4; ++i) {
      const int ci = i * 256 + threadIdx.x;
      const int reg = ci >= 450, cj = ci - reg * 450;
      const int rr = cj / 28;
      int ch = cj - rr * 28; if (ch > 26) ch = 26;
      const int seg = ch / 9, cc = ch - seg * 9;
      const int gcol = (seg == 2 ? 0 : (seg + 1) * D_) + hg * 72 + cc * 8;
      const int rc = rr > 15 ? 15 : rr;
      const int k = sl * 32 + (rc >> 3) * 16 + reg * 8 + (rc & 7);
      v[i] = *(const u32x4*)(Wqkv + (size_t)k * (3 * D_) + gcol);
    }
    if (u < NCNT / 256) cnt[(u * 256 + threadIdx.x) * 32] = 0u;
#pragma unroll
    for (int i = 0; i < 4; ++i) {
      const int ci = i * 256 + threadIdx.x;
      if (ci < 904) *(u32x4*)(dst + ci * 16) = v[i];
    }
  } else {
    const int u = b - 144;
    const char* src = u < 144 ? (const char*)X + (size_t)u * 16384 : (const char*)Wo + (size_t)(u - 144) * 16384;
    const int lim = u < 144 ? 16384 : (D_ * D_ * 2 - (u - 144) * 16384 < 16384 ? D_ * D_ * 2 - (u - 144) * 16384 : 16384);
#pragma unroll
    for (int i = 0; i < 4; ++i) {
      const int o = (i * 256 + threadIdx.x) * 16;
      if (o < lim) { const u32x4 v = *(const u32x4*)(src + o); asm volatile("" ::"v"(v)); }
    }
  }
  STAMPEND(0, 1);
}

constexpr int K1_XT = 80 * 128;
constexpr int K1_WBASE = 4 * K1_XT;
constexpr int K1_AUX = 156928;
constexpr int K1_LDS = K1_AUX + 768;
constexpr int K1_QS = 440;
constexpr int K1_OB = 35200, K1_OS = 160;
constexpr int K2_WR = 9216 + 128, K2_WS = K2_WR + 9216, K2_WBASE = 45568;

DEVI float dot2bf(unsigned a, unsigned b, float c) {
  typedef __bf16 bf2v __attribute__((ext_vector_type(2)));
  return __builtin_amdgcn_fdot2_f32_bf16(__builtin_bit_cast(bf2v, a), __builtin_bit_cast(bf2v, b), c, false);
}

constexpr int K2_RT = 44032;

template <int NJ, int J0>
DEVI void k2_gemm(LAS char* lds, int lane, int wm, int wk, const char* Wo, const unsigned (&woff)[3], unsigned wdst,
                  const char* OB, int obase, unsigned* __restrict__ cnt, int tid, int wid) {
  const int g = lane >> 4, m = lane & 15, q4 = (lane >> 2) & 3, p4 = lane & 3;
  const int wa = K2_WBASE + (g & 1) * K2_WR + ((wk * 2 + (g >> 1)) * 8 + q4) * 288 + J0 * 32 + p4 * 8;
  auto rdW = [&](bf16x8 (&wf)[NJ], int slot) {
#pragma unroll
    for (int j = 0; j < NJ; ++j) wf[j] = lds_tr2(lds + wa + slot * K2_WS + j * 32, lds + wa + slot * K2_WS + j * 32 + 4 * 288);
  };
  bf16x8 w[6][NJ];
  rdW(w[0], 2); rdW(w[1], 3); rdW(w[2], 4); rdW(w[3], 5);
  RSTAMP(8);
  WAIT_V(0); WAIT_L0();
  RSTAMP(9);
  BAR();
  if (tid == 0) __hip_atomic_fetch_add(cnt, 1u, __ATOMIC_RELAXED, __HIP_MEMORY_SCOPE_AGENT);
  RSTAMP(10);
#pragma unroll
  for (int t = 6; t < 9; ++t) glds_2q(Wo + (size_t)t * 64 * D_ * 2, woff[0], woff[1], woff[2], wdst + (t - 4) * K2_WS);
  rdW(w[4], 0); rdW(w[5], 1);
  RSTAMP(11);
  if (wid == 0) {
    unsigned spins = 0;
    while ((unsigned)__builtin_amdgcn_readfirstlane(__hip_atomic_load(cnt, __ATOMIC_RELAXED, __HIP_MEMORY_SCOPE_AGENT)) < 8u) {
      __builtin_amdgcn_s_sleep(1);
      if (++spins > (1u << 22)) break;
    }
  }
  WAIT_V(0); WAIT_L0(); BAR();
  RSTAMP(12);
  const __amdgpu_buffer_rsrc_t rso = __builtin_amdgcn_make_buffer_rsrc((void*)OB, 0, S_ * D_ * 2, 0x00020000);
  u32x4 xf[9];
#pragma unroll
  for (int t = 0; t < 9; ++t) xf[t] = __builtin_amdgcn_raw_buffer_load_b128(rso, obase + (wk * 4 + g) * 512 + (wm * 16 + m) * 16 + t * 4096, 0, 16);
  bf16x8 w6[NJ], w7[NJ], w8[NJ];
  rdW(w6, 2); rdW(w7, 3); rdW(w8, 4);
  RSTAMP(13);
  f32x4 acc[NJ];
#pragma unroll
  for (int j = 0; j < NJ; ++j) acc[j] = (f32x4){0, 0, 0, 0};
#pragma unroll
  for (int t = 0; t < 6; ++t)
#pragma unroll
    for (int j = 0; j < NJ; ++j) acc[j] = mfma16(w[t][j], __builtin_bit_cast(bf16x8, xf[t]), acc[j]);
#pragma unroll
  for (int j = 0; j < NJ; ++j) acc[j] = mfma16(w6[j], __builtin_bit_cast(bf16x8, xf[6]), acc[j]);
#pragma unroll
  for (int j = 0; j < NJ; ++j) acc[j] = mfma16(w7[j], __builtin_bit_cast(bf16x8, xf[7]), acc[j]);
#pragma unroll
  for (int j = 0; j < NJ; ++j) acc[j] = mfma16(w8[j], __builtin_bit_cast(bf16x8, xf[8]), acc[j]);
  RSTAMP(14);
  LAS float* rt = (LAS float*)(lds + K2_RT);
#pragma unroll
  for (int j = 0; j < NJ; ++j) *(LAS f32x4*)(rt + (wk * 32 + wm * 16 + m) * 148 + (J0 + j) * 16 + g * 4) = acc[j];
}

__global__ __launch_bounds__(576) void k1_qkv_attn(const bf16_t* __restrict__ X, const char* __restrict__ Wp, char* __restrict__ OB,
                                                       const bf16_t* __restrict__ Wo, bf16_t* __restrict__ Y, f32x2* __restrict__ stats,
                                                       unsigned* __restrict__ cnt, const bf16_t* __restrict__ Wg, const bf16_t* __restrict__ Wu,
                                                       float* __restrict__ csum) {
  __shared__ __attribute__((aligned(16))) char lds_[K1_LDS];
  LAS char* lds = (LAS char*)lds_;
  const unsigned lbase = ldsa(lds);
  const int tid = threadIdx.x, lane = tid & 63, wid = __builtin_amdgcn_readfirstlane(tid >> 6);
  const int bid = blockIdx.x, xcd = bid & 7, idx = bid >> 3;
  if (wid == 8) {
    if (bid >= 216) return;
    const int kq = bid / 72, cb = bid - kq * 72;
    const bf16_t* Wc = (cb < 36 ? Wg : Wu) + (size_t)(kq * 192 + (lane >> 4)) * F_ + (cb % 36) * 128 + (lane & 15) * 8;
    float c8[8] = {0, 0, 0, 0, 0, 0, 0, 0};
    u32x4 cv[4][6];
#pragma unroll
    for (int st = 0; st < 11; ++st) {
      if (st < 8) {
#pragma unroll
        for (int i = 0; i < 6; ++i) cv[st & 3][i] = *(const u32x4*)(Wc + (size_t)((st * 6 + i) * 4) * F_);
      }
      if (st >= 3) {
#pragma unroll
        for (int i = 0; i < 6; ++i)
#pragma unroll
          for (int e = 0; e < 4; ++e) { c8[2 * e] += bflo(cv[(st - 3) & 3][i][e]); c8[2 * e + 1] += bfhi(cv[(st - 3) & 3][i][e]); }
#pragma unroll
        for (int e = 0; e < 8; ++e) asm volatile("" : "+v"(c8[e]));
      }
      __builtin_amdgcn_sched_barrier(0);
      if (st < 10) BAR();
      __builtin_amdgcn_sched_barrier(0);
    }
#pragma unroll
    for (int e = 0; e < 8; ++e) { float t = c8[e]; t += __shfl_xor(t, 16); t += __shfl_xor(t, 32); c8[e] = t; }
    if (lane < 16) {
      float* cp = csum + kq * 2 * F_ + (cb < 36 ? 0 : F_) + (cb % 36) * 128 + lane * 8;
      *(f32x4*)cp = (f32x4){c8[0], c8[1], c8[2], c8[3]}; *(f32x4*)(cp + 4) = (f32x4){c8[4], c8[5], c8[6], c8[7]};
    }
    return;
  }
  const int tt = (xcd >> 1) * 8 + (idx & 7), hg = (xcd & 1) * 4 + (idx >> 3);
  const int t0 = tt * 64 - 16;
  STAMPK(1, 0);

  unsigned xoff[2], woff[2];
#pragma unroll
  for (int i = 0; i < 2; ++i) {
    int ci = i * 64 + lane; if (ci > 79) ci = 79;
    ci += wid * 80;
    const int row = ci >> 3, c = (ci & 7) ^ ((row >> 1) & 7);
    int grow = t0 + row; if (grow < 0) grow = 0;
    xoff[i] = (unsigned)((grow * D_ + c * 8) * 2 + 1024 - i * 1024);
  }
  woff[0] = (unsigned)((wid * 113 + lane) * 16); woff[1] = woff[0];
  const unsigned wdst = lbase + K1_WBASE + wid * 1808;
  const unsigned long long xmask = wid == 0 ? ~0xffull : ~0ull;
  auto stageX = [&](int kt) {
    glds_2m((const char*)X + kt * 128 - 1024, xoff[0], xoff[1], lbase + (kt & 3) * K1_XT + wid * 1280, xmask, 0xffffull);
  };
  auto stageW = [&](int s) {
    glds_2m(Wp + (size_t)(hg * 18 + s) * K1_WS, woff[0], woff[1], wdst + (s & 7) * K1_WS, ~0ull, 0x1ffffffffffffull);
  };
  auto stageT = [&](int t) { stageW(2 * t); stageX(t); stageW(2 * t + 1); };
  stageT(0); stageT(1);
  if (tid >= 64 && tid < 64 + 32) {
    const int c = tid - 64;
    *(LAS u32x4*)(lds + (c >> 3) * K1_XT + (c & 7) * 16) = (u32x4){0x3f803f80u, 0x3f803f80u, 0x3f803f80u, 0x3f803f80u};
  }
  const int th = hg >> 2, ct = hg & 3;
  unsigned w2off[3];
#pragma unroll
  for (int i = 0; i < 3; ++i) {
    int ci = i * 64 + lane; if (ci > 143) ci = 143;
    const int rr = ci / 18, c = ci - rr * 18;
    w2off[i] = (unsigned)(((wid * 8 + rr) * D_ + ct * 144 + c * 8) * 2 - i * 1024);
  }
  const unsigned w2dst = lbase + K2_WBASE + (wid & 1) * K2_WR + (wid >> 1) * 2304;
  auto stageB = [&](int kt) { glds_2q((const char*)Wo + (size_t)kt * 64 * D_ * 2, w2off[0], w2off[1], w2off[2], w2dst + ((kt + 2) % 6) * K2_WS); };

  const int g = lane >> 4, m = lane & 15, q4 = (lane >> 2) & 3, p4 = lane & 3;
  const int xa0 = m * 128 + ((g ^ (m >> 1)) * 16);
  const int wa0 = K1_WBASE + (g & 1) * K1_WR + ((g >> 1) * 8 + q4) * 448 + (wid < 7 ? wid : 0) * 64 + p4 * 8;
  f32x4 acc[5][2];
#pragma unroll
  for (int i = 0; i < 5; ++i) { acc[i][0] = (f32x4){0, 0, 0, 0}; acc[i][1] = (f32x4){0, 0, 0, 0}; }
  LAS f32x2* ms = (LAS f32x2*)(lds + K1_AUX);
  auto top = [&](int t) {
    __builtin_amdgcn_sched_barrier(0);
    if (t < 6) { asm volatile("s_waitcnt vmcnt(12) lgkmcnt(0)" ::: "memory"); } else { asm volatile("s_waitcnt vmcnt(6) lgkmcnt(0)" ::: "memory"); }
    BAR();
    __builtin_amdgcn_sched_barrier(0);
  };
  auto stg = [&](int t) {
    if (t < 5) stageT(t + 4);
    if (t == 6) { stageB(0); stageB(1); }
    if (t == 7) { stageB(2); stageB(3); }
  };
  WAIT_V(6); WAIT_L0(); BAR();
  RSTAMP(0);
  auto rdX = [&](bf16x8 (&xf)[5], int kt, int ks) {
#pragma unroll
    for (int i = 0; i < 5; ++i) xf[i] = lds_b128(lds + ((xa0 ^ (ks * 64)) + (kt & 3) * K1_XT + i * 2048));
  };
  auto mainloop = [&](auto EARLY) {
    bf16x8 xfa[5], xfb[5], wfa[2], wfb[2];
    auto rdW = [&](bf16x8 (&wf)[2], int s) {
#pragma unroll
      for (int j = 0; j < 2; ++j) wf[j] = lds_tr2(lds + (wa0 + (s & 7) * K1_WS + j * 32), lds + (wa0 + (s & 7) * K1_WS + j * 32 + 4 * 448));
    };
    auto step = [&](bf16x8 (&wc)[2], bf16x8 (&xc)[5], bf16x8 (&wn)[2], bf16x8 (&xn)[5], int s) {
#pragma unroll
      for (int i = 0; i < 5; ++i) {
        if (i < 2) wn[i] = lds_tr2(lds + (wa0 + (s & 7) * K1_WS + i * 32), lds + (wa0 + (s & 7) * K1_WS + i * 32 + 4 * 448));
        acc[i][0] = mfma16(wc[0], xc[i], acc[i][0]);
        __builtin_amdgcn_sched_barrier(0);
        xn[i] = lds_b128(lds + ((xa0 ^ ((s & 1) * 64)) + ((s >> 1) & 3) * K1_XT + i * 2048));
        acc[i][1] = mfma16(wc[1], xc[i], acc[i][1]);
        __builtin_amdgcn_sched_barrier(0);
      }
    };
    rdX(xfa, 0, 0); rdW(wfa, 0);
    stageT(2);
    step(wfa, xfa, wfb, xfb, 1);
    stageT(3);
#pragma unroll
    for (int t = 0; t < 8; ++t) {
      top(t);
      if (decltype(EARLY)::value) stg(t);
      step(wfb, xfb, wfa, xfa, 2 * t + 2);
      if (!decltype(EARLY)::value) stg(t);
      step(wfa, xfa, wfb, xfb, 2 * t + 3);
    }
#pragma unroll
    for (int i = 0; i < 5; ++i)
#pragma unroll
      for (int j = 0; j < 2; ++j) acc[i][j] = mfma16(wfb[j], xfb[i], acc[i][j]);
  };
  if (wid < 4) {
    mainloop(IC<1>{});
  } else if (wid < 7) {
    mainloop(IC<0>{});
  } else {
    bf16x8 xfa[5], xfb[5];
    const bf16x8 ones = {0x3f80, 0x3f80, 0x3f80, 0x3f80, 0x3f80, 0x3f80, 0x3f80, 0x3f80};
    auto step = [&](bf16x8 (&xc)[5], bf16x8 (&xn)[5], int s) {
#pragma unroll
      for (int i = 0; i < 5; ++i) {
        acc[i][0] = mfma16(xc[i], xc[i], acc[i][0]);
        xn[i] = lds_b128(lds + ((xa0 ^ ((s & 1) * 64)) + ((s >> 1) & 3) * K1_XT + i * 2048));
        acc[i][1] = mfma16(ones, xc[i], acc[i][1]);
        __builtin_amdgcn_sched_barrier(0);
      }
    };
    rdX(xfa, 0, 0);
    stageT(2);
    step(xfa, xfb, 1);
    stageT(3);
#pragma unroll
    for (int t = 0; t < 8; ++t) {
      top(t);
      step(xfb, xfa, 2 * t + 2);
      stg(t);
      step(xfa, xfb, 2 * t + 3);
    }
#pragma unroll
    for (int i = 0; i < 5; ++i) { acc[i][0] = mfma16(xfb[i], xfb[i], acc[i][0]); acc[i][1] = mfma16(ones, xfb[i], acc[i][1]); }
#pragma unroll
    for (int i = 0; i < 5; ++i) {
      const float q = (m & 2) ? ((m & 1) ? acc[i][0][3] : acc[i][0][2]) : ((m & 1) ? acc[i][0][1] : acc[i][0][0]);
      const float mean = acc[i][1][0] * (1.0f / D_);
      const float var = fmaxf(q * (1.0f / D_) - mean * mean, 0.f);
      if (g == (m >> 2)) ms[i * 16 + m] = (f32x2){mean, __builtin_amdgcn_rsqf(var + LN_EPS)};
    }
  }
  RSTAMP(1);
  WAIT_L0(); BAR();
  RSTAMP(2);
  stageB(4); stageB(5);
  RSTAMP(3);
  if (wid < 7) {
    f32x2 mr[5];
#pragma unroll
    for (int i = 0; i < 5; ++i) mr[i] = ms[i * 16 + m];
    f32x4 cs[2];
#pragma unroll
    for (int j = 0; j < 2; ++j)
#pragma unroll
      for (int r = 0; r < 4; ++r) cs[j][r] = dpp<0x150>(acc[0][j][r]);
    LAS char* qp = lds + m * K1_QS + (wid * 32 + g * 4) * 2;
    const bool ok1 = wid * 32 + 16 + g * 4 < 216;
#pragma unroll
    for (int i = 0; i < 5; ++i) {
      const float nm = -mr[i].x * mr[i].y;
#pragma unroll
      for (int j = 0; j < 2; ++j) {
        const f32x4 v = acc[i][j] * mr[i].y + cs[j] * nm;
        if (j == 0 || ok1) *(LAS u32x2*)(qp + i * 16 * K1_QS + j * 32) = (u32x2){pk2(v[0], v[1]), pk2(v[2], v[3])};
      }
    }
  }
  RSTAMP(4);
  WAIT_L0(); BAR();
  RSTAMP(5);
  {
    const int half = lane & 1, hh = wid & 3, tok = (wid >> 2) * 32 + (lane >> 1);
    const int row = tok + 16;
    const int gpos = t0 + row;
    const LAS unsigned* bp = (const LAS unsigned*)(lds + row * K1_QS + hh * 36 + half * 20);
    unsigned qv[5], kv[8][5], vv[8][5];
#pragma unroll
    for (int d = 0; d < 5; ++d) qv[d] = bp[72 + (d < 4 ? d : (half ? 0 : 4))];
#pragma unroll
    for (int w = 0; w < 8; ++w)
#pragma unroll
      for (int d = 0; d < 5; ++d) kv[w][d] = bp[d - w * (2 * K1_QS / 4)];
#pragma unroll
    for (int w = 0; w < 8; ++w)
#pragma unroll
      for (int d = 0; d < 5; ++d) vv[w][d] = bp[36 + d - w * (2 * K1_QS / 4)];
    __builtin_amdgcn_sched_barrier(0);
    if (half) qv[4] = 0u;
    float lg[8];
#pragma unroll
    for (int w = 0; w < 8; ++w) {
      float s = 0.f;
#pragma unroll
      for (int d = 0; d < 5; ++d) s = dot2bf(qv[d], kv[w][d], s);
      s += dpp<0xB1>(s);
      lg[w] = (gpos - 2 * w >= 0) ? s * (0.23570226039551584f * 1.4426950408889634f) : -1e30f;
    }
    float mx = lg[0];
#pragma unroll
    for (int w = 1; w < 8; ++w) mx = fmaxf(mx, lg[w]);
    float den = 0.f;
#pragma unroll
    for (int w = 0; w < 8; ++w) { lg[w] = __builtin_amdgcn_exp2f(lg[w] - mx); den += lg[w]; }
    const float inv = __builtin_amdgcn_rcpf(den);
    f32x2 ov2[5];
#pragma unroll
    for (int d = 0; d < 5; ++d) ov2[d] = (f32x2){0.f, 0.f};
#pragma unroll
    for (int w = 0; w < 8; ++w) {
      const f32x2 pw = {lg[w], lg[w]};
#pragma unroll
      for (int d = 0; d < 5; ++d) ov2[d] += pw * (f32x2){bflo(vv[w][d]), bfhi(vv[w][d])};
    }
    float ov[10];
#pragma unroll
    for (int d = 0; d < 5; ++d) { ov[2 * d] = ov2[d].x * inv; ov[2 * d + 1] = ov2[d].y * inv; }
    LAS unsigned* op = (LAS unsigned*)(lds + K1_OB + tok * K1_OS + hh * 36 + half * 20);
#pragma unroll
    for (int d = 0; d < 5; ++d) if (d < 4 || !half) op[d] = pk2(ov[2 * d], ov[2 * d + 1]);
  }
  RSTAMP(6);
  asm volatile("s_waitcnt vmcnt(6) lgkmcnt(0)" ::: "memory");
  BAR();
  RSTAMP(7);
  {
    const __amdgpu_buffer_rsrc_t rso = __builtin_amdgcn_make_buffer_rsrc((void*)OB, 0, S_ * D_ * 2, 0x00020000);
#pragma unroll
    for (int i = 0; i < 2; ++i) {
      const int ci = i * 512 + tid;
      if (ci < 576) {
        const int r = ci & 31, q = ci >> 5, hf = q / 9, cc = q - hf * 9;
        const u32x4 v = *(const LAS u32x4*)(lds + K1_OB + (hf * 32 + r) * K1_OS + cc * 16);
        __builtin_amdgcn_raw_buffer_store_b128(v, rso, (((tt * 2 + hf) * 72 + hg * 9 + cc) * 32 + r) * 16, 0, 16);
      }
    }
  }
  const int prow = tid >> 4, sub = tid & 15;
  const size_t gofs = (size_t)(tt * 64 + th * 32 + prow) * D_ + ct * 144;
  u32x4 rv[2];
  rv[0] = *(const u32x4*)(X + gofs + sub * 8);
  rv[1] = *(const u32x4*)(X + gofs + (sub < 2 ? 16 + sub : sub) * 8);
  {
    const int wm = wid & 1, wk = (wid >> 1) & 1;
    const int obase = (tt * 2 + th) * 72 * 32 * 16;
    if (wid < 4) k2_gemm<5, 0>(lds, lane, wm, wk, (const char*)Wo, w2off, w2dst, OB, obase, cnt + tt * 32, tid, wid);
    else k2_gemm<4, 5>(lds, lane, wm, wk, (const char*)Wo, w2off, w2dst, OB, obase, cnt + tt * 32, tid, wid);
  }
  WAIT_L0(); BAR();
  RSTAMP(15);
  {
    const LAS float* rt = (const LAS float*)(lds + K2_RT);
    float xv[2][8];
    float s = 0.f;
#pragma unroll
    for (int c = 0; c < 2; ++c) {
      const bool ok = c == 0 || sub < 2;
      const int ch = ok ? c * 16 + sub : sub;
      const LAS float* p0 = rt + prow * 148 + ch * 8;
      const f32x4 a0 = *(const LAS f32x4*)p0 + *(const LAS f32x4*)(p0 + 32 * 148);
      const f32x4 a1 = *(const LAS f32x4*)(p0 + 4) + *(const LAS f32x4*)(p0 + 32 * 148 + 4);
      const u32x4 y = {pk2(a0[0], a0[1]), pk2(a0[2], a0[3]), pk2(a1[0], a1[1]), pk2(a1[2], a1[3])};
      u32x4 xo;
#pragma unroll
      for (int e = 0; e < 4; ++e) {
        xo[e] = pk2(bflo(rv[c][e]) + bflo(y[e]), bfhi(rv[c][e]) + bfhi(y[e]));
        xv[c][2 * e] = ok ? bflo(xo[e]) : 0.f; xv[c][2 * e + 1] = ok ? bfhi(xo[e]) : 0.f;
      }
      if (ok) *(u32x4*)(Y + gofs + ch * 8) = xo;
#pragma unroll
      for (int e = 0; e < 8; ++e) s += xv[c][e];
    }
    s = sum8(s); s += dpp<0x140>(s);
    const float mean = s * (1.0f / 144.0f);
    float q = 0.f;
#pragma unroll
    for (int c = 0; c < 2; ++c)
#pragma unroll
      for (int e = 0; e < 8; ++e) { const float d = xv[c][e] - mean; q += (c == 0 || sub < 2) ? d * d : 0.f; }
    q = sum8(q); q += dpp<0x140>(q);
    if (sub == 0) stats[(tt * 64 + th * 32 + prow) * 4 + ct] = (f32x2){mean, q};
  }
  RSTAMP(16);
  STAMPEND(1, 1);
  RSTAMP(17);
  RDUMP(18);
}

constexpr int K3_XS = 32768, K3_UOFF = 9216 + 128, K3_WS = K3_UOFF + 9216;
constexpr int K3_WBASE = 2 * K3_XS, K3_AUX = K3_WBASE + 4 * K3_WS;
constexpr int K3_CS = K3_AUX + 8192;
constexpr int K3_DUMMY = K3_CS + 3584;
constexpr int K3_LDS = K3_DUMMY + 4096;

template <int NJ, int J0>
DEVI void k3_body(LAS char* lds, const char* X, const char* wsrc, bf16_t* __restrict__ H, const unsigned (&xoff)[4], const unsigned (&woff)[3],
                  unsigned xdst, unsigned wdst, int lane, int wr, int pm, int pf, const char* Wd, unsigned pfoff) {
  auto stageX = [&](int t) { glds_4(X + t * 128, xoff[0], xoff[1], xoff[2], xoff[3], xdst + (t & 1) * K3_XS); };
  auto stageW = [&](int s) { glds_2q(wsrc + (size_t)s * 32 * F_ * 2, woff[0], woff[1], woff[2], wdst + (s & 3) * K3_WS); };
  const int tk = lane & 31, h = lane >> 5, q4 = (lane >> 2) & 3, p4 = lane & 3, isup = (lane >> 4) & 1;
  const int xa = (wr * 64 + tk) * 128 + ((h ^ ((tk >> 1) & 7)) * 16);
  const int wa = K3_WBASE + isup * K3_UOFF + (h * 8 + q4) * 288 + J0 * 32 + p4 * 8;
  auto rdW = [&](int wo, int kk, int j) -> bf16x8 {
    const LAS char* p = lds + (wa + wo + kk * (16 * 288) + j * 32);
    return lds_tr2(p, p + 4 * 288);
  };
  auto rdX = [&](int xo, int k4, int i) -> bf16x8 {
    return lds_b128(lds + ((xa ^ (k4 * 32)) + xo + i * 4096));
  };
  f32x16 acc[2][NJ];
#pragma unroll
  for (int i = 0; i < 2; ++i)
#pragma unroll
    for (int j = 0; j < NJ; ++j)
#pragma unroll
      for (int r = 0; r < 16; ++r) acc[i][j][r] = 0.f;
  auto correction = [&]() {
    bf16x8 mf[2];
#pragma unroll
    for (int i = 0; i < 2; ++i) {
      const LAS f32x4* sp = (const LAS f32x4*)(lds + K3_AUX + (wr * 64 + i * 32 + tk) * 32);
      const f32x4 s01 = sp[0], s23 = sp[1];
      const float mean = (s01[0] + s01[2] + s23[0] + s23[2]) * 0.25f;
      const float m0 = rbf(-mean), m1 = rbf(-mean - m0), m2_ = (-mean - m0) - m1;
      u32x4 w = {pk2(m0, m0), pk2(m0, m1), pk2(m1, m2_), 0u};
      if (h) w = (u32x4){0u, 0u, 0u, 0u};
      mf[i] = __builtin_bit_cast(bf16x8, w);
    }
#pragma unroll
    for (int j = 0; j < NJ; ++j) {
      const LAS float* cp = (const LAS float*)(lds + K3_CS) + isup * 144 + (J0 + j) * 16 + (lane & 15);
      const float c = (cp[0] + cp[288]) + cp[576];
      const float c0 = rbf(c), c1 = rbf(c - c0), c2 = (c - c0) - c1;
      const u32x4 w = {pk2(c0, c1), pk2(c2, c0), pk2(c1, c0), 0u};
      const bf16x8 cf = __builtin_bit_cast(bf16x8, w);
#pragma unroll
      for (int i = 0; i < 2; ++i) acc[i][j] = __builtin_amdgcn_mfma_f32_32x32x16_bf16(cf, mf[i], acc[i][j], 0, 0, 0);
    }
  };
  WAIT_V(10); BAR();
  correction();
  bf16x8 xfa[2], xfb[2], xfc[2], wf[4];
  WAIT_V(3); BAR();
  STAMP(1);
  xfa[0] = rdX(0, 0, 0); xfa[1] = rdX(0, 0, 1);
  wf[0] = rdW(0, 0, 0); wf[1] = rdW(0, 0, 1);
  stageX(1); stageW(2);

  if (PRIO == 1 && NJ == 5) __builtin_amdgcn_s_setprio(1);
  if (PRIO == 2 && NJ == 4) __builtin_amdgcn_s_setprio(1);
#pragma unroll 1
  for (int t = 0; t < 9; ++t) {
    const int tp = t & 1;
    const int wo = tp * (2 * K3_WS), won = (1 - tp) * (2 * K3_WS);
    const int xo = tp * K3_XS, xon = (1 - tp) * K3_XS;
#pragma unroll
    for (int k4 = 0; k4 < 4; ++k4) {
      if (k4 == 0) {
        if (t < 8) { WAIT_V(7); }
        ABL_B(BAR());
      }
      if (k4 == 2) {
        if (t < 7) { WAIT_V(3); } else if (t == 7) { WAIT_V(0); }
        WAIT_L0();
        ABL_B(BAR());
        if (t == 7) {
          glds_4(Wd, pfoff, pfoff, pfoff, pfoff, ldsa(lds) + K3_DUMMY);
          glds_2(Wd + 3136, pfoff, pfoff, ldsa(lds) + K3_DUMMY);
        }
      }
      __builtin_amdgcn_sched_barrier(0);
#pragma unroll
      for (int j = 0; j < NJ; ++j) {
        const int n = k4 * NJ + j;
        if (j == 0) {
          if (k4 == (NJ == 4 ? 0 : 1) && t < 8) stageW(2 * t + 3);
          if (k4 == (NJ == 4 ? 2 : 3) && t < 7) { stageW(2 * t + 4); stageX(t + 2); }
        }
        {
          const int n2 = n + 2, k42 = n2 / NJ, j2 = n2 % NJ;
          if (k42 < 4) { ABL_L(wf[n2 & 3] = rdW(wo + (k42 >> 1) * K3_WS, k42 & 1, j2);) }
          else { ABL_L(wf[n2 & 3] = rdW(won, 0, j2);) }
        }
        if (j == 1) {
          if (k4 == 0) { ABL_L(xfb[0] = rdX(xo, 1, 0); xfb[1] = rdX(xo, 1, 1);) }
          if (k4 == 1) { ABL_L(xfa[0] = rdX(xo, 2, 0); xfa[1] = rdX(xo, 2, 1);) }
          if (k4 == 3) { ABL_L(xfa[0] = rdX(xon, 0, 0); xfa[1] = rdX(xon, 0, 1);) }
        }
        if (j == 2 && k4 == 1) { ABL_L(xfc[0] = rdX(xo, 3, 0); xfc[1] = rdX(xo, 3, 1);) }
#pragma unroll
        for (int i = 0; i < 2; ++i)
          acc[i][j] = __builtin_amdgcn_mfma_f32_32x32x16_bf16(wf[n & 3], k4 == 1 ? xfb[i] : (k4 == 3 ? xfc[i] : xfa[i]), acc[i][j], 0, 0, 0);
        __builtin_amdgcn_sched_barrier(0);
      }
    }
  }
  STAMP(2);
  float rs[2];
#pragma unroll
  for (int i = 0; i < 2; ++i) {
    const LAS f32x4* sp = (const LAS f32x4*)(lds + K3_AUX + (wr * 64 + i * 32 + tk) * 32);
    const f32x4 s01 = sp[0], s23 = sp[1];
    const float mean = (s01[0] + s01[2] + s23[0] + s23[2]) * 0.25f;
    const float d0 = s01[0] - mean, d1 = s01[2] - mean, d2 = s23[0] - mean, d3 = s23[2] - mean;
    const float m2 = (s01[1] + s01[3]) + (s23[1] + s23[3]) + 144.0f * ((d0 * d0 + d1 * d1) + (d2 * d2 + d3 * d3));
    rs[i] = m2 * (1.0f / D_) + LN_EPS;
  }
  const __amdgpu_buffer_rsrc_t rsh = __builtin_amdgcn_make_buffer_rsrc((void*)H, 0, S_ * F_ * 2, 0x00020000);
  const int hofs = (((pf * 9 + J0) * 2 + h) * S_ + pm * 256 + wr * 64 + tk) * 16;
#pragma unroll
  for (int i = 0; i < 2; ++i) {
    const float c1 = -1.4426950408889634f * __builtin_amdgcn_rsqf(rs[i]);
    const f32x2 c1v = {c1, c1}, inv = {rs[i], rs[i]};
#pragma unroll
    for (int j = 0; j < NJ; ++j) {
      u32x4 hw;
#pragma unroll
      for (int r = 0; r < 4; ++r) {
        const f32x2 ag = {acc[i][j][2 * r], acc[i][j][2 * r + 1]}, au = {acc[i][j][8 + 2 * r], acc[i][j][8 + 2 * r + 1]};
        const f32x2 z = ag * c1v;
        const f32x2 e = {__builtin_amdgcn_exp2f(z.x), __builtin_amdgcn_exp2f(z.y)};
        const f32x2 d = e * inv + inv;
        const f32x2 rc = {__builtin_amdgcn_rcpf(d.x), __builtin_amdgcn_rcpf(d.y)};
        const f32x2 hv = (ag * au) * rc;
        hw[r] = pk2(hv.x, hv.y);
      }
      __builtin_amdgcn_raw_buffer_store_b128(hw, rsh, hofs + j * (2 * S_ * 16) + i * 512, 0, HAUX);
    }
  }
  STAMP(3);
}

__global__ __launch_bounds__(512, 2) void k3_gateup(const bf16_t* __restrict__ X, const bf16_t* __restrict__ Wg, const bf16_t* __restrict__ Wu,
                                                     const float* __restrict__ csum, const f32x2* __restrict__ stats, bf16_t* __restrict__ H,
                                                     const bf16_t* __restrict__ Wd) {
  __shared__ __attribute__((aligned(16))) char lds_[K3_LDS];
  LAS char* lds = (LAS char*)lds_;
  const unsigned lbase = ldsa(lds);
  const int tid = threadIdx.x, lane = tid & 63, wid = __builtin_amdgcn_readfirstlane(tid >> 6);
  const int bid = blockIdx.x, xcd = bid & 7, idx = bid >> 3;
  const int pm = (xcd >> 2) * 4 + (idx & 3), pf = (xcd & 3) * 8 + (idx >> 2);
  const int wr = wid & 3, wc = wid >> 2;
  STAMP(0);
  glds16((const char*)(stats + pm * 256 * 4), (unsigned)(tid * 16), lbase + K3_AUX + wid * 1024);
  {
    const int l36 = lane < 36 ? lane : 35;
    const int cw = wid < 6 ? wid : 0;
    if (lane < 36) glds16((const char*)(csum + cw * F_ + pf * 144), (unsigned)(l36 * 16), lbase + K3_CS + cw * 576);
  }
  unsigned xoff[4], woff[3];
#pragma unroll
  for (int i = 0; i < 4; ++i) {
    const int row = (wid * 4 + i) * 8 + (lane >> 3);
    const int c = (lane & 7) ^ ((row >> 1) & 7);
    xoff[i] = (unsigned)(((pm * 256 + row) * D_ + c * 8) * 2 - i * 1024);
  }
  const unsigned xdst = lbase + wid * 4096;
  glds_4((const char*)X, xoff[0], xoff[1], xoff[2], xoff[3], xdst);
#pragma unroll
  for (int i = 0; i < 3; ++i) {
    int ci = i * 64 + lane; if (ci > 143) ci = 143;
    const int kk = ci / 18, cp = ci - kk * 18;
    woff[i] = (unsigned)((((wid & 3) * 8 + kk) * F_ + pf * 144 + cp * 8) * 2 - i * 1024);
  }
  const char* wsrc = (const char*)((wid >> 2) ? Wu : Wg);
  const unsigned wdst = lbase + K3_WBASE + (wid >> 2) * K3_UOFF + (wid & 3) * 2304;
  glds_2q(wsrc, woff[0], woff[1], woff[2], wdst);
  glds_2q(wsrc + 32 * F_ * 2, woff[0], woff[1], woff[2], wdst + K3_WS);
  const unsigned pfoff = (unsigned)((xcd & 3) * (1152 * D_ * 2) + (idx * 8 + wid) * 5184 + lane * 16);
  if (wc == 0) k3_body<5, 0>(lds, (const char*)X, wsrc, H, xoff, woff, xdst, wdst, lane, wr, pm, pf, (const char*)Wd, pfoff);
  else k3_body<4, 5>(lds, (const char*)X, wsrc, H, xoff, woff, xdst, wdst, lane, wr, pm, pf, (const char*)Wd, pfoff);
  STAMPEND(3, 4);
}

constexpr int K4_XB = 16384, K4_STG = K4_XB + 18432, K4_LDS = 4 * K4_STG;

template <int NJ, int J0>
DEVI void k4_body(LAS char* lds, const char* X, const char* W, unsigned xoff, const unsigned (&woff)[3], unsigned sdst, unsigned wdst,
                  int lane, int wm, int split, int tile, float* __restrict__ P, unsigned* __restrict__ cnt) {
  auto stage = [&](int kt, int buf) {
    glds_2(X + (size_t)kt * (8 * S_ * 16), xoff, xoff, sdst + buf * K4_STG);
    glds_2q(W + (size_t)kt * 64 * D_ * 2, woff[0], woff[1], woff[2], wdst + buf * K4_STG);
  };
  const int g = lane >> 4, m = lane & 15, q4 = (lane >> 2) & 3, p4 = lane & 3;
  const int xa = g * 2048 + (wm * 32 + m) * 16;
  const int wa = K4_XB + ((g >> 1) * 16 + (g & 1) * 4 + q4) * 288 + J0 * 32 + p4 * 8;
  f32x4 acc[2][NJ];
#pragma unroll
  for (int i = 0; i < 2; ++i)
#pragma unroll
    for (int j = 0; j < NJ; ++j) acc[i][j] = (f32x4){0, 0, 0, 0};
  bf16x8 xa_[2], wa_[NJ], xb_[2], wb_[NJ];
  auto rd = [&](bf16x8 (&xf)[2], bf16x8 (&wf)[NJ], const LAS char* sb, int ks) {
#pragma unroll
    for (int i = 0; i < 2; ++i) xf[i] = lds_b128(sb + xa + ks * 8192 + i * 256);
#pragma unroll
    for (int j = 0; j < NJ; ++j) wf[j] = lds_tr2(sb + wa + ks * (32 * 288) + j * 32, sb + wa + ks * (32 * 288) + j * 32 + 8 * 288);
  };
  auto step = [&](bf16x8 (&xc)[2], bf16x8 (&wc)[NJ], bf16x8 (&xn)[2], bf16x8 (&wn)[NJ], const LAS char* sb, int ks) {
#pragma unroll
    for (int j = 0; j < NJ; ++j) {
      if (j < 2) xn[j] = lds_b128(sb + xa + ks * 8192 + j * 256);
      wn[j] = lds_tr2(sb + wa + ks * (32 * 288) + j * 32, sb + wa + ks * (32 * 288) + j * 32 + 8 * 288);
#pragma unroll
      for (int i = 0; i < 2; ++i) acc[i][j] = mfma16(wc[j], xc[i], acc[i][j]);
      __builtin_amdgcn_sched_barrier(0);
    }
  };
  WAIT_V(5); BAR();
  STAMPK(4, 1);
  rd(xa_, wa_, lds, 0);
  stage(2, 2);
  step(xa_, wa_, xb_, wb_, lds, 1);
  stage(3, 3);
  auto iter = [&](auto INFL, auto MORE, int t) {
    asm volatile("s_waitcnt vmcnt(%0) lgkmcnt(0)" ::"n"(decltype(INFL)::value * 5) : "memory");
    BAR();
    __builtin_amdgcn_sched_barrier(0);
    const LAS char* sn = lds + ((t + 1) & 3) * K4_STG;
    if (decltype(MORE)::value && NJ == 4) stage(t + 4, t & 3);
    step(xb_, wb_, xa_, wa_, sn, 0);
    if (decltype(MORE)::value && NJ == 5) stage(t + 4, t & 3);
    step(xa_, wa_, xb_, wb_, sn, 1);
  };
#pragma unroll 1
  for (int t = 0; t < 14; ++t) iter(IC<2>{}, IC<1>{}, t);
  iter(IC<2>{}, IC<0>{}, 14);
  iter(IC<1>{}, IC<0>{}, 15);
  iter(IC<0>{}, IC<0>{}, 16);
#pragma unroll
  for (int j = 0; j < NJ; ++j)
#pragma unroll
    for (int i = 0; i < 2; ++i) acc[i][j] = mfma16(wb_[j], xb_[i], acc[i][j]);
  STAMPK(4, 2);
  const __amdgpu_buffer_rsrc_t rs = __builtin_amdgcn_make_buffer_rsrc((void*)P, 0, 64 * 16 * 9 * 1024, 0x00020000);
  if (wm != split) {
    const int sb = ((tile * 4 + wm) * 4 + split) * 9 * 1024 + lane * 16;
#pragma unroll
    for (int j = 0; j < NJ; ++j) {
      const u32x4 v = {pk2(acc[0][j][0], acc[0][j][1]), pk2(acc[0][j][2], acc[0][j][3]), pk2(acc[1][j][0], acc[1][j][1]), pk2(acc[1][j][2], acc[1][j][3])};
      __builtin_amdgcn_raw_buffer_store_b128(v, rs, sb + (J0 + j) * 1024, 0, 16);
    }
    WAIT_V(0);
    if (lane == 0) __hip_atomic_fetch_add(cnt + (tile * 4 + wm) * 32, 1u, __ATOMIC_RELAXED, __HIP_MEMORY_SCOPE_AGENT);
  } else {
    LAS f32x4* own = (LAS f32x4*)(lds + 2 * K4_STG);
#pragma unroll
    for (int i = 0; i < 2; ++i)
#pragma unroll
      for (int j = 0; j < NJ; ++j) own[(i * 9 + J0 + j) * 64 + lane] = acc[i][j];
    if (NJ == 5) {
      unsigned spins = 0;
      while ((unsigned)__builtin_amdgcn_readfirstlane(__hip_atomic_load(cnt + (tile * 4 + split) * 32, __ATOMIC_RELAXED, __HIP_MEMORY_SCOPE_AGENT)) < 6u) {
        __builtin_amdgcn_s_sleep(1);
        if (++spins > (1u << 22)) break;
      }
    }
  }
}

__global__ __launch_bounds__(512, 2) void k4_down(const bf16_t* __restrict__ X, const bf16_t* __restrict__ W, const bf16_t* __restrict__ R,
                                                   bf16_t* __restrict__ Y, unsigned* __restrict__ cnt, float* __restrict__ P) {
  __shared__ __attribute__((aligned(16))) char lds_[K4_LDS];
  LAS char* lds = (LAS char*)lds_;
  const unsigned lbase = ldsa(lds);
  const int tid = threadIdx.x, lane = tid & 63, wid = __builtin_amdgcn_readfirstlane(tid >> 6);
  const int bid = blockIdx.x, xcd = bid & 7, idx = bid >> 3;
  const int split = xcd & 3;
  const int m0 = (xcd >> 2) * 1024 + (idx >> 2) * 128, n0 = (idx & 3) * 144, k0 = split * 1152;
  const int wm = wid & 3, wn = wid >> 2;
  STAMPK(4, 0);
  const unsigned xoff = (unsigned)((((k0 >> 3) + wid) * S_ + m0 + lane) * 16);
  unsigned woff[3];
#pragma unroll
  for (int i = 0; i < 3; ++i) {
    int ci = i * 64 + lane; if (ci > 143) ci = 143;
    const int rr = ci / 18, c = ci - rr * 18;
    woff[i] = (unsigned)(((k0 + wid * 8 + rr) * D_ + n0 + c * 8) * 2 - i * 1024);
  }
  const unsigned sdst = lbase + wid * 2048, wdst = lbase + K4_XB + wid * 2304;
#pragma unroll
  for (int s = 0; s < 2; ++s) {
    glds_2((const char*)X + (size_t)s * (8 * S_ * 16), xoff, xoff, sdst + s * K4_STG);
    glds_2q((const char*)W + (size_t)s * 64 * D_ * 2, woff[0], woff[1], woff[2], wdst + s * K4_STG);
  }
  const int tile = (m0 >> 7) * 4 + (idx & 3);
  if (wn == 0) k4_body<5, 0>(lds, (const char*)X, (const char*)W, xoff, woff, sdst, wdst, lane, wm, split, tile, P, cnt);
  else k4_body<4, 5>(lds, (const char*)X, (const char*)W, xoff, woff, sdst, wdst, lane, wm, split, tile, P, cnt);
  const int row = (tid >> 3) & 31, sub = tid & 7;
  const size_t gofs = (size_t)(m0 + split * 32 + row) * D_ + n0;
  u32x4 rv[3];
#pragma unroll
  for (int c = 0; c < 3; ++c) { const int ch = c * 8 + sub; rv[c] = *(const u32x4*)(R + gofs + (ch < 18 ? ch : 0) * 8); }
  WAIT_L0(); BAR();
  STAMPK(4, 3);
  const int g = lane >> 4, m = lane & 15;
  LAS f32x4* own = (LAS f32x4*)(lds + 2 * K4_STG);
  LAS float* rt = (LAS float*)lds;
  {
    const __amdgpu_buffer_rsrc_t rs = __builtin_amdgcn_make_buffer_rsrc((void*)P, 0, 64 * 16 * 9 * 1024, 0x00020000);
    u32x4 pv[2][3];
#pragma unroll
    for (int n = 0; n < 2; ++n) {
      const int j = wid + 8 * n < 9 ? wid + 8 * n : wid;
#pragma unroll
      for (int sx = 0; sx < 3; ++sx) {
        const int src = sx + (sx >= split);
        pv[n][sx] = __builtin_amdgcn_raw_buffer_load_b128(rs, (((tile * 4 + split) * 4 + src) * 9 + j) * 1024 + lane * 16, 0, 16);
      }
    }
#pragma unroll
    for (int n = 0; n < 2; ++n) {
      const int j = wid + 8 * n;
      if (j < 9) {
#pragma unroll
        for (int i = 0; i < 2; ++i) {
          f32x4 v = own[(i * 9 + j) * 64 + lane];
#pragma unroll
          for (int sx = 0; sx < 3; ++sx)
            v += (f32x4){bflo(pv[n][sx][2 * i]), bfhi(pv[n][sx][2 * i]), bflo(pv[n][sx][2 * i + 1]), bfhi(pv[n][sx][2 * i + 1])};
          *(LAS f32x4*)(rt + (i * 16 + m) * 148 + j * 16 + g * 4) = v;
        }
      }
    }
  }
  WAIT_L0(); BAR();
  if (tid < 256) {
#pragma unroll
    for (int c = 0; c < 3; ++c) {
      const int ch = c * 8 + sub;
      if (ch < 18) {
        const LAS float* p0 = rt + row * 148 + ch * 8;
        const f32x4 a0 = *(const LAS f32x4*)p0, a1 = *(const LAS f32x4*)(p0 + 4);
        *(u32x4*)(Y + gofs + ch * 8) = (u32x4){pk2(bflo(rv[c].x) + rbf(a0[0]), bfhi(rv[c].x) + rbf(a0[1])), pk2(bflo(rv[c].y) + rbf(a0[2]), bfhi(rv[c].y) + rbf(a0[3])),
                                               pk2(bflo(rv[c].z) + rbf(a1[0]), bfhi(rv[c].z) + rbf(a1[1])), pk2(bflo(rv[c].w) + rbf(a1[2]), bfhi(rv[c].w) + rbf(a1[3]))};
      }
    }
  }
  STAMPEND(4, 4);
}

extern "C" void kernel_launch(void* const* d_in, const int* in_sizes, int n_in,
                              void* d_out, int out_size, void* d_ws, size_t ws_size,
                              hipStream_t stream) {
  const bf16_t* x0 = (const bf16_t*)d_in[0];
  const bf16_t* wqkv = (const bf16_t*)d_in[1];
  const bf16_t* wo = (const bf16_t*)d_in[2];
  const bf16_t* wg = (const bf16_t*)d_in[4];
  const bf16_t* wu = (const bf16_t*)d_in[5];
  const bf16_t* wd = (const bf16_t*)d_in[6];
  bf16_t* out = (bf16_t*)d_out;
  char* ws = (char*)d_ws;
  constexpr size_t XB = (size_t)S_ * D_ * 2, HB = (size_t)S_ * F_ * 2, PB = (size_t)4 * S_ * D_ * 4;
  size_t off = 0;
  float* csum = (float*)(ws + off); off += 131072;
  unsigned* cnt = (unsigned*)(ws + off); off += NCNT * 128;
  char* wp = ws + off; off += (size_t)144 * K1_WS;
  const bf16_t* xin = x0;
  k0_prep<<<144 + 144 + 41, 256, 0, stream>>>(wqkv, wp, cnt, x0, wo);
  char* o = ws + off; off += XB;
  bf16_t* x1 = (bf16_t*)(ws + off); off += XB;
  f32x2* stats = (f32x2*)(ws + off); off += 65536;
  bf16_t* xm = (bf16_t*)(ws + off); off += XB;
  bf16_t* h = (bf16_t*)(ws + off); off += HB;
  float* P = (float*)(ws + off); off += PB;
  for (int l = 0; l < 2; ++l) {
    bf16_t* x2 = (l == 1) ? out : xm;
    k1_qkv_attn<<<256, l == 0 ? 576 : 512, 0, stream>>>(xin, wp, o, wo, x1, stats, cnt + (l * 512 + 256) * 32, wg, wu, csum);
    k3_gateup<<<256, 512, 0, stream>>>(x1, wg, wu, csum, stats, h, wd);
    k4_down<<<256, 512, 0, stream>>>(h, wd, x1, x2, cnt + (l * 512) * 32, P);
    xin = x2;
  }
}
```

```cpp
#include <hip/hip_runtime.h>
#include <hip/hip_bf16.h>
#include <stdint.h>

typedef unsigned short bf16_t;
typedef short bf16x8 __attribute__((ext_vector_type(8)));
typedef short bf16x4 __attribute__((ext_vector_type(4)));
typedef float f32x4 __attribute__((ext_vector_type(4)));
typedef float f32x2 __attribute__((ext_vector_type(2)));
typedef float f32x16 __attribute__((ext_vector_type(16)));
typedef unsigned u32x4 __attribute__((ext_vector_type(4)));
typedef unsigned u32x2 __attribute__((ext_vector_type(2)));
#define LAS __attribute__((address_space(3)))
#define DEVI __device__ __forceinline__

constexpr int S_ = 2048, D_ = 576, F_ = 4608, NH_ = 32, DH_ = 18;
constexpr float LN_EPS = 1e-5f;

DEVI float bf2f(unsigned b) { return __uint_as_float(b << 16); }
DEVI float bflo(unsigned w) { return __uint_as_float(w << 16); }
DEVI float bfhi(unsigned w) { return __uint_as_float(w & 0xffff0000u); }
DEVI unsigned pk2(float lo, float hi) {
  typedef __bf16 bf2v __attribute__((ext_vector_type(2)));
  bf2v v; v.x = (__bf16)lo; v.y = (__bf16)hi;
  return __builtin_bit_cast(unsigned, v);
}
template <int CTRL> DEVI float dpp(float x) { return __builtin_bit_cast(float, __builtin_amdgcn_update_dpp(0, __builtin_bit_cast(int, x), CTRL, 0xf, 0xf, true)); }
DEVI float sum8(float x) {
  x += dpp<0xB1>(x); x += dpp<0x4E>(x); x += dpp<0x141>(x); return x;
}
DEVI float rbf(float x) { return (float)((__bf16)x); }

#define RSTAMP(i)
#define RDUMP(n)
#define STAMPW(kid, i, cond)
#define STAMPK(kid, i)
#define STAMPEND(kid, i)
#define STAMP(i) STAMPK(3, i)
#define ABL_G(x) x
#define ABL_B(x) x
#define ABL_L(...) __VA_ARGS__
#define LNMIMIC 1
#define HAUX 16
#define GV 3
#define PRIO 0
template <int N> struct IC { static constexpr int value = N; };
template <int A, int B, class F> __device__ __forceinline__ void static_for(F&& f) {
  if constexpr (A < B) { f(IC<A>{}); static_for<A + 1, B>(f); }
}
#define WAIT_V(n) asm volatile("s_waitcnt vmcnt(" #n ")" ::: "memory")
#define WAIT_L0() asm volatile("s_waitcnt lgkmcnt(0)" ::: "memory")
#define BAR() do { asm volatile("" ::: "memory"); __builtin_amdgcn_s_barrier(); asm volatile("" ::: "memory"); } while (0)

DEVI void glds16(const char* sbase, unsigned voff, unsigned ldsaddr) {
  asm volatile("s_mov_b32 m0, %2\n\ts_nop 0\n\tglobal_load_lds_dwordx4 %0, %1" ::"v"(voff), "s"(sbase), "s"(ldsaddr) : "memory");
}
DEVI void glds_1(const char* sb, unsigned v0, unsigned la) { glds16(sb, v0, la); }
DEVI void glds_2(const char* sb, unsigned v0, unsigned v1, unsigned la) {
  asm volatile("s_mov_b32 m0, %3\n\ts_nop 0\n\tglobal_load_lds_dwordx4 %0, %2\n\tglobal_load_lds_dwordx4 %1, %2 offset:1024"
               ::"v"(v0), "v"(v1), "s"(sb), "s"(la) : "memory");
}
DEVI void glds_4(const char* sb, unsigned v0, unsigned v1, unsigned v2, unsigned v3, unsigned la) {
  asm volatile("s_mov_b32 m0, %5\n\ts_nop 0\n\tglobal_load_lds_dwordx4 %0, %4\n\tglobal_load_lds_dwordx4 %1, %4 offset:1024\n\t"
               "global_load_lds_dwordx4 %2, %4 offset:2048\n\tglobal_load_lds_dwordx4 %3, %4 offset:3072"
               ::"v"(v0), "v"(v1), "v"(v2), "v"(v3), "s"(sb), "s"(la) : "memory");
}
DEVI void glds_2q(const char* sb, unsigned v0, unsigned v1, unsigned v2, unsigned la) {
  asm volatile("s_mov_b32 m0, %4\n\ts_nop 0\n\tglobal_load_lds_dwordx4 %0, %3\n\tglobal_load_lds_dwordx4 %1, %3 offset:1024\n\t"
               "s_mov_b64 exec, 0xffff\n\tglobal_load_lds_dwordx4 %2, %3 offset:2048\n\ts_mov_b64 exec, -1"
               ::"v"(v0), "v"(v1), "v"(v2), "s"(sb), "s"(la) : "memory");
}
DEVI void glds_2m(const char* sb, unsigned v0, unsigned v1, unsigned la, unsigned long long m0_, unsigned long long m1_) {
  asm volatile("s_mov_b32 m0, %3\n\ts_mov_b64 exec, %4\n\tglobal_load_lds_dwordx4 %0, %2\n\t"
               "s_mov_b64 exec, %5\n\tglobal_load_lds_dwordx4 %1, %2 offset:1024\n\ts_mov_b64 exec, -1"
               ::"v"(v0), "v"(v1), "s"(sb), "s"(la), "s"(m0_), "s"(m1_) : "memory");
}
DEVI unsigned ldsa(const LAS char* p) { return (unsigned)(size_t)p; }
DEVI bf16x8 lds_b128(const LAS char* p) { return *(const LAS bf16x8*)p; }
DEVI bf16x8 lds_tr2(const LAS char* p0, const LAS char* p1) {
  bf16x4 a = __builtin_amdgcn_ds_read_tr16_b64_v4i16((LAS bf16x4*)p0);
  bf16x4 b = __builtin_amdgcn_ds_read_tr16_b64_v4i16((LAS bf16x4*)p1);
  return __builtin_shufflevector(a, b, 0, 1, 2, 3, 4, 5, 6, 7);
}
DEVI f32x4 mfma16(bf16x8 a, bf16x8 b, f32x4 c) { return __builtin_amdgcn_mfma_f32_16x16x32_bf16(a, b, c, 0, 0, 0); }

constexpr int K1_WR = 16 * 448 + 32;
constexpr int K1_WS = 14464;
constexpr int NCNT = 1024;

__global__ __launch_bounds__(256) void k0_prep(const bf16_t* __restrict__ Wqkv, char* __restrict__ Wp, unsigned* __restrict__ cnt,
                                                const bf16_t* __restrict__ X, const bf16_t* __restrict__ Wo) {
  STAMPK(0, 0);
  const int b = blockIdx.x;
  if (b < 144) {
    const int u = b, hg = u / 18, sl = u - hg * 18;
    char* dst = Wp + (size_t)u * K1_WS;
    u32x4 v[4];
#pragma unroll
    for (int i = 0; i < 4; ++i) {
      const int ci = i * 256 + threadIdx.x;
      const int reg = ci >= 450, cj = ci - reg * 450;
      const int rr = cj / 28;
      int ch = cj - rr * 28; if (ch > 26) ch = 26;
      const int seg = ch / 9, cc = ch - seg * 9;
      const int gcol = (seg == 2 ? 0 : (seg + 1) * D_) + hg * 72 + cc * 8;
      const int rc = rr > 15 ? 15 : rr;
      const int k = sl * 32 + (rc >> 3) * 16 + reg * 8 + (rc & 7);
      v[i] = *(const u32x4*)(Wqkv + (size_t)k * (3 * D_) + gcol);
    }
    if (u < NCNT / 256) cnt[(u * 256 + threadIdx.x) * 32] = 0u;
#pragma unroll
    for (int i = 0; i < 4; ++i) {
      const int ci = i * 256 + threadIdx.x;
      if (ci < 904) *(u32x4*)(dst + ci * 16) = v[i];
    }
  } else {
    const int u = b - 144;
    const char* src = u < 144 ? (const char*)X + (size_t)u * 16384 : (const char*)Wo + (size_t)(u - 144) * 16384;
    const int lim = u < 144 ? 16384 : (D_ * D_ * 2 - (u - 144) * 16384 < 16384 ? D_ * D_ * 2 - (u - 144) * 16384 : 16384);
#pragma unroll
    for (int i = 0; i < 4; ++i) {
      const int o = (i * 256 + threadIdx.x) * 16;
      if (o < lim) { const u32x4 v = *(const u32x4*)(src + o); asm volatile("" ::"v"(v)); }
    }
  }
  STAMPEND(0, 1);
}

constexpr int K1_XT = 80 * 128;
constexpr int K1_WBASE = 4 * K1_XT;
constexpr int K1_AUX = 156928;
constexpr int K1_LDS = K1_AUX + 768;
constexpr int K1_QS = 440;
constexpr int K1_OB = 35200, K1_OS = 160;
constexpr int K2_WR = 9216 + 128, K2_WS = K2_WR + 9216, K2_WBASE = 45568;

DEVI float dot2bf(unsigned a, unsigned b, float c) {
  typedef __bf16 bf2v __attribute__((ext_vector_type(2)));
  return __builtin_amdgcn_fdot2_f32_bf16(__builtin_bit_cast(bf2v, a), __builtin_bit_cast(bf2v, b), c, false);
}

constexpr int K2_RT = 44032;

template <int NJ, int J0>
DEVI void k2_gemm(LAS char* lds, int lane, int wm, int wk, const char* Wo, const unsigned (&woff)[3], unsigned wdst,
                  const char* OB, int obase, unsigned* __restrict__ cnt, int tid, int wid) {
  const int g = lane >> 4, m = lane & 15, q4 = (lane >> 2) & 3, p4 = lane & 3;
  const int wa = K2_WBASE + (g & 1) * K2_WR + ((wk * 2 + (g >> 1)) * 8 + q4) * 288 + J0 * 32 + p4 * 8;
  auto rdW = [&](bf16x8 (&wf)[NJ], int slot) {
#pragma unroll
    for (int j = 0; j < NJ; ++j) wf[j] = lds_tr2(lds + wa + slot * K2_WS + j * 32, lds + wa + slot * K2_WS + j * 32 + 4 * 288);
  };
  bf16x8 w[6][NJ];
  rdW(w[0], 2); rdW(w[1], 3); rdW(w[2], 4); rdW(w[3], 5);
  RSTAMP(8);
  WAIT_V(0); WAIT_L0();
  RSTAMP(9);
  BAR();
  if (tid == 0) __hip_atomic_fetch_add(cnt, 1u, __ATOMIC_RELAXED, __HIP_MEMORY_SCOPE_AGENT);
  RSTAMP(10);
#pragma unroll
  for (int t = 6; t < 9; ++t) glds_2q(Wo + (size_t)t * 64 * D_ * 2, woff[0], woff[1], woff[2], wdst + (t - 4) * K2_WS);
  rdW(w[4], 0); rdW(w[5], 1);
  RSTAMP(11);
  if (wid == 0) {
    unsigned spins = 0;
    while ((unsigned)__builtin_amdgcn_readfirstlane(__hip_atomic_load(cnt, __ATOMIC_RELAXED, __HIP_MEMORY_SCOPE_AGENT)) < 8u) {
      __builtin_amdgcn_s_sleep(1);
      if (++spins > (1u << 22)) break;
    }
  }
  WAIT_V(0); WAIT_L0(); BAR();
  RSTAMP(12);
  const __amdgpu_buffer_rsrc_t rso = __builtin_amdgcn_make_buffer_rsrc((void*)OB, 0, S_ * D_ * 2, 0x00020000);
  u32x4 xf[9];
#pragma unroll
  for (int t = 0; t < 9; ++t) xf[t] = __builtin_amdgcn_raw_buffer_load_b128(rso, obase + (wk * 4 + g) * 512 + (wm * 16 + m) * 16 + t * 4096, 0, 16);
  bf16x8 w6[NJ], w7[NJ], w8[NJ];
  rdW(w6, 2); rdW(w7, 3); rdW(w8, 4);
  RSTAMP(13);
  f32x4 acc[NJ];
#pragma unroll
  for (int j = 0; j < NJ; ++j) acc[j] = (f32x4){0, 0, 0, 0};
#pragma unroll
  for (int t = 0; t < 6; ++t)
#pragma unroll
    for (int j = 0; j < NJ; ++j) acc[j] = mfma16(w[t][j], __builtin_bit_cast(bf16x8, xf[t]), acc[j]);
#pragma unroll
  for (int j = 0; j < NJ; ++j) acc[j] = mfma16(w6[j], __builtin_bit_cast(bf16x8, xf[6]), acc[j]);
#pragma unroll
  for (int j = 0; j < NJ; ++j) acc[j] = mfma16(w7[j], __builtin_bit_cast(bf16x8, xf[7]), acc[j]);
#pragma unroll
  for (int j = 0; j < NJ; ++j) acc[j] = mfma16(w8[j], __builtin_bit_cast(bf16x8, xf[8]), acc[j]);
  RSTAMP(14);
  LAS float* rt = (LAS float*)(lds + K2_RT);
#pragma unroll
  for (int j = 0; j < NJ; ++j) *(LAS f32x4*)(rt + (wk * 32 + wm * 16 + m) * 148 + (J0 + j) * 16 + g * 4) = acc[j];
}

__global__ __launch_bounds__(576) void k1_qkv_attn(const bf16_t* __restrict__ X, const char* __restrict__ Wp, char* __restrict__ OB,
                                                       const bf16_t* __restrict__ Wo, bf16_t* __restrict__ Y, f32x2* __restrict__ stats,
                                                       unsigned* __restrict__ cnt, const bf16_t* __restrict__ Wg, const bf16_t* __restrict__ Wu,
                                                       float* __restrict__ csum) {
  __shared__ __attribute__((aligned(16))) char lds_[K1_LDS];
  LAS char* lds = (LAS char*)lds_;
  const unsigned lbase = ldsa(lds);
  const int tid = threadIdx.x, lane = tid & 63, wid = __builtin_amdgcn_readfirstlane(tid >> 6);
  const int bid = blockIdx.x, xcd = bid & 7, idx = bid >> 3;
  if (wid == 8) {
    if (bid >= 216) return;
    const int kq = bid / 72, cb = bid - kq * 72;
    const bf16_t* Wc = (cb < 36 ? Wg : Wu) + (size_t)(kq * 192 + (lane >> 4)) * F_ + (cb % 36) * 128 + (lane & 15) * 8;
    float c8[8] = {0, 0, 0, 0, 0, 0, 0, 0};
    u32x4 cv[4][6];
#pragma unroll
    for (int st = 0; st < 11; ++st) {
      if (st < 8) {
#pragma unroll
        for (int i = 0; i < 6; ++i) cv[st & 3][i] = *(const u32x4*)(Wc + (size_t)((st * 6 + i) * 4) * F_);
      }
      if (st >= 3) {
#pragma unroll
        for (int i = 0; i < 6; ++i)
#pragma unroll
          for (int e = 0; e < 4; ++e) { c8[2 * e] += bflo(cv[(st - 3) & 3][i][e]); c8[2 * e + 1] += bfhi(cv[(st - 3) & 3][i][e]); }
#pragma unroll
        for (int e = 0; e < 8; ++e) asm volatile("" : "+v"(c8[e]));
      }
      __builtin_amdgcn_sched_barrier(0);
      if (st < 10) BAR();
      __builtin_amdgcn_sched_barrier(0);
    }
#pragma unroll
    for (int e = 0; e < 8; ++e) { float t = c8[e]; t += __shfl_xor(t, 16); t += __shfl_xor(t, 32); c8[e] = t; }
    if (lane < 16) {
      float* cp = csum + kq * 2 * F_ + (cb < 36 ? 0 : F_) + (cb % 36) * 128 + lane * 8;
      *(f32x4*)cp = (f32x4){c8[0], c8[1], c8[2], c8[3]}; *(f32x4*)(cp + 4) = (f32x4){c8[4], c8[5], c8[6], c8[7]};
    }
    return;
  }
  const int tt = (xcd >> 1) * 8 + (idx & 7), hg = (xcd & 1) * 4 + (idx >> 3);
  const int t0 = tt * 64 - 16;
  STAMPK(1, 0);

  unsigned xoff[2], woff[2];
#pragma unroll
  for (int i = 0; i < 2; ++i) {
    int ci = i * 64 + lane; if (ci > 79) ci = 79;
    ci += wid * 80;
    const int row = ci >> 3, c = (ci & 7) ^ ((row >> 1) & 7);
    int grow = t0 + row; if (grow < 0) grow = 0;
    xoff[i] = (unsigned)((grow * D_ + c * 8) * 2 + 1024 - i * 1024);
  }
  woff[0] = (unsigned)((wid * 113 + lane) * 16); woff[1] = woff[0];
  const unsigned wdst = lbase + K1_WBASE + wid * 1808;
  const unsigned long long xmask = wid == 0 ? ~0xffull : ~0ull;
  auto stageX = [&](int kt) {
    glds_2m((const char*)X + kt * 128 - 1024, xoff[0], xoff[1], lbase + (kt & 3) * K1_XT + wid * 1280, xmask, 0xffffull);
  };
  auto stageW = [&](int s) {
    glds_2m(Wp + (size_t)(hg * 18 + s) * K1_WS, woff[0], woff[1], wdst + (s & 7) * K1_WS, ~0ull, 0x1ffffffffffffull);
  };
  auto stageT = [&](int t) { stageW(2 * t); stageX(t); stageW(2 * t + 1); };
  stageT(0); stageT(1);
  if (tid >= 64 && tid < 64 + 32) {
    const int c = tid - 64;
    *(LAS u32x4*)(lds + (c >> 3) * K1_XT + (c & 7) * 16) = (u32x4){0x3f803f80u, 0x3f803f80u, 0x3f803f80u, 0x3f803f80u};
  }
  const int th = hg >> 2, ct = hg & 3;
  unsigned w2off[3];
#pragma unroll
  for (int i = 0; i < 3; ++i) {
    int ci = i * 64 + lane; if (ci > 143) ci = 143;
    const int rr = ci / 18, c = ci - rr * 18;
    w2off[i] = (unsigned)(((wid * 8 + rr) * D_ + ct * 144 + c * 8) * 2 - i * 1024);
  }
  const unsigned w2dst = lbase + K2_WBASE + (wid & 1) * K2_WR + (wid >> 1) * 2304;
  auto stageB = [&](int kt) { glds_2q((const char*)Wo + (size_t)kt * 64 * D_ * 2, w2off[0], w2off[1], w2off[2], w2dst + ((kt + 2) % 6) * K2_WS); };

  const int g = lane >> 4, m = lane & 15, q4 = (lane >> 2) & 3, p4 = lane & 3;
  const int xa0 = m * 128 + ((g ^ (m >> 1)) * 16);
  const int wa0 = K1_WBASE + (g & 1) * K1_WR + ((g >> 1) * 8 + q4) * 448 + (wid < 7 ? wid : 0) * 64 + p4 * 8;
  f32x4 acc[5][2];
#pragma unroll
  for (int i = 0; i < 5; ++i) { acc[i][0] = (f32x4){0, 0, 0, 0}; acc[i][1] = (f32x4){0, 0, 0, 0}; }
  LAS f32x2* ms = (LAS f32x2*)(lds + K1_AUX);
  auto top = [&](int t) {
    __builtin_amdgcn_sched_barrier(0);
    if (t < 6) { asm volatile("s_waitcnt vmcnt(12) lgkmcnt(0)" ::: "memory"); } else { asm volatile("s_waitcnt vmcnt(6) lgkmcnt(0)" ::: "memory"); }
    BAR();
    __builtin_amdgcn_sched_barrier(0);
  };
  auto stg = [&](int t) {
    if (t < 5) stageT(t + 4);
    if (t == 6) { stageB(0); stageB(1); }
    if (t == 7) { stageB(2); stageB(3); }
  };
  WAIT_V(6); WAIT_L0(); BAR();
  RSTAMP(0);
  auto rdX = [&](bf16x8 (&xf)[5], int kt, int ks) {
#pragma unroll
    for (int i = 0; i < 5; ++i) xf[i] = lds_b128(lds + ((xa0 ^ (ks * 64)) + (kt & 3) * K1_XT + i * 2048));
  };
  auto mainloop = [&](auto EARLY) {
    bf16x8 xfa[5], xfb[5], wfa[2], wfb[2];
    auto rdW = [&](bf16x8 (&wf)[2], int s) {
#pragma unroll
      for (int j = 0; j < 2; ++j) wf[j] = lds_tr2(lds + (wa0 + (s & 7) * K1_WS + j * 32), lds + (wa0 + (s & 7) * K1_WS + j * 32 + 4 * 448));
    };
    auto step = [&](bf16x8 (&wc)[2], bf16x8 (&xc)[5], bf16x8 (&wn)[2], bf16x8 (&xn)[5], int s) {
#pragma unroll
      for (int i = 0; i < 5; ++i) {
        if (i < 2) wn[i] = lds_tr2(lds + (wa0 + (s & 7) * K1_WS + i * 32), lds + (wa0 + (s & 7) * K1_WS + i * 32 + 4 * 448));
        acc[i][0] = mfma16(wc[0], xc[i], acc[i][0]);
        __builtin_amdgcn_sched_barrier(0);
        xn[i] = lds_b128(lds + ((xa0 ^ ((s & 1) * 64)) + ((s >> 1) & 3) * K1_XT + i * 2048));
        acc[i][1] = mfma16(wc[1], xc[i], acc[i][1]);
        __builtin_amdgcn_sched_barrier(0);
      }
    };
    rdX(xfa, 0, 0); rdW(wfa, 0);
    stageT(2);
    step(wfa, xfa, wfb, xfb, 1);
    stageT(3);
#pragma unroll
    for (int t = 0; t < 8; ++t) {
      top(t);
      if (decltype(EARLY)::value) stg(t);
      step(wfb, xfb, wfa, xfa, 2 * t + 2);
      if (!decltype(EARLY)::value) stg(t);
      step(wfa, xfa, wfb, xfb, 2 * t + 3);
    }
#pragma unroll
    for (int i = 0; i < 5; ++i)
#pragma unroll
      for (int j = 0; j < 2; ++j) acc[i][j] = mfma16(wfb[j], xfb[i], acc[i][j]);
  };
  if (wid < 4) {
    mainloop(IC<1>{});
  } else if (wid < 7) {
    mainloop(IC<0>{});
  } else {
    bf16x8 xfa[5], xfb[5];
    const bf16x8 ones = {0x3f80, 0x3f80, 0x3f80, 0x3f80, 0x3f80, 0x3f80, 0x3f80, 0x3f80};
    auto step = [&](bf16x8 (&xc)[5], bf16x8 (&xn)[5], int s) {
#pragma unroll
      for (int i = 0; i < 5; ++i) {
        acc[i][0] = mfma16(xc[i], xc[i], acc[i][0]);
        xn[i] = lds_b128(lds + ((xa0 ^ ((s & 1) * 64)) + ((s >> 1) & 3) * K1_XT + i * 2048));
        acc[i][1] = mfma16(ones, xc[i], acc[i][1]);
        __builtin_amdgcn_sched_barrier(0);
      }
    };
    rdX(xfa, 0, 0);
    stageT(2);
    step(xfa, xfb, 1);
    stageT(3);
#pragma unroll
    for (int t = 0; t < 8; ++t) {
      top(t);
      step(xfb, xfa, 2 * t + 2);
      stg(t);
      step(xfa, xfb, 2 * t + 3);
    }
#pragma unroll
    for (int i = 0; i < 5; ++i) { acc[i][0] = mfma16(xfb[i], xfb[i], acc[i][0]); acc[i][1] = mfma16(ones, xfb[i], acc[i][1]); }
#pragma unroll
    for (int i = 0; i < 5; ++i) {
      const float q = (m & 2) ? ((m & 1) ? acc[i][0][3] : acc[i][0][2]) : ((m & 1) ? acc[i][0][1] : acc[i][0][0]);
      const float mean = acc[i][1][0] * (1.0f / D_);
      const float var = fmaxf(q * (1.0f / D_) - mean * mean, 0.f);
      const float den = rbf(__builtin_amdgcn_sqrtf(rbf(rbf(var) + LN_EPS)));
      if (g == (m >> 2)) ms[i * 16 + m] = (f32x2){rbf(mean), __builtin_amdgcn_rcpf(den)};
    }
  }
  RSTAMP(1);
  WAIT_L0(); BAR();
  RSTAMP(2);
  stageB(4); stageB(5);
  RSTAMP(3);
  if (wid < 7) {
    f32x2 mr[5];
#pragma unroll
    for (int i = 0; i < 5; ++i) mr[i] = ms[i * 16 + m];
    f32x4 cs[2];
#pragma unroll
    for (int j = 0; j < 2; ++j)
#pragma unroll
      for (int r = 0; r < 4; ++r) cs[j][r] = dpp<0x150>(acc[0][j][r]);
    LAS char* qp = lds + m * K1_QS + (wid * 32 + g * 4) * 2;
    const bool ok1 = wid * 32 + 16 + g * 4 < 216;
#pragma unroll
    for (int i = 0; i < 5; ++i) {
      const float nm = -mr[i].x * mr[i].y;
#pragma unroll
      for (int j = 0; j < 2; ++j) {
        const f32x4 v = acc[i][j] * mr[i].y + cs[j] * nm;
        if (j == 0 || ok1) *(LAS u32x2*)(qp + i * 16 * K1_QS + j * 32) = (u32x2){pk2(v[0], v[1]), pk2(v[2], v[3])};
      }
    }
  }
  RSTAMP(4);
  WAIT_L0(); BAR();
  RSTAMP(5);
  {
    const int half = lane & 1, hh = wid & 3, tok = (wid >> 2) * 32 + (lane >> 1);
    const int row = tok + 16;
    const int gpos = t0 + row;
    const LAS unsigned* bp = (const LAS unsigned*)(lds + row * K1_QS + hh * 36 + half * 20);
    unsigned qv[5], kv[8][5], vv[8][5];
#pragma unroll
    for (int d = 0; d < 5; ++d) qv[d] = bp[72 + (d < 4 ? d : (half ? 0 : 4))];
#pragma unroll
    for (int w = 0; w < 8; ++w)
#pragma unroll
      for (int d = 0; d < 5; ++d) kv[w][d] = bp[d - w * (2 * K1_QS / 4)];
#pragma unroll
    for (int w = 0; w < 8; ++w)
#pragma unroll
      for (int d = 0; d < 5; ++d) vv[w][d] = bp[36 + d - w * (2 * K1_QS / 4)];
    __builtin_amdgcn_sched_barrier(0);
    if (half) qv[4] = 0u;
    float lg[8];
#pragma unroll
    for (int w = 0; w < 8; ++w) {
      float s = 0.f;
#pragma unroll
      for (int d = 0; d < 5; ++d) s = dot2bf(qv[d], kv[w][d], s);
      s += dpp<0xB1>(s);
      lg[w] = (gpos - 2 * w >= 0) ? s * (0.23570226039551584f * 1.4426950408889634f) : -1e30f;
    }
    float mx = lg[0];
#pragma unroll
    for (int w = 1; w < 8; ++w) mx = fmaxf(mx, lg[w]);
    float den = 0.f;
#pragma unroll
    for (int w = 0; w < 8; ++w) { lg[w] = __builtin_amdgcn_exp2f(lg[w] - mx); den += lg[w]; }
    const float inv = __builtin_amdgcn_rcpf(den);
    f32x2 ov2[5];
#pragma unroll
    for (int d = 0; d < 5; ++d) ov2[d] = (f32x2){0.f, 0.f};
#pragma unroll
    for (int w = 0; w < 8; ++w) {
      const f32x2 pw = {lg[w], lg[w]};
#pragma unroll
      for (int d = 0; d < 5; ++d) ov2[d] += pw * (f32x2){bflo(vv[w][d]), bfhi(vv[w][d])};
    }
    float ov[10];
#pragma unroll
    for (int d = 0; d < 5; ++d) { ov[2 * d] = ov2[d].x * inv; ov[2 * d + 1] = ov2[d].y * inv; }
    LAS unsigned* op = (LAS unsigned*)(lds + K1_OB + tok * K1_OS + hh * 36 + half * 20);
#pragma unroll
    for (int d = 0; d < 5; ++d) if (d < 4 || !half) op[d] = pk2(ov[2 * d], ov[2 * d + 1]);
  }
  RSTAMP(6);
  asm volatile("s_waitcnt vmcnt(6) lgkmcnt(0)" ::: "memory");
  BAR();
  RSTAMP(7);
  {
    const __amdgpu_buffer_rsrc_t rso = __builtin_amdgcn_make_buffer_rsrc((void*)OB, 0, S_ * D_ * 2, 0x00020000);
#pragma unroll
    for (int i = 0; i < 2; ++i) {
      const int ci = i * 512 + tid;
      if (ci < 576) {
        const int r = ci & 31, q = ci >> 5, hf = q / 9, cc = q - hf * 9;
        const u32x4 v = *(const LAS u32x4*)(lds + K1_OB + (hf * 32 + r) * K1_OS + cc * 16);
        __builtin_amdgcn_raw_buffer_store_b128(v, rso, (((tt * 2 + hf) * 72 + hg * 9 + cc) * 32 + r) * 16, 0, 16);
      }
    }
  }
  const int prow = tid >> 4, sub = tid & 15;
  const size_t gofs = (size_t)(tt * 64 + th * 32 + prow) * D_ + ct * 144;
  u32x4 rv[2];
  rv[0] = *(const u32x4*)(X + gofs + sub * 8);
  rv[1] = *(const u32x4*)(X + gofs + (sub < 2 ? 16 + sub : sub) * 8);
  {
    const int wm = wid & 1, wk = (wid >> 1) & 1;
    const int obase = (tt * 2 + th) * 72 * 32 * 16;
    if (wid < 4) k2_gemm<5, 0>(lds, lane, wm, wk, (const char*)Wo, w2off, w2dst, OB, obase, cnt + tt * 32, tid, wid);
    else k2_gemm<4, 5>(lds, lane, wm, wk, (const char*)Wo, w2off, w2dst, OB, obase, cnt + tt * 32, tid, wid);
  }
  WAIT_L0(); BAR();
  RSTAMP(15);
  {
    const LAS float* rt = (const LAS float*)(lds + K2_RT);
    float xv[2][8];
    float s = 0.f;
#pragma unroll
    for (int c = 0; c < 2; ++c) {
      const bool ok = c == 0 || sub < 2;
      const int ch = ok ? c * 16 + sub : sub;
      const LAS float* p0 = rt + prow * 148 + ch * 8;
      const f32x4 a0 = *(const LAS f32x4*)p0 + *(const LAS f32x4*)(p0 + 32 * 148);
      const f32x4 a1 = *(const LAS f32x4*)(p0 + 4) + *(const LAS f32x4*)(p0 + 32 * 148 + 4);
      const u32x4 y = {pk2(a0[0], a0[1]), pk2(a0[2], a0[3]), pk2(a1[0], a1[1]), pk2(a1[2], a1[3])};
      u32x4 xo;
#pragma unroll
      for (int e = 0; e < 4; ++e) {
        xo[e] = pk2(bflo(rv[c][e]) + bflo(y[e]), bfhi(rv[c][e]) + bfhi(y[e]));
        xv[c][2 * e] = ok ? bflo(xo[e]) : 0.f; xv[c][2 * e + 1] = ok ? bfhi(xo[e]) : 0.f;
      }
      if (ok) *(u32x4*)(Y + gofs + ch * 8) = xo;
#pragma unroll
      for (int e = 0; e < 8; ++e) s += xv[c][e];
    }
    s = sum8(s); s += dpp<0x140>(s);
    const float mean = s * (1.0f / 144.0f);
    float q = 0.f;
#pragma unroll
    for (int c = 0; c < 2; ++c)
#pragma unroll
      for (int e = 0; e < 8; ++e) { const float d = xv[c][e] - mean; q += (c == 0 || sub < 2) ? d * d : 0.f; }
    q = sum8(q); q += dpp<0x140>(q);
    if (sub == 0) stats[(tt * 64 + th * 32 + prow) * 4 + ct] = (f32x2){mean, q};
  }
  RSTAMP(16);
  STAMPEND(1, 1);
  RSTAMP(17);
  RDUMP(18);
}

constexpr int K3_XS = 32768, K3_UOFF = 9216 + 128, K3_WS = K3_UOFF + 9216;
constexpr int K3_WBASE = 2 * K3_XS, K3_AUX = K3_WBASE + 4 * K3_WS;
constexpr int K3_CS = K3_AUX + 8192;
constexpr int K3_DUMMY = K3_CS + 3584;
constexpr int K3_LDS = K3_DUMMY + 4096;

template <int NJ, int J0>
DEVI void k3_body(LAS char* lds, const char* X, const char* wsrc, bf16_t* __restrict__ H, const unsigned (&xoff)[4], const unsigned (&woff)[3],
                  unsigned xdst, unsigned wdst, int lane, int wr, int pm, int pf, const char* Wd, unsigned pfoff) {
  auto stageX = [&](int t) { glds_4(X + t * 128, xoff[0], xoff[1], xoff[2], xoff[3], xdst + (t & 1) * K3_XS); };
  auto stageW = [&](int s) { glds_2q(wsrc + (size_t)s * 32 * F_ * 2, woff[0], woff[1], woff[2], wdst + (s & 3) * K3_WS); };
  const int tk = lane & 31, h = lane >> 5, q4 = (lane >> 2) & 3, p4 = lane & 3, isup = (lane >> 4) & 1;
  const int xa = (wr * 64 + tk) * 128 + ((h ^ ((tk >> 1) & 7)) * 16);
  const int wa = K3_WBASE + isup * K3_UOFF + (h * 8 + q4) * 288 + J0 * 32 + p4 * 8;
  auto rdW = [&](int wo, int kk, int j) -> bf16x8 {
    const LAS char* p = lds + (wa + wo + kk * (16 * 288) + j * 32);
    return lds_tr2(p, p + 4 * 288);
  };
  auto rdX = [&](int xo, int k4, int i) -> bf16x8 {
    return lds_b128(lds + ((xa ^ (k4 * 32)) + xo + i * 4096));
  };
  f32x16 acc[2][NJ];
#pragma unroll
  for (int i = 0; i < 2; ++i)
#pragma unroll
    for (int j = 0; j < NJ; ++j)
#pragma unroll
      for (int r = 0; r < 16; ++r) acc[i][j][r] = 0.f;
  auto correction = [&]() {
    bf16x8 mf[2];
#pragma unroll
    for (int i = 0; i < 2; ++i) {
      const LAS f32x4* sp = (const LAS f32x4*)(lds + K3_AUX + (wr * 64 + i * 32 + tk) * 32);
      const f32x4 s01 = sp[0], s23 = sp[1];
      const float mean = rbf((s01[0] + s01[2] + s23[0] + s23[2]) * 0.25f);
      const float m0 = rbf(-mean), m1 = rbf(-mean - m0), m2_ = (-mean - m0) - m1;
      u32x4 w = {pk2(m0, m0), pk2(m0, m1), pk2(m1, m2_), 0u};
      if (h) w = (u32x4){0u, 0u, 0u, 0u};
      mf[i] = __builtin_bit_cast(bf16x8, w);
    }
#pragma unroll
    for (int j = 0; j < NJ; ++j) {
      const LAS float* cp = (const LAS float*)(lds + K3_CS) + isup * 144 + (J0 + j) * 16 + (lane & 15);
      const float c = (cp[0] + cp[288]) + cp[576];
      const float c0 = rbf(c), c1 = rbf(c - c0), c2 = (c - c0) - c1;
      const u32x4 w = {pk2(c0, c1), pk2(c2, c0), pk2(c1, c0), 0u};
      const bf16x8 cf = __builtin_bit_cast(bf16x8, w);
#pragma unroll
      for (int i = 0; i < 2; ++i) acc[i][j] = __builtin_amdgcn_mfma_f32_32x32x16_bf16(cf, mf[i], acc[i][j], 0, 0, 0);
    }
  };
  WAIT_V(10); BAR();
  correction();
  bf16x8 xfa[2], xfb[2], xfc[2], wf[4];
  WAIT_V(3); BAR();
  STAMP(1);
  xfa[0] = rdX(0, 0, 0); xfa[1] = rdX(0, 0, 1);
  wf[0] = rdW(0, 0, 0); wf[1] = rdW(0, 0, 1);
  stageX(1); stageW(2);

  if (PRIO == 1 && NJ == 5) __builtin_amdgcn_s_setprio(1);
  if (PRIO == 2 && NJ == 4) __builtin_amdgcn_s_setprio(1);
#pragma unroll 1
  for (int t = 0; t < 9; ++t) {
    const int tp = t & 1;
    const int wo = tp * (2 * K3_WS), won = (1 - tp) * (2 * K3_WS);
    const int xo = tp * K3_XS, xon = (1 - tp) * K3_XS;
#pragma unroll
    for (int k4 = 0; k4 < 4; ++k4) {
      if (k4 == 0) {
        if (t < 8) { WAIT_V(7); }
        ABL_B(BAR());
      }
      if (k4 == 2) {
        if (t < 7) { WAIT_V(3); } else if (t == 7) { WAIT_V(0); }
        WAIT_L0();
        ABL_B(BAR());
        if (t == 7) {
          glds_4(Wd, pfoff, pfoff, pfoff, pfoff, ldsa(lds) + K3_DUMMY);
          glds_2(Wd + 3136, pfoff, pfoff, ldsa(lds) + K3_DUMMY);
        }
      }
      __builtin_amdgcn_sched_barrier(0);
#pragma unroll
      for (int j = 0; j < NJ; ++j) {
        const int n = k4 * NJ + j;
        if (j == 0) {
          if (k4 == (NJ == 4 ? 0 : 1) && t < 8) stageW(2 * t + 3);
          if (k4 == (NJ == 4 ? 2 : 3) && t < 7) { stageW(2 * t + 4); stageX(t + 2); }
        }
        {
          const int n2 = n + 2, k42 = n2 / NJ, j2 = n2 % NJ;
          if (k42 < 4) { ABL_L(wf[n2 & 3] = rdW(wo + (k42 >> 1) * K3_WS, k42 & 1, j2);) }
          else { ABL_L(wf[n2 & 3] = rdW(won, 0, j2);) }
        }
        if (j == 1) {
          if (k4 == 0) { ABL_L(xfb[0] = rdX(xo, 1, 0); xfb[1] = rdX(xo, 1, 1);) }
          if (k4 == 1) { ABL_L(xfa[0] = rdX(xo, 2, 0); xfa[1] = rdX(xo, 2, 1);) }
          if (k4 == 3) { ABL_L(xfa[0] = rdX(xon, 0, 0); xfa[1] = rdX(xon, 0, 1);) }
        }
        if (j == 2 && k4 == 1) { ABL_L(xfc[0] = rdX(xo, 3, 0); xfc[1] = rdX(xo, 3, 1);) }
#pragma unroll
        for (int i = 0; i < 2; ++i)
          acc[i][j] = __builtin_amdgcn_mfma_f32_32x32x16_bf16(wf[n & 3], k4 == 1 ? xfb[i] : (k4 == 3 ? xfc[i] : xfa[i]), acc[i][j], 0, 0, 0);
        __builtin_amdgcn_sched_barrier(0);
      }
    }
  }
  STAMP(2);
  float rs[2];
#pragma unroll
  for (int i = 0; i < 2; ++i) {
    const LAS f32x4* sp = (const LAS f32x4*)(lds + K3_AUX + (wr * 64 + i * 32 + tk) * 32);
    const f32x4 s01 = sp[0], s23 = sp[1];
    const float mean = (s01[0] + s01[2] + s23[0] + s23[2]) * 0.25f;
    const float d0 = s01[0] - mean, d1 = s01[2] - mean, d2 = s23[0] - mean, d3 = s23[2] - mean;
    const float m2 = (s01[1] + s01[3]) + (s23[1] + s23[3]) + 144.0f * ((d0 * d0 + d1 * d1) + (d2 * d2 + d3 * d3));
    { const float den = rbf(__builtin_amdgcn_sqrtf(rbf(rbf(m2 * (1.0f / D_)) + LN_EPS))); rs[i] = den * den; }
  }
  const __amdgpu_buffer_rsrc_t rsh = __builtin_amdgcn_make_buffer_rsrc((void*)H, 0, S_ * F_ * 2, 0x00020000);
  const int hofs = (((pf * 9 + J0) * 2 + h) * S_ + pm * 256 + wr * 64 + tk) * 16;
#pragma unroll
  for (int i = 0; i < 2; ++i) {
    const float c1 = -1.4426950408889634f * __builtin_amdgcn_rsqf(rs[i]);
    const f32x2 c1v = {c1, c1}, inv = {rs[i], rs[i]};
#pragma unroll
    for (int j = 0; j < NJ; ++j) {
      u32x4 hw;
#pragma unroll
      for (int r = 0; r < 4; ++r) {
        const f32x2 ag = {acc[i][j][2 * r], acc[i][j][2 * r + 1]}, au = {acc[i][j][8 + 2 * r], acc[i][j][8 + 2 * r + 1]};
        const f32x2 z = ag * c1v;
        const f32x2 e = {__builtin_amdgcn_exp2f(z.x), __builtin_amdgcn_exp2f(z.y)};
        const f32x2 d = e * inv + inv;
        const f32x2 rc = {__builtin_amdgcn_rcpf(d.x), __builtin_amdgcn_rcpf(d.y)};
        const f32x2 hv = (ag * au) * rc;
        hw[r] = pk2(hv.x, hv.y);
      }
      __builtin_amdgcn_raw_buffer_store_b128(hw, rsh, hofs + j * (2 * S_ * 16) + i * 512, 0, HAUX);
    }
  }
  STAMP(3);
}

__global__ __launch_bounds__(512, 2) void k3_gateup(const bf16_t* __restrict__ X, const bf16_t* __restrict__ Wg, const bf16_t* __restrict__ Wu,
                                                     const float* __restrict__ csum, const f32x2* __restrict__ stats, bf16_t* __restrict__ H,
                                                     const bf16_t* __restrict__ Wd) {
  __shared__ __attribute__((aligned(16))) char lds_[K3_LDS];
  LAS char* lds = (LAS char*)lds_;
  const unsigned lbase = ldsa(lds);
  const int tid = threadIdx.x, lane = tid & 63, wid = __builtin_amdgcn_readfirstlane(tid >> 6);
  const int bid = blockIdx.x, xcd = bid & 7, idx = bid >> 3;
  const int pm = (xcd >> 2) * 4 + (idx & 3), pf = (xcd & 3) * 8 + (idx >> 2);
  const int wr = wid & 3, wc = wid >> 2;
  STAMP(0);
  glds16((const char*)(stats + pm * 256 * 4), (unsigned)(tid * 16), lbase + K3_AUX + wid * 1024);
  {
    const int l36 = lane < 36 ? lane : 35;
    const int cw = wid < 6 ? wid : 0;
    if (lane < 36) glds16((const char*)(csum + cw * F_ + pf * 144), (unsigned)(l36 * 16), lbase + K3_CS + cw * 576);
  }
  unsigned xoff[4], woff[3];
#pragma unroll
  for (int i = 0; i < 4; ++i) {
    const int row = (wid * 4 + i) * 8 + (lane >> 3);
    const int c = (lane & 7) ^ ((row >> 1) & 7);
    xoff[i] = (unsigned)(((pm * 256 + row) * D_ + c * 8) * 2 - i * 1024);
  }
  const unsigned xdst = lbase + wid * 4096;
  glds_4((const char*)X, xoff[0], xoff[1], xoff[2], xoff[3], xdst);
#pragma unroll
  for (int i = 0; i < 3; ++i) {
    int ci = i * 64 + lane; if (ci > 143) ci = 143;
    const int kk = ci / 18, cp = ci - kk * 18;
    woff[i] = (unsigned)((((wid & 3) * 8 + kk) * F_ + pf * 144 + cp * 8) * 2 - i * 1024);
  }
  const char* wsrc = (const char*)((wid >> 2) ? Wu : Wg);
  const unsigned wdst = lbase + K3_WBASE + (wid >> 2) * K3_UOFF + (wid & 3) * 2304;
  glds_2q(wsrc, woff[0], woff[1], woff[2], wdst);
  glds_2q(wsrc + 32 * F_ * 2, woff[0], woff[1], woff[2], wdst + K3_WS);
  const unsigned pfoff = (unsigned)((xcd & 3) * (1152 * D_ * 2) + (idx * 8 + wid) * 5184 + lane * 16);
  if (wc == 0) k3_body<5, 0>(lds, (const char*)X, wsrc, H, xoff, woff, xdst, wdst, lane, wr, pm, pf, (const char*)Wd, pfoff);
  else k3_body<4, 5>(lds, (const char*)X, wsrc, H, xoff, woff, xdst, wdst, lane, wr, pm, pf, (const char*)Wd, pfoff);
  STAMPEND(3, 4);
}

constexpr int K4_XB = 16384, K4_STG = K4_XB + 18432, K4_LDS = 4 * K4_STG;

template <int NJ, int J0>
DEVI void k4_body(LAS char* lds, const char* X, const char* W, unsigned xoff, const unsigned (&woff)[3], unsigned sdst, unsigned wdst,
                  int lane, int wm, int split, int tile, float* __restrict__ P, unsigned* __restrict__ cnt) {
  auto stage = [&](int kt, int buf) {
    glds_2(X + (size_t)kt * (8 * S_ * 16), xoff, xoff, sdst + buf * K4_STG);
    glds_2q(W + (size_t)kt * 64 * D_ * 2, woff[0], woff[1], woff[2], wdst + buf * K4_STG);
  };
  const int g = lane >> 4, m = lane & 15, q4 = (lane >> 2) & 3, p4 = lane & 3;
  const int xa = g * 2048 + (wm * 32 + m) * 16;
  const int wa = K4_XB + ((g >> 1) * 16 + (g & 1) * 4 + q4) * 288 + J0 * 32 + p4 * 8;
  f32x4 acc[2][NJ];
#pragma unroll
  for (int i = 0; i < 2; ++i)
#pragma unroll
    for (int j = 0; j < NJ; ++j) acc[i][j] = (f32x4){0, 0, 0, 0};
  bf16x8 xa_[2], wa_[NJ], xb_[2], wb_[NJ];
  auto rd = [&](bf16x8 (&xf)[2], bf16x8 (&wf)[NJ], const LAS char* sb, int ks) {
#pragma unroll
    for (int i = 0; i < 2; ++i) xf[i] = lds_b128(sb + xa + ks * 8192 + i * 256);
#pragma unroll
    for (int j = 0; j < NJ; ++j) wf[j] = lds_tr2(sb + wa + ks * (32 * 288) + j * 32, sb + wa + ks * (32 * 288) + j * 32 + 8 * 288);
  };
  auto step = [&](bf16x8 (&xc)[2], bf16x8 (&wc)[NJ], bf16x8 (&xn)[2], bf16x8 (&wn)[NJ], const LAS char* sb, int ks) {
#pragma unroll
    for (int j = 0; j < NJ; ++j) {
      if (j < 2) xn[j] = lds_b128(sb + xa + ks * 8192 + j * 256);
      wn[j] = lds_tr2(sb + wa + ks * (32 * 288) + j * 32, sb + wa + ks * (32 * 288) + j * 32 + 8 * 288);
#pragma unroll
      for (int i = 0; i < 2; ++i) acc[i][j] = mfma16(wc[j], xc[i], acc[i][j]);
      __builtin_amdgcn_sched_barrier(0);
    }
  };
  WAIT_V(5); BAR();
  STAMPK(4, 1);
  rd(xa_, wa_, lds, 0);
  stage(2, 2);
  step(xa_, wa_, xb_, wb_, lds, 1);
  stage(3, 3);
  auto iter = [&](auto INFL, auto MORE, int t) {
    asm volatile("s_waitcnt vmcnt(%0) lgkmcnt(0)" ::"n"(decltype(INFL)::value * 5) : "memory");
    BAR();
    __builtin_amdgcn_sched_barrier(0);
    const LAS char* sn = lds + ((t + 1) & 3) * K4_STG;
    if (decltype(MORE)::value && NJ == 4) stage(t + 4, t & 3);
    step(xb_, wb_, xa_, wa_, sn, 0);
    if (decltype(MORE)::value && NJ == 5) stage(t + 4, t & 3);
    step(xa_, wa_, xb_, wb_, sn, 1);
  };
#pragma unroll 1
  for (int t = 0; t < 14; ++t) iter(IC<2>{}, IC<1>{}, t);
  iter(IC<2>{}, IC<0>{}, 14);
  iter(IC<1>{}, IC<0>{}, 15);
  iter(IC<0>{}, IC<0>{}, 16);
#pragma unroll
  for (int j = 0; j < NJ; ++j)
#pragma unroll
    for (int i = 0; i < 2; ++i) acc[i][j] = mfma16(wb_[j], xb_[i], acc[i][j]);
  STAMPK(4, 2);
  const __amdgpu_buffer_rsrc_t rs = __builtin_amdgcn_make_buffer_rsrc((void*)P, 0, 64 * 16 * 9 * 1024, 0x00020000);
  if (wm != split) {
    const int sb = ((tile * 4 + wm) * 4 + split) * 9 * 1024 + lane * 16;
#pragma unroll
    for (int j = 0; j < NJ; ++j) {
      const u32x4 v = {pk2(acc[0][j][0], acc[0][j][1]), pk2(acc[0][j][2], acc[0][j][3]), pk2(acc[1][j][0], acc[1][j][1]), pk2(acc[1][j][2], acc[1][j][3])};
      __builtin_amdgcn_raw_buffer_store_b128(v, rs, sb + (J0 + j) * 1024, 0, 16);
    }
    WAIT_V(0);
    if (lane == 0) __hip_atomic_fetch_add(cnt + (tile * 4 + wm) * 32, 1u, __ATOMIC_RELAXED, __HIP_MEMORY_SCOPE_AGENT);
  } else {
    LAS f32x4* own = (LAS f32x4*)(lds + 2 * K4_STG);
#pragma unroll
    for (int i = 0; i < 2; ++i)
#pragma unroll
      for (int j = 0; j < NJ; ++j) own[(i * 9 + J0 + j) * 64 + lane] = acc[i][j];
    if (NJ == 5) {
      unsigned spins = 0;
      while ((unsigned)__builtin_amdgcn_readfirstlane(__hip_atomic_load(cnt + (tile * 4 + split) * 32, __ATOMIC_RELAXED, __HIP_MEMORY_SCOPE_AGENT)) < 6u) {
        __builtin_amdgcn_s_sleep(1);
        if (++spins > (1u << 22)) break;
      }
    }
  }
}

__global__ __launch_bounds__(512, 2) void k4_down(const bf16_t* __restrict__ X, const bf16_t* __restrict__ W, const bf16_t* __restrict__ R,
                                                   bf16_t* __restrict__ Y, unsigned* __restrict__ cnt, float* __restrict__ P) {
  __shared__ __attribute__((aligned(16))) char lds_[K4_LDS];
  LAS char* lds = (LAS char*)lds_;
  const unsigned lbase = ldsa(lds);
  const int tid = threadIdx.x, lane = tid & 63, wid = __builtin_amdgcn_readfirstlane(tid >> 6);
  const int bid = blockIdx.x, xcd = bid & 7, idx = bid >> 3;
  const int split = xcd & 3;
  const int m0 = (xcd >> 2) * 1024 + (idx >> 2) * 128, n0 = (idx & 3) * 144, k0 = split * 1152;
  const int wm = wid & 3, wn = wid >> 2;
  STAMPK(4, 0);
  const unsigned xoff = (unsigned)((((k0 >> 3) + wid) * S_ + m0 + lane) * 16);
  unsigned woff[3];
#pragma unroll
  for (int i = 0; i < 3; ++i) {
    int ci = i * 64 + lane; if (ci > 143) ci = 143;
    const int rr = ci / 18, c = ci - rr * 18;
    woff[i] = (unsigned)(((k0 + wid * 8 + rr) * D_ + n0 + c * 8) * 2 - i * 1024);
  }
  const unsigned sdst = lbase + wid * 2048, wdst = lbase + K4_XB + wid * 2304;
#pragma unroll
  for (int s = 0; s < 2; ++s) {
    glds_2((const char*)X + (size_t)s * (8 * S_ * 16), xoff, xoff, sdst + s * K4_STG);
    glds_2q((const char*)W + (size_t)s * 64 * D_ * 2, woff[0], woff[1], woff[2], wdst + s * K4_STG);
  }
  const int tile = (m0 >> 7) * 4 + (idx & 3);
  if (wn == 0) k4_body<5, 0>(lds, (const char*)X, (const char*)W, xoff, woff, sdst, wdst, lane, wm, split, tile, P, cnt);
  else k4_body<4, 5>(lds, (const char*)X, (const char*)W, xoff, woff, sdst, wdst, lane, wm, split, tile, P, cnt);
  const int row = (tid >> 3) & 31, sub = tid & 7;
  const size_t gofs = (size_t)(m0 + split * 32 + row) * D_ + n0;
  u32x4 rv[3];
#pragma unroll
  for (int c = 0; c < 3; ++c) { const int ch = c * 8 + sub; rv[c] = *(const u32x4*)(R + gofs + (ch < 18 ? ch : 0) * 8); }
  WAIT_L0(); BAR();
  STAMPK(4, 3);
  const int g = lane >> 4, m = lane & 15;
  LAS f32x4* own = (LAS f32x4*)(lds + 2 * K4_STG);
  LAS float* rt = (LAS float*)lds;
  {
    const __amdgpu_buffer_rsrc_t rs = __builtin_amdgcn_make_buffer_rsrc((void*)P, 0, 64 * 16 * 9 * 1024, 0x00020000);
    u32x4 pv[2][3];
#pragma unroll
    for (int n = 0; n < 2; ++n) {
      const int j = wid + 8 * n < 9 ? wid + 8 * n : wid;
#pragma unroll
      for (int sx = 0; sx < 3; ++sx) {
        const int src = sx + (sx >= split);
        pv[n][sx] = __builtin_amdgcn_raw_buffer_load_b128(rs, (((tile * 4 + split) * 4 + src) * 9 + j) * 1024 + lane * 16, 0, 16);
      }
    }
#pragma unroll
    for (int n = 0; n < 2; ++n) {
      const int j = wid + 8 * n;
      if (j < 9) {
#pragma unroll
        for (int i = 0; i < 2; ++i) {
          f32x4 v = own[(i * 9 + j) * 64 + lane];
#pragma unroll
          for (int sx = 0; sx < 3; ++sx)
            v += (f32x4){bflo(pv[n][sx][2 * i]), bfhi(pv[n][sx][2 * i]), bflo(pv[n][sx][2 * i + 1]), bfhi(pv[n][sx][2 * i + 1])};
          *(LAS f32x4*)(rt + (i * 16 + m) * 148 + j * 16 + g * 4) = v;
        }
      }
    }
  }
  WAIT_L0(); BAR();
  if (tid < 256) {
#pragma unroll
    for (int c = 0; c < 3; ++c) {
      const int ch = c * 8 + sub;
      if (ch < 18) {
        const LAS float* p0 = rt + row * 148 + ch * 8;
        const f32x4 a0 = *(const LAS f32x4*)p0, a1 = *(const LAS f32x4*)(p0 + 4);
        *(u32x4*)(Y + gofs + ch * 8) = (u32x4){pk2(bflo(rv[c].x) + rbf(a0[0]), bfhi(rv[c].x) + rbf(a0[1])), pk2(bflo(rv[c].y) + rbf(a0[2]), bfhi(rv[c].y) + rbf(a0[3])),
                                               pk2(bflo(rv[c].z) + rbf(a1[0]), bfhi(rv[c].z) + rbf(a1[1])), pk2(bflo(rv[c].w) + rbf(a1[2]), bfhi(rv[c].w) + rbf(a1[3]))};
      }
    }
  }
  STAMPEND(4, 4);
}

extern "C" void kernel_launch(void* const* d_in, const int* in_sizes, int n_in,
                              void* d_out, int out_size, void* d_ws, size_t ws_size,
                              hipStream_t stream) {
  const bf16_t* x0 = (const bf16_t*)d_in[0];
  const bf16_t* wqkv = (const bf16_t*)d_in[1];
  const bf16_t* wo = (const bf16_t*)d_in[2];
  const bf16_t* wg = (const bf16_t*)d_in[4];
  const bf16_t* wu = (const bf16_t*)d_in[5];
  const bf16_t* wd = (const bf16_t*)d_in[6];
  bf16_t* out = (bf16_t*)d_out;
  char* ws = (char*)d_ws;
  constexpr size_t XB = (size_t)S_ * D_ * 2, HB = (size_t)S_ * F_ * 2, PB = (size_t)4 * S_ * D_ * 4;
  size_t off = 0;
  float* csum = (float*)(ws + off); off += 131072;
  unsigned* cnt = (unsigned*)(ws + off); off += NCNT * 128;
  char* wp = ws + off; off += (size_t)144 * K1_WS;
  const bf16_t* xin = x0;
  k0_prep<<<144 + 144 + 41, 256, 0, stream>>>(wqkv, wp, cnt, x0, wo);
  char* o = ws + off; off += XB;
  bf16_t* x1 = (bf16_t*)(ws + off); off += XB;
  f32x2* stats = (f32x2*)(ws + off); off += 65536;
  bf16_t* xm = (bf16_t*)(ws + off); off += XB;
  bf16_t* h = (bf16_t*)(ws + off); off += HB;
  float* P = (float*)(ws + off); off += PB;
  for (int l = 0; l < 2; ++l) {
    bf16_t* x2 = (l == 1) ? out : xm;
    k1_qkv_attn<<<256, l == 0 ? 576 : 512, 0, stream>>>(xin, wp, o, wo, x1, stats, cnt + (l * 512 + 256) * 32, wg, wu, csum);
    k3_gateup<<<256, 512, 0, stream>>>(x1, wg, wu, csum, stats, h, wd);
    k4_down<<<256, 512, 0, stream>>>(h, wd, x1, x2, cnt + (l * 512) * 32, P);
    xin = x2;
  }
}
```

```cpp
#include <hip/hip_runtime.h>
#include <hip/hip_bf16.h>
#include <stdint.h>

typedef unsigned short bf16_t;
typedef short bf16x8 __attribute__((ext_vector_type(8)));
typedef short bf16x4 __attribute__((ext_vector_type(4)));
typedef float f32x4 __attribute__((ext_vector_type(4)));
typedef float f32x2 __attribute__((ext_vector_type(2)));
typedef float f32x16 __attribute__((ext_vector_type(16)));
typedef unsigned u32x4 __attribute__((ext_vector_type(4)));
typedef unsigned u32x2 __attribute__((ext_vector_type(2)));
#define LAS __attribute__((address_space(3)))
#define DEVI __device__ __forceinline__

constexpr int S_ = 2048, D_ = 576, F_ = 4608, NH_ = 32, DH_ = 18;
constexpr float LN_EPS = 1e-5f;

DEVI float bf2f(unsigned b) { return __uint_as_float(b << 16); }
DEVI float bflo(unsigned w) { return __uint_as_float(w << 16); }
DEVI float bfhi(unsigned w) { return __uint_as_float(w & 0xffff0000u); }
DEVI unsigned pk2(float lo, float hi) {
  typedef __bf16 bf2v __attribute__((ext_vector_type(2)));
  bf2v v; v.x = (__bf16)lo; v.y = (__bf16)hi;
  return __builtin_bit_cast(unsigned, v);
}
template <int CTRL> DEVI float dpp(float x) { return __builtin_bit_cast(float, __builtin_amdgcn_update_dpp(0, __builtin_bit_cast(int, x), CTRL, 0xf, 0xf, true)); }
DEVI float sum8(float x) {
  x += dpp<0xB1>(x); x += dpp<0x4E>(x); x += dpp<0x141>(x); return x;
}
DEVI float rbf(float x) { return (float)((__bf16)x); }

#define RSTAMP(i)
#define RDUMP(n)
#define STAMPW(kid, i, cond)
#define STAMPK(kid, i)
#define STAMPEND(kid, i)
#define STAMP(i) STAMPK(3, i)
#define ABL_G(x) x
#define ABL_B(x) x
#define ABL_L(...) __VA_ARGS__
#define LNMIMIC 1
#define HAUX 16
#define GV 3
#define PRIO 0
template <int N> struct IC { static constexpr int value = N; };
template <int A, int B, class F> __device__ __forceinline__ void static_for(F&& f) {
  if constexpr (A < B) { f(IC<A>{}); static_for<A + 1, B>(f); }
}
#define WAIT_V(n) asm volatile("s_waitcnt vmcnt(" #n ")" ::: "memory")
#define WAIT_L0() asm volatile("s_waitcnt lgkmcnt(0)" ::: "memory")
#define BAR() do { asm volatile("" ::: "memory"); __builtin_amdgcn_s_barrier(); asm volatile("" ::: "memory"); } while (0)

DEVI void glds16(const char* sbase, unsigned voff, unsigned ldsaddr) {
  asm volatile("s_mov_b32 m0, %2\n\ts_nop 0\n\tglobal_load_lds_dwordx4 %0, %1" ::"v"(voff), "s"(sbase), "s"(ldsaddr) : "memory");
}
DEVI void glds_1(const char* sb, unsigned v0, unsigned la) { glds16(sb, v0, la); }
DEVI void glds_2(const char* sb, unsigned v0, unsigned v1, unsigned la) {
  asm volatile("s_mov_b32 m0, %3\n\ts_nop 0\n\tglobal_load_lds_dwordx4 %0, %2\n\tglobal_load_lds_dwordx4 %1, %2 offset:1024"
               ::"v"(v0), "v"(v1), "s"(sb), "s"(la) : "memory");
}
DEVI void glds_4(const char* sb, unsigned v0, unsigned v1, unsigned v2, unsigned v3, unsigned la) {
  asm volatile("s_mov_b32 m0, %5\n\ts_nop 0\n\tglobal_load_lds_dwordx4 %0, %4\n\tglobal_load_lds_dwordx4 %1, %4 offset:1024\n\t"
               "global_load_lds_dwordx4 %2, %4 offset:2048\n\tglobal_load_lds_dwordx4 %3, %4 offset:3072"
               ::"v"(v0), "v"(v1), "v"(v2), "v"(v3), "s"(sb), "s"(la) : "memory");
}
DEVI void glds_2q(const char* sb, unsigned v0, unsigned v1, unsigned v2, unsigned la) {
  asm volatile("s_mov_b32 m0, %4\n\ts_nop 0\n\tglobal_load_lds_dwordx4 %0, %3\n\tglobal_load_lds_dwordx4 %1, %3 offset:1024\n\t"
               "s_mov_b64 exec, 0xffff\n\tglobal_load_lds_dwordx4 %2, %3 offset:2048\n\ts_mov_b64 exec, -1"
               ::"v"(v0), "v"(v1), "v"(v2), "s"(sb), "s"(la) : "memory");
}
DEVI void glds_2m(const char* sb, unsigned v0, unsigned v1, unsigned la, unsigned long long m0_, unsigned long long m1_) {
  asm volatile("s_mov_b32 m0, %3\n\ts_mov_b64 exec, %4\n\tglobal_load_lds_dwordx4 %0, %2\n\t"
               "s_mov_b64 exec, %5\n\tglobal_load_lds_dwordx4 %1, %2 offset:1024\n\ts_mov_b64 exec, -1"
               ::"v"(v0), "v"(v1), "s"(sb), "s"(la), "s"(m0_), "s"(m1_) : "memory");
}
DEVI unsigned ldsa(const LAS char* p) { return (unsigned)(size_t)p; }
DEVI bf16x8 lds_b128(const LAS char* p) { return *(const LAS bf16x8*)p; }
DEVI bf16x8 lds_tr2(const LAS char* p0, const LAS char* p1) {
  bf16x4 a = __builtin_amdgcn_ds_read_tr16_b64_v4i16((LAS bf16x4*)p0);
  bf16x4 b = __builtin_amdgcn_ds_read_tr16_b64_v4i16((LAS bf16x4*)p1);
  return __builtin_shufflevector(a, b, 0, 1, 2, 3, 4, 5, 6, 7);
}
DEVI f32x4 mfma16(bf16x8 a, bf16x8 b, f32x4 c) { return __builtin_amdgcn_mfma_f32_16x16x32_bf16(a, b, c, 0, 0, 0); }

constexpr int K1_WR = 16 * 448 + 32;
constexpr int K1_WS = 14464;
constexpr int NCNT = 1024;

__global__ __launch_bounds__(256) void k0_prep(const bf16_t* __restrict__ Wqkv, char* __restrict__ Wp, unsigned* __restrict__ cnt,
                                                const bf16_t* __restrict__ X, const bf16_t* __restrict__ Wo) {
  STAMPK(0, 0);
  const int b = blockIdx.x;
  {
    unsigned long long pc0, pc1;
    asm volatile("s_getpc_b64 %0" : "=s"(pc0));
    asm volatile("s_getpc_b64 s[40:41]\n\ts_add_u32 s40, s40, k9_end@rel32@lo+4\n\ts_addc_u32 s41, s41, k9_end@rel32@hi+12\n\ts_mov_b64 %0, s[40:41]"
                 : "=s"(pc1) : : "s40", "s41", "scc");
    const unsigned long long a = (pc0 & ~15ull) + (unsigned long long)(b >> 3) * 4096 + threadIdx.x * 16;
    if (pc1 > pc0 && a + 16 <= pc1) { const u32x4 v = *(const u32x4*)a; asm volatile("" ::"v"(v)); }
  }
  if (b < 144) {
    const int u = b, hg = u / 18, sl = u - hg * 18;
    char* dst = Wp + (size_t)u * K1_WS;
    u32x4 v[4];
#pragma unroll
    for (int i = 0; i < 4; ++i) {
      const int ci = i * 256 + threadIdx.x;
      const int reg = ci >= 450, cj = ci - reg * 450;
      const int rr = cj / 28;
      int ch = cj - rr * 28; if (ch > 26) ch = 26;
      const int seg = ch / 9, cc = ch - seg * 9;
      const int gcol = (seg == 2 ? 0 : (seg + 1) * D_) + hg * 72 + cc * 8;
      const int rc = rr > 15 ? 15 : rr;
      const int k = sl * 32 + (rc >> 3) * 16 + reg * 8 + (rc & 7);
      v[i] = *(const u32x4*)(Wqkv + (size_t)k * (3 * D_) + gcol);
    }
    if (u < NCNT / 256) cnt[(u * 256 + threadIdx.x) * 32] = 0u;
#pragma unroll
    for (int i = 0; i < 4; ++i) {
      const int ci = i * 256 + threadIdx.x;
      if (ci < 904) *(u32x4*)(dst + ci * 16) = v[i];
    }
  } else {
    const int u = b - 144;
    const char* src = u < 144 ? (const char*)X + (size_t)u * 16384 : (const char*)Wo + (size_t)(u - 144) * 16384;
    const int lim = u < 144 ? 16384 : (D_ * D_ * 2 - (u - 144) * 16384 < 16384 ? D_ * D_ * 2 - (u - 144) * 16384 : 16384);
#pragma unroll
    for (int i = 0; i < 4; ++i) {
      const int o = (i * 256 + threadIdx.x) * 16;
      if (o < lim) { const u32x4 v = *(const u32x4*)(src + o); asm volatile("" ::"v"(v)); }
    }
  }
  STAMPEND(0, 1);
}

constexpr int K1_XT = 80 * 128;
constexpr int K1_WBASE = 4 * K1_XT;
constexpr int K1_AUX = 156928;
constexpr int K1_LDS = K1_AUX + 768;
constexpr int K1_QS = 440;
constexpr int K1_OB = 35200, K1_OS = 160;
constexpr int K2_WR = 9216 + 128, K2_WS = K2_WR + 9216, K2_WBASE = 45568;

DEVI float dot2bf(unsigned a, unsigned b, float c) {
  typedef __bf16 bf2v __attribute__((ext_vector_type(2)));
  return __builtin_amdgcn_fdot2_f32_bf16(__builtin_bit_cast(bf2v, a), __builtin_bit_cast(bf2v, b), c, false);
}

constexpr int K2_RT = 44032;

template <int NJ, int J0>
DEVI void k2_gemm(LAS char* lds, int lane, int wm, int wk, const char* Wo, const unsigned (&woff)[3], unsigned wdst,
                  const char* OB, int obase, unsigned* __restrict__ cnt, int tid, int wid) {
  const int g = lane >> 4, m = lane & 15, q4 = (lane >> 2) & 3, p4 = lane & 3;
  const int wa = K2_WBASE + (g & 1) * K2_WR + ((wk * 2 + (g >> 1)) * 8 + q4) * 288 + J0 * 32 + p4 * 8;
  auto rdW = [&](bf16x8 (&wf)[NJ], int slot) {
#pragma unroll
    for (int j = 0; j < NJ; ++j) wf[j] = lds_tr2(lds + wa + slot * K2_WS + j * 32, lds + wa + slot * K2_WS + j * 32 + 4 * 288);
  };
  bf16x8 w[6][NJ];
  rdW(w[0], 2); rdW(w[1], 3); rdW(w[2], 4); rdW(w[3], 5);
  RSTAMP(8);
  WAIT_V(0); WAIT_L0();
  RSTAMP(9);
  BAR();
  if (tid == 0) __hip_atomic_fetch_add(cnt, 1u, __ATOMIC_RELAXED, __HIP_MEMORY_SCOPE_AGENT);
  RSTAMP(10);
#pragma unroll
  for (int t = 6; t < 9; ++t) glds_2q(Wo + (size_t)t * 64 * D_ * 2, woff[0], woff[1], woff[2], wdst + (t - 4) * K2_WS);
  rdW(w[4], 0); rdW(w[5], 1);
  RSTAMP(11);
  if (wid == 0) {
    unsigned spins = 0;
    while ((unsigned)__builtin_amdgcn_readfirstlane(__hip_atomic_load(cnt, __ATOMIC_RELAXED, __HIP_MEMORY_SCOPE_AGENT)) < 8u) {
      __builtin_amdgcn_s_sleep(1);
      if (++spins > (1u << 22)) break;
    }
  }
  WAIT_V(0); WAIT_L0(); BAR();
  RSTAMP(12);
  const __amdgpu_buffer_rsrc_t rso = __builtin_amdgcn_make_buffer_rsrc((void*)OB, 0, S_ * D_ * 2, 0x00020000);
  u32x4 xf[9];
#pragma unroll
  for (int t = 0; t < 9; ++t) xf[t] = __builtin_amdgcn_raw_buffer_load_b128(rso, obase + (wk * 4 + g) * 512 + (wm * 16 + m) * 16 + t * 4096, 0, 16);
  bf16x8 w6[NJ], w7[NJ], w8[NJ];
  rdW(w6, 2); rdW(w7, 3); rdW(w8, 4);
  RSTAMP(13);
  f32x4 acc[NJ];
#pragma unroll
  for (int j = 0; j < NJ; ++j) acc[j] = (f32x4){0, 0, 0, 0};
#pragma unroll
  for (int t = 0; t < 6; ++t)
#pragma unroll
    for (int j = 0; j < NJ; ++j) acc[j] = mfma16(w[t][j], __builtin_bit_cast(bf16x8, xf[t]), acc[j]);
#pragma unroll
  for (int j = 0; j < NJ; ++j) acc[j] = mfma16(w6[j], __builtin_bit_cast(bf16x8, xf[6]), acc[j]);
#pragma unroll
  for (int j = 0; j < NJ; ++j) acc[j] = mfma16(w7[j], __builtin_bit_cast(bf16x8, xf[7]), acc[j]);
#pragma unroll
  for (int j = 0; j < NJ; ++j) acc[j] = mfma16(w8[j], __builtin_bit_cast(bf16x8, xf[8]), acc[j]);
  RSTAMP(14);
  LAS float* rt = (LAS float*)(lds + K2_RT);
#pragma unroll
  for (int j = 0; j < NJ; ++j) *(LAS f32x4*)(rt + (wk * 32 + wm * 16 + m) * 148 + (J0 + j) * 16 + g * 4) = acc[j];
}

__global__ __launch_bounds__(576) void k1_qkv_attn(const bf16_t* __restrict__ X, const char* __restrict__ Wp, char* __restrict__ OB,
                                                       const bf16_t* __restrict__ Wo, bf16_t* __restrict__ Y, f32x2* __restrict__ stats,
                                                       unsigned* __restrict__ cnt, const bf16_t* __restrict__ Wg, const bf16_t* __restrict__ Wu,
                                                       float* __restrict__ csum) {
  __shared__ __attribute__((aligned(16))) char lds_[K1_LDS];
  LAS char* lds = (LAS char*)lds_;
  const unsigned lbase = ldsa(lds);
  const int tid = threadIdx.x, lane = tid & 63, wid = __builtin_amdgcn_readfirstlane(tid >> 6);
  const int bid = blockIdx.x, xcd = bid & 7, idx = bid >> 3;
  if (wid == 8) {
    if (bid >= 216) return;
    const int kq = bid / 72, cb = bid - kq * 72;
    const bf16_t* Wc = (cb < 36 ? Wg : Wu) + (size_t)(kq * 192 + (lane >> 4)) * F_ + (cb % 36) * 128 + (lane & 15) * 8;
    float c8[8] = {0, 0, 0, 0, 0, 0, 0, 0};
    u32x4 cv[4][6];
#pragma unroll
    for (int st = 0; st < 11; ++st) {
      if (st < 8) {
#pragma unroll
        for (int i = 0; i < 6; ++i) cv[st & 3][i] = *(const u32x4*)(Wc + (size_t)((st * 6 + i) * 4) * F_);
      }
      if (st >= 3) {
#pragma unroll
        for (int i = 0; i < 6; ++i)
#pragma unroll
          for (int e = 0; e < 4; ++e) { c8[2 * e] += bflo(cv[(st - 3) & 3][i][e]); c8[2 * e + 1] += bfhi(cv[(st - 3) & 3][i][e]); }
#pragma unroll
        for (int e = 0; e < 8; ++e) asm volatile("" : "+v"(c8[e]));
      }
      __builtin_amdgcn_sched_barrier(0);
      if (st < 10) BAR();
      __builtin_amdgcn_sched_barrier(0);
    }
#pragma unroll
    for (int e = 0; e < 8; ++e) { float t = c8[e]; t += __shfl_xor(t, 16); t += __shfl_xor(t, 32); c8[e] = t; }
    if (lane < 16) {
      float* cp = csum + kq * 2 * F_ + (cb < 36 ? 0 : F_) + (cb % 36) * 128 + lane * 8;
      *(f32x4*)cp = (f32x4){c8[0], c8[1], c8[2], c8[3]}; *(f32x4*)(cp + 4) = (f32x4){c8[4], c8[5], c8[6], c8[7]};
    }
    return;
  }
  const int tt = (xcd >> 1) * 8 + (idx & 7), hg = (xcd & 1) * 4 + (idx >> 3);
  const int t0 = tt * 64 - 16;
  STAMPK(1, 0);

  unsigned xoff[2], woff[2];
#pragma unroll
  for (int i = 0; i < 2; ++i) {
    int ci = i * 64 + lane; if (ci > 79) ci = 79;
    ci += wid * 80;
    const int row = ci >> 3, c = (ci & 7) ^ ((row >> 1) & 7);
    int grow = t0 + row; if (grow < 0) grow = 0;
    xoff[i] = (unsigned)((grow * D_ + c * 8) * 2 + 1024 - i * 1024);
  }
  woff[0] = (unsigned)((wid * 113 + lane) * 16); woff[1] = woff[0];
  const unsigned wdst = lbase + K1_WBASE + wid * 1808;
  const unsigned long long xmask = wid == 0 ? ~0xffull : ~0ull;
  auto stageX = [&](int kt) {
    glds_2m((const char*)X + kt * 128 - 1024, xoff[0], xoff[1], lbase + (kt & 3) * K1_XT + wid * 1280, xmask, 0xffffull);
  };
  auto stageW = [&](int s) {
    glds_2m(Wp + (size_t)(hg * 18 + s) * K1_WS, woff[0], woff[1], wdst + (s & 7) * K1_WS, ~0ull, 0x1ffffffffffffull);
  };
  auto stageT = [&](int t) { stageW(2 * t); stageX(t); stageW(2 * t + 1); };
  stageT(0); stageT(1);
  if (tid >= 64 && tid < 64 + 32) {
    const int c = tid - 64;
    *(LAS u32x4*)(lds + (c >> 3) * K1_XT + (c & 7) * 16) = (u32x4){0x3f803f80u, 0x3f803f80u, 0x3f803f80u, 0x3f803f80u};
  }
  const int th = hg >> 2, ct = hg & 3;
  unsigned w2off[3];
#pragma unroll
  for (int i = 0; i < 3; ++i) {
    int ci = i * 64 + lane; if (ci > 143) ci = 143;
    const int rr = ci / 18, c = ci - rr * 18;
    w2off[i] = (unsigned)(((wid * 8 + rr) * D_ + ct * 144 + c * 8) * 2 - i * 1024);
  }
  const unsigned w2dst = lbase + K2_WBASE + (wid & 1) * K2_WR + (wid >> 1) * 2304;
  auto stageB = [&](int kt) { glds_2q((const char*)Wo + (size_t)kt * 64 * D_ * 2, w2off[0], w2off[1], w2off[2], w2dst + ((kt + 2) % 6) * K2_WS); };

  const int g = lane >> 4, m = lane & 15, q4 = (lane >> 2) & 3, p4 = lane & 3;
  const int xa0 = m * 128 + ((g ^ (m >> 1)) * 16);
  const int wa0 = K1_WBASE + (g & 1) * K1_WR + ((g >> 1) * 8 + q4) * 448 + (wid < 7 ? wid : 0) * 64 + p4 * 8;
  f32x4 acc[5][2];
#pragma unroll
  for (int i = 0; i < 5; ++i) { acc[i][0] = (f32x4){0, 0, 0, 0}; acc[i][1] = (f32x4){0, 0, 0, 0}; }
  LAS f32x2* ms = (LAS f32x2*)(lds + K1_AUX);
  auto top = [&](int t) {
    __builtin_amdgcn_sched_barrier(0);
    if (t < 6) { asm volatile("s_waitcnt vmcnt(12) lgkmcnt(0)" ::: "memory"); } else { asm volatile("s_waitcnt vmcnt(6) lgkmcnt(0)" ::: "memory"); }
    BAR();
    __builtin_amdgcn_sched_barrier(0);
  };
  auto stg = [&](int t) {
    if (t < 5) stageT(t + 4);
    if (t == 6) { stageB(0); stageB(1); }
    if (t == 7) { stageB(2); stageB(3); }
  };
  WAIT_V(6); WAIT_L0(); BAR();
  RSTAMP(0);
  auto rdX = [&](bf16x8 (&xf)[5], int kt, int ks) {
#pragma unroll
    for (int i = 0; i < 5; ++i) xf[i] = lds_b128(lds + ((xa0 ^ (ks * 64)) + (kt & 3) * K1_XT + i * 2048));
  };
  auto mainloop = [&](auto EARLY) {
    bf16x8 xfa[5], xfb[5], wfa[2], wfb[2];
    auto rdW = [&](bf16x8 (&wf)[2], int s) {
#pragma unroll
      for (int j = 0; j < 2; ++j) wf[j] = lds_tr2(lds + (wa0 + (s & 7) * K1_WS + j * 32), lds + (wa0 + (s & 7) * K1_WS + j * 32 + 4 * 448));
    };
    auto step = [&](bf16x8 (&wc)[2], bf16x8 (&xc)[5], bf16x8 (&wn)[2], bf16x8 (&xn)[5], int s) {
#pragma unroll
      for (int i = 0; i < 5; ++i) {
        if (i < 2) wn[i] = lds_tr2(lds + (wa0 + (s & 7) * K1_WS + i * 32), lds + (wa0 + (s & 7) * K1_WS + i * 32 + 4 * 448));
        acc[i][0] = mfma16(wc[0], xc[i], acc[i][0]);
        __builtin_amdgcn_sched_barrier(0);
        xn[i] = lds_b128(lds + ((xa0 ^ ((s & 1) * 64)) + ((s >> 1) & 3) * K1_XT + i * 2048));
        acc[i][1] = mfma16(wc[1], xc[i], acc[i][1]);
        __builtin_amdgcn_sched_barrier(0);
      }
    };
    rdX(xfa, 0, 0); rdW(wfa, 0);
    stageT(2);
    step(wfa, xfa, wfb, xfb, 1);
    stageT(3);
#pragma unroll
    for (int t = 0; t < 8; ++t) {
      top(t);
      if (decltype(EARLY)::value) stg(t);
      step(wfb, xfb, wfa, xfa, 2 * t + 2);
      if (!decltype(EARLY)::value) stg(t);
      step(wfa, xfa, wfb, xfb, 2 * t + 3);
    }
#pragma unroll
    for (int i = 0; i < 5; ++i)
#pragma unroll
      for (int j = 0; j < 2; ++j) acc[i][j] = mfma16(wfb[j], xfb[i], acc[i][j]);
  };
  if (wid < 4) {
    mainloop(IC<1>{});
  } else if (wid < 7) {
    mainloop(IC<0>{});
  } else {
    bf16x8 xfa[5], xfb[5];
    const bf16x8 ones = {0x3f80, 0x3f80, 0x3f80, 0x3f80, 0x3f80, 0x3f80, 0x3f80, 0x3f80};
    auto step = [&](bf16x8 (&xc)[5], bf16x8 (&xn)[5], int s) {
#pragma unroll
      for (int i = 0; i < 5; ++i) {
        acc[i][0] = mfma16(xc[i], xc[i], acc[i][0]);
        xn[i] = lds_b128(lds + ((xa0 ^ ((s & 1) * 64)) + ((s >> 1) & 3) * K1_XT + i * 2048));
        acc[i][1] = mfma16(ones, xc[i], acc[i][1]);
        __builtin_amdgcn_sched_barrier(0);
      }
    };
    rdX(xfa, 0, 0);
    stageT(2);
    step(xfa, xfb, 1);
    stageT(3);
#pragma unroll
    for (int t = 0; t < 8; ++t) {
      top(t);
      step(xfb, xfa, 2 * t + 2);
      stg(t);
      step(xfa, xfb, 2 * t + 3);
    }
#pragma unroll
    for (int i = 0; i < 5; ++i) { acc[i][0] = mfma16(xfb[i], xfb[i], acc[i][0]); acc[i][1] = mfma16(ones, xfb[i], acc[i][1]); }
#pragma unroll
    for (int i = 0; i < 5; ++i) {
      const float q = (m & 2) ? ((m & 1) ? acc[i][0][3] : acc[i][0][2]) : ((m & 1) ? acc[i][0][1] : acc[i][0][0]);
      const float mean = acc[i][1][0] * (1.0f / D_);
      const float var = fmaxf(q * (1.0f / D_) - mean * mean, 0.f);
      const float den = rbf(__builtin_amdgcn_sqrtf(rbf(rbf(var) + LN_EPS)));
      if (g == (m >> 2)) ms[i * 16 + m] = (f32x2){rbf(mean), __builtin_amdgcn_rcpf(den)};
    }
  }
  RSTAMP(1);
  WAIT_L0(); BAR();
  RSTAMP(2);
  stageB(4); stageB(5);
  RSTAMP(3);
  if (wid < 7) {
    f32x2 mr[5];
#pragma unroll
    for (int i = 0; i < 5; ++i) mr[i] = ms[i * 16 + m];
    f32x4 cs[2];
#pragma unroll
    for (int j = 0; j < 2; ++j)
#pragma unroll
      for (int r = 0; r < 4; ++r) cs[j][r] = dpp<0x150>(acc[0][j][r]);
    LAS char* qp = lds + m * K1_QS + (wid * 32 + g * 4) * 2;
    const bool ok1 = wid * 32 + 16 + g * 4 < 216;
#pragma unroll
    for (int i = 0; i < 5; ++i) {
      const float nm = -mr[i].x * mr[i].y;
#pragma unroll
      for (int j = 0; j < 2; ++j) {
        const f32x4 v = acc[i][j] * mr[i].y + cs[j] * nm;
        if (j == 0 || ok1) *(LAS u32x2*)(qp + i * 16 * K1_QS + j * 32) = (u32x2){pk2(v[0], v[1]), pk2(v[2], v[3])};
      }
    }
  }
  RSTAMP(4);
  WAIT_L0(); BAR();
  RSTAMP(5);
  {
    const int half = lane & 1, hh = wid & 3, tok = (wid >> 2) * 32 + (lane >> 1);
    const int row = tok + 16;
    const int gpos = t0 + row;
    const LAS unsigned* bp = (const LAS unsigned*)(lds + row * K1_QS + hh * 36 + half * 20);
    unsigned qv[5], kv[8][5], vv[8][5];
#pragma unroll
    for (int d = 0; d < 5; ++d) qv[d] = bp[72 + (d < 4 ? d : (half ? 0 : 4))];
#pragma unroll
    for (int w = 0; w < 8; ++w)
#pragma unroll
      for (int d = 0; d < 5; ++d) kv[w][d] = bp[d - w * (2 * K1_QS / 4)];
#pragma unroll
    for (int w = 0; w < 8; ++w)
#pragma unroll
      for (int d = 0; d < 5; ++d) vv[w][d] = bp[36 + d - w * (2 * K1_QS / 4)];
    __builtin_amdgcn_sched_barrier(0);
    if (half) qv[4] = 0u;
    float lg[8];
#pragma unroll
    for (int w = 0; w < 8; ++w) {
      float s = 0.f;
#pragma unroll
      for (int d = 0; d < 5; ++d) s = dot2bf(qv[d], kv[w][d], s);
      s += dpp<0xB1>(s);
      lg[w] = (gpos - 2 * w >= 0) ? s * (0.23570226039551584f * 1.4426950408889634f) : -1e30f;
    }
    float mx = lg[0];
#pragma unroll
    for (int w = 1; w < 8; ++w) mx = fmaxf(mx, lg[w]);
    float den = 0.f;
#pragma unroll
    for (int w = 0; w < 8; ++w) { lg[w] = __builtin_amdgcn_exp2f(lg[w] - mx); den += lg[w]; }
    const float inv = __builtin_amdgcn_rcpf(den);
    f32x2 ov2[5];
#pragma unroll
    for (int d = 0; d < 5; ++d) ov2[d] = (f32x2){0.f, 0.f};
#pragma unroll
    for (int w = 0; w < 8; ++w) {
      const f32x2 pw = {lg[w], lg[w]};
#pragma unroll
      for (int d = 0; d < 5; ++d) ov2[d] += pw * (f32x2){bflo(vv[w][d]), bfhi(vv[w][d])};
    }
    float ov[10];
#pragma unroll
    for (int d = 0; d < 5; ++d) { ov[2 * d] = ov2[d].x * inv; ov[2 * d + 1] = ov2[d].y * inv; }
    LAS unsigned* op = (LAS unsigned*)(lds + K1_OB + tok * K1_OS + hh * 36 + half * 20);
#pragma unroll
    for (int d = 0; d < 5; ++d) if (d < 4 || !half) op[d] = pk2(ov[2 * d], ov[2 * d + 1]);
  }
  RSTAMP(6);
  asm volatile("s_waitcnt vmcnt(6) lgkmcnt(0)" ::: "memory");
  BAR();
  RSTAMP(7);
  {
    const __amdgpu_buffer_rsrc_t rso = __builtin_amdgcn_make_buffer_rsrc((void*)OB, 0, S_ * D_ * 2, 0x00020000);
#pragma unroll
    for (int i = 0; i < 2; ++i) {
      const int ci = i * 512 + tid;
      if (ci < 576) {
        const int r = ci & 31, q = ci >> 5, hf = q / 9, cc = q - hf * 9;
        const u32x4 v = *(const LAS u32x4*)(lds + K1_OB + (hf * 32 + r) * K1_OS + cc * 16);
        __builtin_amdgcn_raw_buffer_store_b128(v, rso, (((tt * 2 + hf) * 72 + hg * 9 + cc) * 32 + r) * 16, 0, 16);
      }
    }
  }
  const int prow = tid >> 4, sub = tid & 15;
  const size_t gofs = (size_t)(tt * 64 + th * 32 + prow) * D_ + ct * 144;
  u32x4 rv[2];
  rv[0] = *(const u32x4*)(X + gofs + sub * 8);
  rv[1] = *(const u32x4*)(X + gofs + (sub < 2 ? 16 + sub : sub) * 8);
  {
    const int wm = wid & 1, wk = (wid >> 1) & 1;
    const int obase = (tt * 2 + th) * 72 * 32 * 16;
    if (wid < 4) k2_gemm<5, 0>(lds, lane, wm, wk, (const char*)Wo, w2off, w2dst, OB, obase, cnt + tt * 32, tid, wid);
    else k2_gemm<4, 5>(lds, lane, wm, wk, (const char*)Wo, w2off, w2dst, OB, obase, cnt + tt * 32, tid, wid);
  }
  WAIT_L0(); BAR();
  RSTAMP(15);
  {
    const LAS float* rt = (const LAS float*)(lds + K2_RT);
    float xv[2][8];
    float s = 0.f;
#pragma unroll
    for (int c = 0; c < 2; ++c) {
      const bool ok = c == 0 || sub < 2;
      const int ch = ok ? c * 16 + sub : sub;
      const LAS float* p0 = rt + prow * 148 + ch * 8;
      const f32x4 a0 = *(const LAS f32x4*)p0 + *(const LAS f32x4*)(p0 + 32 * 148);
      const f32x4 a1 = *(const LAS f32x4*)(p0 + 4) + *(const LAS f32x4*)(p0 + 32 * 148 + 4);
      const u32x4 y = {pk2(a0[0], a0[1]), pk2(a0[2], a0[3]), pk2(a1[0], a1[1]), pk2(a1[2], a1[3])};
      u32x4 xo;
#pragma unroll
      for (int e = 0; e < 4; ++e) {
        xo[e] = pk2(bflo(rv[c][e]) + bflo(y[e]), bfhi(rv[c][e]) + bfhi(y[e]));
        xv[c][2 * e] = ok ? bflo(xo[e]) : 0.f; xv[c][2 * e + 1] = ok ? bfhi(xo[e]) : 0.f;
      }
      if (ok) *(u32x4*)(Y + gofs + ch * 8) = xo;
#pragma unroll
      for (int e = 0; e < 8; ++e) s += xv[c][e];
    }
    s = sum8(s); s += dpp<0x140>(s);
    const float mean = s * (1.0f / 144.0f);
    float q = 0.f;
#pragma unroll
    for (int c = 0; c < 2; ++c)
#pragma unroll
      for (int e = 0; e < 8; ++e) { const float d = xv[c][e] - mean; q += (c == 0 || sub < 2) ? d * d : 0.f; }
    q = sum8(q); q += dpp<0x140>(q);
    if (sub == 0) stats[(tt * 64 + th * 32 + prow) * 4 + ct] = (f32x2){mean, q};
  }
  RSTAMP(16);
  STAMPEND(1, 1);
  RSTAMP(17);
  RDUMP(18);
}

constexpr int K3_XS = 32768, K3_UOFF = 9216 + 128, K3_WS = K3_UOFF + 9216;
constexpr int K3_WBASE = 2 * K3_XS, K3_AUX = K3_WBASE + 4 * K3_WS;
constexpr int K3_CS = K3_AUX + 8192;
constexpr int K3_DUMMY = K3_CS + 3584;
constexpr int K3_LDS = K3_DUMMY + 4096;

template <int NJ, int J0>
DEVI void k3_body(LAS char* lds, const char* X, const char* wsrc, bf16_t* __restrict__ H, const unsigned (&xoff)[4], const unsigned (&woff)[3],
                  unsigned xdst, unsigned wdst, int lane, int wr, int pm, int pf, const char* Wd, unsigned pfoff) {
  auto stageX = [&](int t) { glds_4(X + t * 128, xoff[0], xoff[1], xoff[2], xoff[3], xdst + (t & 1) * K3_XS); };
  auto stageW = [&](int s) { glds_2q(wsrc + (size_t)s * 32 * F_ * 2, woff[0], woff[1], woff[2], wdst + (s & 3) * K3_WS); };
  const int tk = lane & 31, h = lane >> 5, q4 = (lane >> 2) & 3, p4 = lane & 3, isup = (lane >> 4) & 1;
  const int xa = (wr * 64 + tk) * 128 + ((h ^ ((tk >> 1) & 7)) * 16);
  const int wa = K3_WBASE + isup * K3_UOFF + (h * 8 + q4) * 288 + J0 * 32 + p4 * 8;
  auto rdW = [&](int wo, int kk, int j) -> bf16x8 {
    const LAS char* p = lds + (wa + wo + kk * (16 * 288) + j * 32);
    return lds_tr2(p, p + 4 * 288);
  };
  auto rdX = [&](int xo, int k4, int i) -> bf16x8 {
    return lds_b128(lds + ((xa ^ (k4 * 32)) + xo + i * 4096));
  };
  f32x16 acc[2][NJ];
#pragma unroll
  for (int i = 0; i < 2; ++i)
#pragma unroll
    for (int j = 0; j < NJ; ++j)
#pragma unroll
      for (int r = 0; r < 16; ++r) acc[i][j][r] = 0.f;
  auto correction = [&]() {
    bf16x8 mf[2];
#pragma unroll
    for (int i = 0; i < 2; ++i) {
      const LAS f32x4* sp = (const LAS f32x4*)(lds + K3_AUX + (wr * 64 + i * 32 + tk) * 32);
      const f32x4 s01 = sp[0], s23 = sp[1];
      const float mean = rbf((s01[0] + s01[2] + s23[0] + s23[2]) * 0.25f);
      const float m0 = rbf(-mean), m1 = rbf(-mean - m0), m2_ = (-mean - m0) - m1;
      u32x4 w = {pk2(m0, m0), pk2(m0, m1), pk2(m1, m2_), 0u};
      if (h) w = (u32x4){0u, 0u, 0u, 0u};
      mf[i] = __builtin_bit_cast(bf16x8, w);
    }
#pragma unroll
    for (int j = 0; j < NJ; ++j) {
      const LAS float* cp = (const LAS float*)(lds + K3_CS) + isup * 144 + (J0 + j) * 16 + (lane & 15);
      const float c = (cp[0] + cp[288]) + cp[576];
      const float c0 = rbf(c), c1 = rbf(c - c0), c2 = (c - c0) - c1;
      const u32x4 w = {pk2(c0, c1), pk2(c2, c0), pk2(c1, c0), 0u};
      const bf16x8 cf = __builtin_bit_cast(bf16x8, w);
#pragma unroll
      for (int i = 0; i < 2; ++i) acc[i][j] = __builtin_amdgcn_mfma_f32_32x32x16_bf16(cf, mf[i], acc[i][j], 0, 0, 0);
    }
  };
  WAIT_V(10); BAR();
  correction();
  bf16x8 xfa[2], xfb[2], xfc[2], wf[4];
  WAIT_V(3); BAR();
  STAMP(1);
  xfa[0] = rdX(0, 0, 0); xfa[1] = rdX(0, 0, 1);
  wf[0] = rdW(0, 0, 0); wf[1] = rdW(0, 0, 1);
  stageX(1); stageW(2);

  if (PRIO == 1 && NJ == 5) __builtin_amdgcn_s_setprio(1);
  if (PRIO == 2 && NJ == 4) __builtin_amdgcn_s_setprio(1);
#pragma unroll 1
  for (int t = 0; t < 9; ++t) {
    const int tp = t & 1;
    const int wo = tp * (2 * K3_WS), won = (1 - tp) * (2 * K3_WS);
    const int xo = tp * K3_XS, xon = (1 - tp) * K3_XS;
#pragma unroll
    for (int k4 = 0; k4 < 4; ++k4) {
      if (k4 == 0) {
        if (t < 8) { WAIT_V(7); }
        ABL_B(BAR());
      }
      if (k4 == 2) {
        if (t < 7) { WAIT_V(3); } else if (t == 7) { WAIT_V(0); }
        WAIT_L0();
        ABL_B(BAR());
        if (t == 7) {
          glds_4(Wd, pfoff, pfoff, pfoff, pfoff, ldsa(lds) + K3_DUMMY);
          glds_2(Wd + 3136, pfoff, pfoff, ldsa(lds) + K3_DUMMY);
        }
      }
      __builtin_amdgcn_sched_barrier(0);
#pragma unroll
      for (int j = 0; j < NJ; ++j) {
        const int n = k4 * NJ + j;
        if (j == 0) {
          if (k4 == (NJ == 4 ? 0 : 1) && t < 8) stageW(2 * t + 3);
          if (k4 == (NJ == 4 ? 2 : 3) && t < 7) { stageW(2 * t + 4); stageX(t + 2); }
        }
        {
          const int n2 = n + 2, k42 = n2 / NJ, j2 = n2 % NJ;
          if (k42 < 4) { ABL_L(wf[n2 & 3] = rdW(wo + (k42 >> 1) * K3_WS, k42 & 1, j2);) }
          else { ABL_L(wf[n2 & 3] = rdW(won, 0, j2);) }
        }
        if (j == 1) {
          if (k4 == 0) { ABL_L(xfb[0] = rdX(xo, 1, 0); xfb[1] = rdX(xo, 1, 1);) }
          if (k4 == 1) { ABL_L(xfa[0] = rdX(xo, 2, 0); xfa[1] = rdX(xo, 2, 1);) }
          if (k4 == 3) { ABL_L(xfa[0] = rdX(xon, 0, 0); xfa[1] = rdX(xon, 0, 1);) }
        }
        if (j == 2 && k4 == 1) { ABL_L(xfc[0] = rdX(xo, 3, 0); xfc[1] = rdX(xo, 3, 1);) }
#pragma unroll
        for (int i = 0; i < 2; ++i)
          acc[i][j] = __builtin_amdgcn_mfma_f32_32x32x16_bf16(wf[n & 3], k4 == 1 ? xfb[i] : (k4 == 3 ? xfc[i] : xfa[i]), acc[i][j], 0, 0, 0);
        __builtin_amdgcn_sched_barrier(0);
      }
    }
  }
  STAMP(2);
  float rs[2];
#pragma unroll
  for (int i = 0; i < 2; ++i) {
    const LAS f32x4* sp = (const LAS f32x4*)(lds + K3_AUX + (wr * 64 + i * 32 + tk) * 32);
    const f32x4 s01 = sp[0], s23 = sp[1];
    const float mean = (s01[0] + s01[2] + s23[0] + s23[2]) * 0.25f;
    const float d0 = s01[0] - mean, d1 = s01[2] - mean, d2 = s23[0] - mean, d3 = s23[2] - mean;
    const float m2 = (s01[1] + s01[3]) + (s23[1] + s23[3]) + 144.0f * ((d0 * d0 + d1 * d1) + (d2 * d2 + d3 * d3));
    { const float den = rbf(__builtin_amdgcn_sqrtf(rbf(rbf(m2 * (1.0f / D_)) + LN_EPS))); rs[i] = den * den; }
  }
  const __amdgpu_buffer_rsrc_t rsh = __builtin_amdgcn_make_buffer_rsrc((void*)H, 0, S_ * F_ * 2, 0x00020000);
  const int hofs = (((pf * 9 + J0) * 2 + h) * S_ + pm * 256 + wr * 64 + tk) * 16;
#pragma unroll
  for (int i = 0; i < 2; ++i) {
    const float c1 = -1.4426950408889634f * __builtin_amdgcn_rsqf(rs[i]);
    const f32x2 c1v = {c1, c1}, inv = {rs[i], rs[i]};
#pragma unroll
    for (int j = 0; j < NJ; ++j) {
      u32x4 hw;
#pragma unroll
      for (int r = 0; r < 4; ++r) {
        const f32x2 ag = {acc[i][j][2 * r], acc[i][j][2 * r + 1]}, au = {acc[i][j][8 + 2 * r], acc[i][j][8 + 2 * r + 1]};
        const f32x2 z = ag * c1v;
        const f32x2 e = {__builtin_amdgcn_exp2f(z.x), __builtin_amdgcn_exp2f(z.y)};
        const f32x2 d = e * inv + inv;
        const f32x2 rc = {__builtin_amdgcn_rcpf(d.x), __builtin_amdgcn_rcpf(d.y)};
        const f32x2 hv = (ag * au) * rc;
        hw[r] = pk2(hv.x, hv.y);
      }
      __builtin_amdgcn_raw_buffer_store_b128(hw, rsh, hofs + j * (2 * S_ * 16) + i * 512, 0, HAUX);
    }
  }
  STAMP(3);
}

__global__ __launch_bounds__(512, 2) void k3_gateup(const bf16_t* __restrict__ X, const bf16_t* __restrict__ Wg, const bf16_t* __restrict__ Wu,
                                                     const float* __restrict__ csum, const f32x2* __restrict__ stats, bf16_t* __restrict__ H,
                                                     const bf16_t* __restrict__ Wd) {
  __shared__ __attribute__((aligned(16))) char lds_[K3_LDS];
  LAS char* lds = (LAS char*)lds_;
  const unsigned lbase = ldsa(lds);
  const int tid = threadIdx.x, lane = tid & 63, wid = __builtin_amdgcn_readfirstlane(tid >> 6);
  const int bid = blockIdx.x, xcd = bid & 7, idx = bid >> 3;
  const int pm = (xcd >> 2) * 4 + (idx & 3), pf = (xcd & 3) * 8 + (idx >> 2);
  const int wr = wid & 3, wc = wid >> 2;
  STAMP(0);
  glds16((const char*)(stats + pm * 256 * 4), (unsigned)(tid * 16), lbase + K3_AUX + wid * 1024);
  {
    const int l36 = lane < 36 ? lane : 35;
    const int cw = wid < 6 ? wid : 0;
    if (lane < 36) glds16((const char*)(csum + cw * F_ + pf * 144), (unsigned)(l36 * 16), lbase + K3_CS + cw * 576);
  }
  unsigned xoff[4], woff[3];
#pragma unroll
  for (int i = 0; i < 4; ++i) {
    const int row = (wid * 4 + i) * 8 + (lane >> 3);
    const int c = (lane & 7) ^ ((row >> 1) & 7);
    xoff[i] = (unsigned)(((pm * 256 + row) * D_ + c * 8) * 2 - i * 1024);
  }
  const unsigned xdst = lbase + wid * 4096;
  glds_4((const char*)X, xoff[0], xoff[1], xoff[2], xoff[3], xdst);
#pragma unroll
  for (int i = 0; i < 3; ++i) {
    int ci = i * 64 + lane; if (ci > 143) ci = 143;
    const int kk = ci / 18, cp = ci - kk * 18;
    woff[i] = (unsigned)((((wid & 3) * 8 + kk) * F_ + pf * 144 + cp * 8) * 2 - i * 1024);
  }
  const char* wsrc = (const char*)((wid >> 2) ? Wu : Wg);
  const unsigned wdst = lbase + K3_WBASE + (wid >> 2) * K3_UOFF + (wid & 3) * 2304;
  glds_2q(wsrc, woff[0], woff[1], woff[2], wdst);
  glds_2q(wsrc + 32 * F_ * 2, woff[0], woff[1], woff[2], wdst + K3_WS);
  const unsigned pfoff = (unsigned)((xcd & 3) * (1152 * D_ * 2) + (idx * 8 + wid) * 5184 + lane * 16);
  if (wc == 0) k3_body<5, 0>(lds, (const char*)X, wsrc, H, xoff, woff, xdst, wdst, lane, wr, pm, pf, (const char*)Wd, pfoff);
  else k3_body<4, 5>(lds, (const char*)X, wsrc, H, xoff, woff, xdst, wdst, lane, wr, pm, pf, (const char*)Wd, pfoff);
  STAMPEND(3, 4);
}

constexpr int K4_XB = 16384, K4_STG = K4_XB + 18432, K4_LDS = 4 * K4_STG;

template <int NJ, int J0>
DEVI void k4_body(LAS char* lds, const char* X, const char* W, unsigned xoff, const unsigned (&woff)[3], unsigned sdst, unsigned wdst,
                  int lane, int wm, int split, int tile, float* __restrict__ P, unsigned* __restrict__ cnt) {
  auto stage = [&](int kt, int buf) {
    glds_2(X + (size_t)kt * (8 * S_ * 16), xoff, xoff, sdst + buf * K4_STG);
    glds_2q(W + (size_t)kt * 64 * D_ * 2, woff[0], woff[1], woff[2], wdst + buf * K4_STG);
  };
  const int g = lane >> 4, m = lane & 15, q4 = (lane >> 2) & 3, p4 = lane & 3;
  const int xa = g * 2048 + (wm * 32 + m) * 16;
  const int wa = K4_XB + ((g >> 1) * 16 + (g & 1) * 4 + q4) * 288 + J0 * 32 + p4 * 8;
  f32x4 acc[2][NJ];
#pragma unroll
  for (int i = 0; i < 2; ++i)
#pragma unroll
    for (int j = 0; j < NJ; ++j) acc[i][j] = (f32x4){0, 0, 0, 0};
  bf16x8 xa_[2], wa_[NJ], xb_[2], wb_[NJ];
  auto rd = [&](bf16x8 (&xf)[2], bf16x8 (&wf)[NJ], const LAS char* sb, int ks) {
#pragma unroll
    for (int i = 0; i < 2; ++i) xf[i] = lds_b128(sb + xa + ks * 8192 + i * 256);
#pragma unroll
    for (int j = 0; j < NJ; ++j) wf[j] = lds_tr2(sb + wa + ks * (32 * 288) + j * 32, sb + wa + ks * (32 * 288) + j * 32 + 8 * 288);
  };
  auto step = [&](bf16x8 (&xc)[2], bf16x8 (&wc)[NJ], bf16x8 (&xn)[2], bf16x8 (&wn)[NJ], const LAS char* sb, int ks) {
#pragma unroll
    for (int j = 0; j < NJ; ++j) {
      if (j < 2) xn[j] = lds_b128(sb + xa + ks * 8192 + j * 256);
      wn[j] = lds_tr2(sb + wa + ks * (32 * 288) + j * 32, sb + wa + ks * (32 * 288) + j * 32 + 8 * 288);
#pragma unroll
      for (int i = 0; i < 2; ++i) acc[i][j] = mfma16(wc[j], xc[i], acc[i][j]);
      __builtin_amdgcn_sched_barrier(0);
    }
  };
  WAIT_V(5); BAR();
  STAMPK(4, 1);
  rd(xa_, wa_, lds, 0);
  stage(2, 2);
  step(xa_, wa_, xb_, wb_, lds, 1);
  stage(3, 3);
  auto iter = [&](auto INFL, auto MORE, int t) {
    asm volatile("s_waitcnt vmcnt(%0) lgkmcnt(0)" ::"n"(decltype(INFL)::value * 5) : "memory");
    BAR();
    __builtin_amdgcn_sched_barrier(0);
    const LAS char* sn = lds + ((t + 1) & 3) * K4_STG;
    if (decltype(MORE)::value && NJ == 4) stage(t + 4, t & 3);
    step(xb_, wb_, xa_, wa_, sn, 0);
    if (decltype(MORE)::value && NJ == 5) stage(t + 4, t & 3);
    step(xa_, wa_, xb_, wb_, sn, 1);
  };
#pragma unroll 1
  for (int t = 0; t < 14; ++t) iter(IC<2>{}, IC<1>{}, t);
  iter(IC<2>{}, IC<0>{}, 14);
  iter(IC<1>{}, IC<0>{}, 15);
  iter(IC<0>{}, IC<0>{}, 16);
#pragma unroll
  for (int j = 0; j < NJ; ++j)
#pragma unroll
    for (int i = 0; i < 2; ++i) acc[i][j] = mfma16(wb_[j], xb_[i], acc[i][j]);
  STAMPK(4, 2);
  const __amdgpu_buffer_rsrc_t rs = __builtin_amdgcn_make_buffer_rsrc((void*)P, 0, 64 * 16 * 9 * 1024, 0x00020000);
  if (wm != split) {
    const int sb = ((tile * 4 + wm) * 4 + split) * 9 * 1024 + lane * 16;
#pragma unroll
    for (int j = 0; j < NJ; ++j) {
      const u32x4 v = {pk2(acc[0][j][0], acc[0][j][1]), pk2(acc[0][j][2], acc[0][j][3]), pk2(acc[1][j][0], acc[1][j][1]), pk2(acc[1][j][2], acc[1][j][3])};
      __builtin_amdgcn_raw_buffer_store_b128(v, rs, sb + (J0 + j) * 1024, 0, 16);
    }
    WAIT_V(0);
    if (lane == 0) __hip_atomic_fetch_add(cnt + (tile * 4 + wm) * 32, 1u, __ATOMIC_RELAXED, __HIP_MEMORY_SCOPE_AGENT);
  } else {
    LAS f32x4* own = (LAS f32x4*)(lds + 2 * K4_STG);
#pragma unroll
    for (int i = 0; i < 2; ++i)
#pragma unroll
      for (int j = 0; j < NJ; ++j) own[(i * 9 + J0 + j) * 64 + lane] = acc[i][j];
    if (NJ == 5) {
      unsigned spins = 0;
      while ((unsigned)__builtin_amdgcn_readfirstlane(__hip_atomic_load(cnt + (tile * 4 + split) * 32, __ATOMIC_RELAXED, __HIP_MEMORY_SCOPE_AGENT)) < 6u) {
        __builtin_amdgcn_s_sleep(1);
        if (++spins > (1u << 22)) break;
      }
    }
  }
}

__global__ __launch_bounds__(512, 2) void k4_down(const bf16_t* __restrict__ X, const bf16_t* __restrict__ W, const bf16_t* __restrict__ R,
                                                   bf16_t* __restrict__ Y, unsigned* __restrict__ cnt, float* __restrict__ P) {
  __shared__ __attribute__((aligned(16))) char lds_[K4_LDS];
  LAS char* lds = (LAS char*)lds_;
  const unsigned lbase = ldsa(lds);
  const int tid = threadIdx.x, lane = tid & 63, wid = __builtin_amdgcn_readfirstlane(tid >> 6);
  const int bid = blockIdx.x, xcd = bid & 7, idx = bid >> 3;
  const int split = xcd & 3;
  const int m0 = (xcd >> 2) * 1024 + (idx >> 2) * 128, n0 = (idx & 3) * 144, k0 = split * 1152;
  const int wm = wid & 3, wn = wid >> 2;
  STAMPK(4, 0);
  const unsigned xoff = (unsigned)((((k0 >> 3) + wid) * S_ + m0 + lane) * 16);
  unsigned woff[3];
#pragma unroll
  for (int i = 0; i < 3; ++i) {
    int ci = i * 64 + lane; if (ci > 143) ci = 143;
    const int rr = ci / 18, c = ci - rr * 18;
    woff[i] = (unsigned)(((k0 + wid * 8 + rr) * D_ + n0 + c * 8) * 2 - i * 1024);
  }
  const unsigned sdst = lbase + wid * 2048, wdst = lbase + K4_XB + wid * 2304;
#pragma unroll
  for (int s = 0; s < 2; ++s) {
    glds_2((const char*)X + (size_t)s * (8 * S_ * 16), xoff, xoff, sdst + s * K4_STG);
    glds_2q((const char*)W + (size_t)s * 64 * D_ * 2, woff[0], woff[1], woff[2], wdst + s * K4_STG);
  }
  const int tile = (m0 >> 7) * 4 + (idx & 3);
  if (wn == 0) k4_body<5, 0>(lds, (const char*)X, (const char*)W, xoff, woff, sdst, wdst, lane, wm, split, tile, P, cnt);
  else k4_body<4, 5>(lds, (const char*)X, (const char*)W, xoff, woff, sdst, wdst, lane, wm, split, tile, P, cnt);
  const int row = (tid >> 3) & 31, sub = tid & 7;
  const size_t gofs = (size_t)(m0 + split * 32 + row) * D_ + n0;
  u32x4 rv[3];
#pragma unroll
  for (int c = 0; c < 3; ++c) { const int ch = c * 8 + sub; rv[c] = *(const u32x4*)(R + gofs + (ch < 18 ? ch : 0) * 8); }
  WAIT_L0(); BAR();
  STAMPK(4, 3);
  const int g = lane >> 4, m = lane & 15;
  LAS f32x4* own = (LAS f32x4*)(lds + 2 * K4_STG);
  LAS float* rt = (LAS float*)lds;
  {
    const __amdgpu_buffer_rsrc_t rs = __builtin_amdgcn_make_buffer_rsrc((void*)P, 0, 64 * 16 * 9 * 1024, 0x00020000);
    u32x4 pv[2][3];
#pragma unroll
    for (int n = 0; n < 2; ++n) {
      const int j = wid + 8 * n < 9 ? wid + 8 * n : wid;
#pragma unroll
      for (int sx = 0; sx < 3; ++sx) {
        const int src = sx + (sx >= split);
        pv[n][sx] = __builtin_amdgcn_raw_buffer_load_b128(rs, (((tile * 4 + split) * 4 + src) * 9 + j) * 1024 + lane * 16, 0, 16);
      }
    }
#pragma unroll
    for (int n = 0; n < 2; ++n) {
      const int j = wid + 8 * n;
      if (j < 9) {
#pragma unroll
        for (int i = 0; i < 2; ++i) {
          f32x4 v = own[(i * 9 + j) * 64 + lane];
#pragma unroll
          for (int sx = 0; sx < 3; ++sx)
            v += (f32x4){bflo(pv[n][sx][2 * i]), bfhi(pv[n][sx][2 * i]), bflo(pv[n][sx][2 * i + 1]), bfhi(pv[n][sx][2 * i + 1])};
          *(LAS f32x4*)(rt + (i * 16 + m) * 148 + j * 16 + g * 4) = v;
        }
      }
    }
  }
  WAIT_L0(); BAR();
  if (tid < 256) {
#pragma unroll
    for (int c = 0; c < 3; ++c) {
      const int ch = c * 8 + sub;
      if (ch < 18) {
        const LAS float* p0 = rt + row * 148 + ch * 8;
        const f32x4 a0 = *(const LAS f32x4*)p0, a1 = *(const LAS f32x4*)(p0 + 4);
        *(u32x4*)(Y + gofs + ch * 8) = (u32x4){pk2(bflo(rv[c].x) + rbf(a0[0]), bfhi(rv[c].x) + rbf(a0[1])), pk2(bflo(rv[c].y) + rbf(a0[2]), bfhi(rv[c].y) + rbf(a0[3])),
                                               pk2(bflo(rv[c].z) + rbf(a1[0]), bfhi(rv[c].z) + rbf(a1[1])), pk2(bflo(rv[c].w) + rbf(a1[2]), bfhi(rv[c].w) + rbf(a1[3]))};
      }
    }
  }
  STAMPEND(4, 4);
}

extern "C" __global__ void k9_end() {}

extern "C" void kernel_launch(void* const* d_in, const int* in_sizes, int n_in,
                              void* d_out, int out_size, void* d_ws, size_t ws_size,
                              hipStream_t stream) {
  const bf16_t* x0 = (const bf16_t*)d_in[0];
  const bf16_t* wqkv = (const bf16_t*)d_in[1];
  const bf16_t* wo = (const bf16_t*)d_in[2];
  const bf16_t* wg = (const bf16_t*)d_in[4];
  const bf16_t* wu = (const bf16_t*)d_in[5];
  const bf16_t* wd = (const bf16_t*)d_in[6];
  bf16_t* out = (bf16_t*)d_out;
  char* ws = (char*)d_ws;
  constexpr size_t XB = (size_t)S_ * D_ * 2, HB = (size_t)S_ * F_ * 2, PB = (size_t)4 * S_ * D_ * 4;
  size_t off = 0;
  float* csum = (float*)(ws + off); off += 131072;
  unsigned* cnt = (unsigned*)(ws + off); off += NCNT * 128;
  char* wp = ws + off; off += (size_t)144 * K1_WS;
  const bf16_t* xin = x0;
  k0_prep<<<144 + 144 + 41, 256, 0, stream>>>(wqkv, wp, cnt, x0, wo);
  char* o = ws + off; off += XB;
  bf16_t* x1 = (bf16_t*)(ws + off); off += XB;
  f32x2* stats = (f32x2*)(ws + off); off += 65536;
  bf16_t* xm = (bf16_t*)(ws + off); off += XB;
  bf16_t* h = (bf16_t*)(ws + off); off += HB;
  float* P = (float*)(ws + off); off += PB;
  for (int l = 0; l < 2; ++l) {
    bf16_t* x2 = (l == 1) ? out : xm;
    k1_qkv_attn<<<256, l == 0 ? 576 : 512, 0, stream>>>(xin, wp, o, wo, x1, stats, cnt + (l * 512 + 256) * 32, wg, wu, csum);
    k3_gateup<<<256, 512, 0, stream>>>(x1, wg, wu, csum, stats, h, wd);
    k4_down<<<256, 512, 0, stream>>>(h, wd, x1, x2, cnt + (l * 512) * 32, P);
    xin = x2;
  }
}
```

```cpp
#include <hip/hip_runtime.h>
#include <hip/hip_bf16.h>
#include <stdint.h>

typedef unsigned short bf16_t;
typedef short bf16x8 __attribute__((ext_vector_type(8)));
typedef short bf16x4 __attribute__((ext_vector_type(4)));
typedef float f32x4 __attribute__((ext_vector_type(4)));
typedef float f32x2 __attribute__((ext_vector_type(2)));
typedef float f32x16 __attribute__((ext_vector_type(16)));
typedef unsigned u32x4 __attribute__((ext_vector_type(4)));
typedef unsigned u32x2 __attribute__((ext_vector_type(2)));
#define LAS __attribute__((address_space(3)))
#define DEVI __device__ __forceinline__

constexpr int S_ = 2048, D_ = 576, F_ = 4608, NH_ = 32, DH_ = 18;
constexpr float LN_EPS = 1e-5f;

DEVI float bf2f(unsigned b) { return __uint_as_float(b << 16); }
DEVI float bflo(unsigned w) { return __uint_as_float(w << 16); }
DEVI float bfhi(unsigned w) { return __uint_as_float(w & 0xffff0000u); }
DEVI unsigned pk2(float lo, float hi) {
  typedef __bf16 bf2v __attribute__((ext_vector_type(2)));
  bf2v v; v.x = (__bf16)lo; v.y = (__bf16)hi;
  return __builtin_bit_cast(unsigned, v);
}
template <int CTRL> DEVI float dpp(float x) { return __builtin_bit_cast(float, __builtin_amdgcn_update_dpp(0, __builtin_bit_cast(int, x), CTRL, 0xf, 0xf, true)); }
DEVI float sum8(float x) {
  x += dpp<0xB1>(x); x += dpp<0x4E>(x); x += dpp<0x141>(x); return x;
}
DEVI float rbf(float x) { return (float)((__bf16)x); }

#define TOT0()
#define TOT1()
#define RSTAMP(i)
#define RDUMP(n)
#define STAMPW(kid, i, cond)
#define STAMPK(kid, i)
#define STAMPEND(kid, i)
#define STAMP(i) STAMPK(3, i)
#define ABL_G(x) x
#define ABL_B(x) x
#define ABL_L(...) __VA_ARGS__
#define LNMIMIC 1
#define TAILPRIO 0
#define HAUX 16
#define GV 3
#define PRIO 0
template <int N> struct IC { static constexpr int value = N; };
template <int A, int B, class F> __device__ __forceinline__ void static_for(F&& f) {
  if constexpr (A < B) { f(IC<A>{}); static_for<A + 1, B>(f); }
}
#define WAIT_V(n) asm volatile("s_waitcnt vmcnt(" #n ")" ::: "memory")
#define WAIT_L0() asm volatile("s_waitcnt lgkmcnt(0)" ::: "memory")
#define BAR() do { asm volatile("" ::: "memory"); __builtin_amdgcn_s_barrier(); asm volatile("" ::: "memory"); } while (0)

DEVI void glds16(const char* sbase, unsigned voff, unsigned ldsaddr) {
  asm volatile("s_mov_b32 m0, %2\n\ts_nop 0\n\tglobal_load_lds_dwordx4 %0, %1" ::"v"(voff), "s"(sbase), "s"(ldsaddr) : "memory");
}
DEVI void glds_1(const char* sb, unsigned v0, unsigned la) { glds16(sb, v0, la); }
DEVI void glds_2(const char* sb, unsigned v0, unsigned v1, unsigned la) {
  asm volatile("s_mov_b32 m0, %3\n\ts_nop 0\n\tglobal_load_lds_dwordx4 %0, %2\n\tglobal_load_lds_dwordx4 %1, %2 offset:1024"
               ::"v"(v0), "v"(v1), "s"(sb), "s"(la) : "memory");
}
DEVI void glds_4(const char* sb, unsigned v0, unsigned v1, unsigned v2, unsigned v3, unsigned la) {
  asm volatile("s_mov_b32 m0, %5\n\ts_nop 0\n\tglobal_load_lds_dwordx4 %0, %4\n\tglobal_load_lds_dwordx4 %1, %4 offset:1024\n\t"
               "global_load_lds_dwordx4 %2, %4 offset:2048\n\tglobal_load_lds_dwordx4 %3, %4 offset:3072"
               ::"v"(v0), "v"(v1), "v"(v2), "v"(v3), "s"(sb), "s"(la) : "memory");
}
DEVI void glds_2q(const char* sb, unsigned v0, unsigned v1, unsigned v2, unsigned la) {
  asm volatile("s_mov_b32 m0, %4\n\ts_nop 0\n\tglobal_load_lds_dwordx4 %0, %3\n\tglobal_load_lds_dwordx4 %1, %3 offset:1024\n\t"
               "s_mov_b64 exec, 0xffff\n\tglobal_load_lds_dwordx4 %2, %3 offset:2048\n\ts_mov_b64 exec, -1"
               ::"v"(v0), "v"(v1), "v"(v2), "s"(sb), "s"(la) : "memory");
}
DEVI void glds_2m(const char* sb, unsigned v0, unsigned v1, unsigned la, unsigned long long m0_, unsigned long long m1_) {
  asm volatile("s_mov_b32 m0, %3\n\ts_mov_b64 exec, %4\n\tglobal_load_lds_dwordx4 %0, %2\n\t"
               "s_mov_b64 exec, %5\n\tglobal_load_lds_dwordx4 %1, %2 offset:1024\n\ts_mov_b64 exec, -1"
               ::"v"(v0), "v"(v1), "s"(sb), "s"(la), "s"(m0_), "s"(m1_) : "memory");
}
DEVI unsigned ldsa(const LAS char* p) { return (unsigned)(size_t)p; }
DEVI bf16x8 lds_b128(const LAS char* p) { return *(const LAS bf16x8*)p; }
DEVI bf16x8 lds_tr2(const LAS char* p0, const LAS char* p1) {
  bf16x4 a = __builtin_amdgcn_ds_read_tr16_b64_v4i16((LAS bf16x4*)p0);
  bf16x4 b = __builtin_amdgcn_ds_read_tr16_b64_v4i16((LAS bf16x4*)p1);
  return __builtin_shufflevector(a, b, 0, 1, 2, 3, 4, 5, 6, 7);
}
DEVI f32x4 mfma16(bf16x8 a, bf16x8 b, f32x4 c) { return __builtin_amdgcn_mfma_f32_16x16x32_bf16(a, b, c, 0, 0, 0); }

constexpr int K1_WR = 16 * 448 + 32;
constexpr int K1_WS = 14464;
constexpr int NCNT = 1024;

DEVI void row_stats(const u32x4& v0, const u32x4& v1, int sub, f32x2* __restrict__ dst) {
  float xv[16];
#pragma unroll
  for (int e = 0; e < 4; ++e) {
    xv[2 * e] = bflo(v0[e]); xv[2 * e + 1] = bfhi(v0[e]);
    xv[8 + 2 * e] = sub < 2 ? bflo(v1[e]) : 0.f; xv[8 + 2 * e + 1] = sub < 2 ? bfhi(v1[e]) : 0.f;
  }
  float s = 0.f;
#pragma unroll
  for (int e = 0; e < 16; ++e) s += xv[e];
  s = sum8(s); s += dpp<0x140>(s);
  const float mean = s * (1.0f / 144.0f);
  float q = 0.f;
#pragma unroll
  for (int e = 0; e < 16; ++e) { const float d = xv[e] - mean; q += (e < 8 || sub < 2) ? d * d : 0.f; }
  q = sum8(q); q += dpp<0x140>(q);
  if (sub == 0) *dst = (f32x2){mean, q};
}

__global__ __launch_bounds__(256) void k0_prep(const bf16_t* __restrict__ Wqkv, char* __restrict__ Wp, unsigned* __restrict__ cnt,
                                                const bf16_t* __restrict__ X, const bf16_t* __restrict__ Wo, f32x2* __restrict__ xstats) {
  STAMPK(0, 0);
  if (blockIdx.x < 8) TOT0();
  const int b = blockIdx.x;
  {
    unsigned long long pc0, pc1;
    asm volatile("s_getpc_b64 %0" : "=s"(pc0));
    asm volatile("s_getpc_b64 s[40:41]\n\ts_add_u32 s40, s40, k9_end@rel32@lo+4\n\ts_addc_u32 s41, s41, k9_end@rel32@hi+12\n\ts_mov_b64 %0, s[40:41]"
                 : "=s"(pc1) : : "s40", "s41", "scc");
    const unsigned long long a = (pc0 & ~15ull) + (unsigned long long)(b >> 3) * 4096 + threadIdx.x * 16;
    if (pc1 > pc0 && a + 16 <= pc1) { const u32x4 v = *(const u32x4*)a; asm volatile("" ::"v"(v)); }
  }
  if (b < 144) {
    const int u = b, hg = u / 18, sl = u - hg * 18;
    char* dst = Wp + (size_t)u * K1_WS;
    u32x4 v[4];
#pragma unroll
    for (int i = 0; i < 4; ++i) {
      const int ci = i * 256 + threadIdx.x;
      const int reg = ci >= 450, cj = ci - reg * 450;
      const int rr = cj / 28;
      int ch = cj - rr * 28; if (ch > 26) ch = 26;
      const int seg = ch / 9, cc = ch - seg * 9;
      const int gcol = (seg == 2 ? 0 : (seg + 1) * D_) + hg * 72 + cc * 8;
      const int rc = rr > 15 ? 15 : rr;
      const int k = sl * 32 + (rc >> 3) * 16 + reg * 8 + (rc & 7);
      v[i] = *(const u32x4*)(Wqkv + (size_t)k * (3 * D_) + gcol);
    }
    if (u < NCNT / 256) cnt[(u * 256 + threadIdx.x) * 32] = 0u;
#pragma unroll
    for (int i = 0; i < 4; ++i) {
      const int ci = i * 256 + threadIdx.x;
      if (ci < 904) *(u32x4*)(dst + ci * 16) = v[i];
    }
  } else if (b < 144 + 512) {
    const int u = (b - 144) * 16 + (threadIdx.x >> 4), sub = threadIdx.x & 15;
    const bf16_t* xr = X + (size_t)(u >> 2) * D_ + (u & 3) * 144;
    const u32x4 v0 = *(const u32x4*)(xr + sub * 8), v1 = *(const u32x4*)(xr + (sub < 2 ? 16 + sub : sub) * 8);
    row_stats(v0, v1, sub, xstats + u);
  } else {
    const int u = b - 656;
    const char* src = (const char*)Wo + (size_t)u * 16384;
    const int lim = D_ * D_ * 2 - u * 16384 < 16384 ? D_ * D_ * 2 - u * 16384 : 16384;
#pragma unroll
    for (int i = 0; i < 4; ++i) {
      const int o = (i * 256 + threadIdx.x) * 16;
      if (o < lim) { const u32x4 v = *(const u32x4*)(src + o); asm volatile("" ::"v"(v)); }
    }
  }
  STAMPEND(0, 1);
}

constexpr int K1_XT = 80 * 128;
constexpr int K1_WBASE = 4 * K1_XT;
constexpr int K1_AUX = 156928;
constexpr int K1_LDS = K1_AUX + 768;
constexpr int K1_QS = 440;
constexpr int K1_OB = 35200, K1_OS = 160;
constexpr int K2_WR = 9216 + 128, K2_WS = K2_WR + 9216, K2_WBASE = 45568;

DEVI float dot2bf(unsigned a, unsigned b, float c) {
  typedef __bf16 bf2v __attribute__((ext_vector_type(2)));
  return __builtin_amdgcn_fdot2_f32_bf16(__builtin_bit_cast(bf2v, a), __builtin_bit_cast(bf2v, b), c, false);
}

constexpr int K2_RT = 44032;

template <int NJ, int J0>
DEVI void k2_gemm(LAS char* lds, int lane, int wm, int wk, const char* Wo, const unsigned (&woff)[3], unsigned wdst,
                  const char* OB, int obase, unsigned* __restrict__ cnt, int tid, int wid) {
  const int g = lane >> 4, m = lane & 15, q4 = (lane >> 2) & 3, p4 = lane & 3;
  const int wa = K2_WBASE + (g & 1) * K2_WR + ((wk * 2 + (g >> 1)) * 8 + q4) * 288 + J0 * 32 + p4 * 8;
  auto rdW = [&](bf16x8 (&wf)[NJ], int slot) {
#pragma unroll
    for (int j = 0; j < NJ; ++j) wf[j] = lds_tr2(lds + wa + slot * K2_WS + j * 32, lds + wa + slot * K2_WS + j * 32 + 4 * 288);
  };
  bf16x8 w[6][NJ];
  rdW(w[0], 2); rdW(w[1], 3); rdW(w[2], 4); rdW(w[3], 5);
  RSTAMP(8);
  WAIT_V(0); WAIT_L0();
  RSTAMP(9);
  BAR();
  if (tid == 0) __hip_atomic_fetch_add(cnt, 1u, __ATOMIC_RELAXED, __HIP_MEMORY_SCOPE_AGENT);
  RSTAMP(10);
#pragma unroll
  for (int t = 6; t < 9; ++t) glds_2q(Wo + (size_t)t * 64 * D_ * 2, woff[0], woff[1], woff[2], wdst + (t - 4) * K2_WS);
  rdW(w[4], 0); rdW(w[5], 1);
  RSTAMP(11);
  if (wid == 0) {
    unsigned spins = 0;
    while ((unsigned)__builtin_amdgcn_readfirstlane(__hip_atomic_load(cnt, __ATOMIC_RELAXED, __HIP_MEMORY_SCOPE_AGENT)) < 8u) {
      __builtin_amdgcn_s_sleep(1);
      if (++spins > (1u << 22)) break;
    }
  }
  WAIT_V(0); WAIT_L0(); BAR();
  RSTAMP(12);
  const __amdgpu_buffer_rsrc_t rso = __builtin_amdgcn_make_buffer_rsrc((void*)OB, 0, S_ * D_ * 2, 0x00020000);
  u32x4 xf[9];
#pragma unroll
  for (int t = 0; t < 9; ++t) xf[t] = __builtin_amdgcn_raw_buffer_load_b128(rso, obase + (wk * 4 + g) * 512 + (wm * 16 + m) * 16 + t * 4096, 0, 16);
  bf16x8 w6[NJ], w7[NJ], w8[NJ];
  rdW(w6, 2); rdW(w7, 3); rdW(w8, 4);
  RSTAMP(13);
  f32x4 acc[NJ];
#pragma unroll
  for (int j = 0; j < NJ; ++j) acc[j] = (f32x4){0, 0, 0, 0};
#pragma unroll
  for (int t = 0; t < 6; ++t)
#pragma unroll
    for (int j = 0; j < NJ; ++j) acc[j] = mfma16(w[t][j], __builtin_bit_cast(bf16x8, xf[t]), acc[j]);
#pragma unroll
  for (int j = 0; j < NJ; ++j) acc[j] = mfma16(w6[j], __builtin_bit_cast(bf16x8, xf[6]), acc[j]);
#pragma unroll
  for (int j = 0; j < NJ; ++j) acc[j] = mfma16(w7[j], __builtin_bit_cast(bf16x8, xf[7]), acc[j]);
#pragma unroll
  for (int j = 0; j < NJ; ++j) acc[j] = mfma16(w8[j], __builtin_bit_cast(bf16x8, xf[8]), acc[j]);
  RSTAMP(14);
  LAS float* rt = (LAS float*)(lds + K2_RT);
#pragma unroll
  for (int j = 0; j < NJ; ++j) *(LAS f32x4*)(rt + (wk * 32 + wm * 16 + m) * 148 + (J0 + j) * 16 + g * 4) = acc[j];
}

__global__ __launch_bounds__(576) void k1_qkv_attn(const bf16_t* __restrict__ X, const char* __restrict__ Wp, char* __restrict__ OB,
                                                       const bf16_t* __restrict__ Wo, bf16_t* __restrict__ Y, f32x2* __restrict__ stats,
                                                       unsigned* __restrict__ cnt, const bf16_t* __restrict__ Wg, const bf16_t* __restrict__ Wu,
                                                       float* __restrict__ csum, const f32x2* __restrict__ xstats) {
  __shared__ __attribute__((aligned(16))) char lds_[K1_LDS];
  LAS char* lds = (LAS char*)lds_;
  const unsigned lbase = ldsa(lds);
  const int tid = threadIdx.x, lane = tid & 63, wid = __builtin_amdgcn_readfirstlane(tid >> 6);
  const int bid = blockIdx.x, xcd = bid & 7, idx = bid >> 3;
  if (wid == 8) {
    if (bid >= 216) return;
    const int kq = bid / 72, cb = bid - kq * 72;
    const bf16_t* Wc = (cb < 36 ? Wg : Wu) + (size_t)(kq * 192 + (lane >> 4)) * F_ + (cb % 36) * 128 + (lane & 15) * 8;
    float c8[8] = {0, 0, 0, 0, 0, 0, 0, 0};
    u32x4 cv[4][6];
#pragma unroll
    for (int st = 0; st < 11; ++st) {
      if (st < 8) {
#pragma unroll
        for (int i = 0; i < 6; ++i) cv[st & 3][i] = *(const u32x4*)(Wc + (size_t)((st * 6 + i) * 4) * F_);
      }
      if (st >= 3) {
#pragma unroll
        for (int i = 0; i < 6; ++i)
#pragma unroll
          for (int e = 0; e < 4; ++e) { c8[2 * e] += bflo(cv[(st - 3) & 3][i][e]); c8[2 * e + 1] += bfhi(cv[(st - 3) & 3][i][e]); }
#pragma unroll
        for (int e = 0; e < 8; ++e) asm volatile("" : "+v"(c8[e]));
      }
      __builtin_amdgcn_sched_barrier(0);
      if (st < 10) BAR();
      __builtin_amdgcn_sched_barrier(0);
    }
#pragma unroll
    for (int e = 0; e < 8; ++e) { float t = c8[e]; t += __shfl_xor(t, 16); t += __shfl_xor(t, 32); c8[e] = t; }
    if (lane < 16) {
      float* cp = csum + kq * 2 * F_ + (cb < 36 ? 0 : F_) + (cb % 36) * 128 + lane * 8;
      *(f32x4*)cp = (f32x4){c8[0], c8[1], c8[2], c8[3]}; *(f32x4*)(cp + 4) = (f32x4){c8[4], c8[5], c8[6], c8[7]};
    }
    return;
  }
  const int tt = (xcd >> 1) * 8 + (idx & 7), hg = (xcd & 1) * 4 + (idx >> 3);
  const int t0 = tt * 64 - 16;
  STAMPK(1, 0);

  unsigned xoff[2], woff[2];
#pragma unroll
  for (int i = 0; i < 2; ++i) {
    int ci = i * 64 + lane; if (ci > 79) ci = 79;
    ci += wid * 80;
    const int row = ci >> 3, c = (ci & 7) ^ ((row >> 1) & 7);
    int grow = t0 + row; if (grow < 0) grow = 0;
    xoff[i] = (unsigned)((grow * D_ + c * 8) * 2 + 1024 - i * 1024);
  }
  woff[0] = (unsigned)((wid * 113 + lane) * 16); woff[1] = woff[0];
  const unsigned wdst = lbase + K1_WBASE + wid * 1808;
  const unsigned long long xmask = wid == 0 ? ~0xffull : ~0ull;
  auto stageX = [&](int kt) {
    glds_2m((const char*)X + kt * 128 - 1024, xoff[0], xoff[1], lbase + (kt & 3) * K1_XT + wid * 1280, xmask, 0xffffull);
  };
  auto stageW = [&](int s) {
    glds_2m(Wp + (size_t)(hg * 18 + s) * K1_WS, woff[0], woff[1], wdst + (s & 7) * K1_WS, ~0ull, 0x1ffffffffffffull);
  };
  auto stageT = [&](int t) { stageW(2 * t); stageX(t); stageW(2 * t + 1); };
  f32x4 sa[2], sb[2];
#pragma unroll
  for (int i = 0; i < 2; ++i) {
    int gr = t0 + i * 64 + (i ? (lane & 15) : lane); if (gr < 0) gr = 0;
    asm volatile("global_load_dwordx4 %0, %2, %3\n\tglobal_load_dwordx4 %1, %2, %3 offset:16" : "=&v"(sa[i]), "=&v"(sb[i]) : "v"(gr * 32), "s"(xstats) : "memory");
  }
  stageT(0); stageT(1);
  if (tid >= 64 && tid < 64 + 32) {
    const int c = tid - 64;
    *(LAS u32x4*)(lds + (c >> 3) * K1_XT + (c & 7) * 16) = (u32x4){0x3f803f80u, 0x3f803f80u, 0x3f803f80u, 0x3f803f80u};
  }
  const int th = hg >> 2, ct = hg & 3;
  unsigned w2off[3];
#pragma unroll
  for (int i = 0; i < 3; ++i) {
    int ci = i * 64 + lane; if (ci > 143) ci = 143;
    const int rr = ci / 18, c = ci - rr * 18;
    w2off[i] = (unsigned)(((wid * 8 + rr) * D_ + ct * 144 + c * 8) * 2 - i * 1024);
  }
  const unsigned w2dst = lbase + K2_WBASE + (wid & 1) * K2_WR + (wid >> 1) * 2304;
  auto stageB = [&](int kt) { glds_2q((const char*)Wo + (size_t)kt * 64 * D_ * 2, w2off[0], w2off[1], w2off[2], w2dst + ((kt + 2) % 6) * K2_WS); };

  const int g = lane >> 4, m = lane & 15, q4 = (lane >> 2) & 3, p4 = lane & 3;
  const int xa0 = m * 128 + ((g ^ (m >> 1)) * 16);
  const int wa0 = K1_WBASE + (g & 1) * K1_WR + ((g >> 1) * 8 + q4) * 448 + (wid < 7 ? wid : 0) * 64 + p4 * 8;
  f32x4 acc[5][2];
#pragma unroll
  for (int i = 0; i < 5; ++i) { acc[i][0] = (f32x4){0, 0, 0, 0}; acc[i][1] = (f32x4){0, 0, 0, 0}; }
  LAS f32x2* ms = (LAS f32x2*)(lds + K1_AUX);
  auto top = [&](int t) {
    __builtin_amdgcn_sched_barrier(0);
    if (t < 6) { asm volatile("s_waitcnt vmcnt(12) lgkmcnt(0)" ::: "memory"); } else { asm volatile("s_waitcnt vmcnt(6) lgkmcnt(0)" ::: "memory"); }
    BAR();
    __builtin_amdgcn_sched_barrier(0);
  };
  auto stg = [&](int t) {
    if (t < 5) stageT(t + 4);
    if (t == 6) { stageB(0); stageB(1); }
    if (t == 7) { stageB(2); stageB(3); }
  };
  WAIT_V(6); WAIT_L0(); BAR();
  RSTAMP(0);
  auto rdX = [&](bf16x8 (&xf)[5], int kt, int ks) {
#pragma unroll
    for (int i = 0; i < 5; ++i) xf[i] = lds_b128(lds + ((xa0 ^ (ks * 64)) + (kt & 3) * K1_XT + i * 2048));
  };
  auto mainloop = [&](auto EARLY) {
    bf16x8 xfa[5], xfb[5], wfa[2], wfb[2];
    auto rdW = [&](bf16x8 (&wf)[2], int s) {
#pragma unroll
      for (int j = 0; j < 2; ++j) wf[j] = lds_tr2(lds + (wa0 + (s & 7) * K1_WS + j * 32), lds + (wa0 + (s & 7) * K1_WS + j * 32 + 4 * 448));
    };
    auto step = [&](bf16x8 (&wc)[2], bf16x8 (&xc)[5], bf16x8 (&wn)[2], bf16x8 (&xn)[5], int s) {
#pragma unroll
      for (int i = 0; i < 5; ++i) {
        if (i < 2) wn[i] = lds_tr2(lds + (wa0 + (s & 7) * K1_WS + i * 32), lds + (wa0 + (s & 7) * K1_WS + i * 32 + 4 * 448));
        acc[i][0] = mfma16(wc[0], xc[i], acc[i][0]);
        __builtin_amdgcn_sched_barrier(0);
        xn[i] = lds_b128(lds + ((xa0 ^ ((s & 1) * 64)) + ((s >> 1) & 3) * K1_XT + i * 2048));
        acc[i][1] = mfma16(wc[1], xc[i], acc[i][1]);
        __builtin_amdgcn_sched_barrier(0);
      }
    };
    rdX(xfa, 0, 0); rdW(wfa, 0);
    stageT(2);
    step(wfa, xfa, wfb, xfb, 1);
    stageT(3);
#pragma unroll
    for (int t = 0; t < 8; ++t) {
      top(t);
      if (decltype(EARLY)::value) stg(t);
      step(wfb, xfb, wfa, xfa, 2 * t + 2);
      if (!decltype(EARLY)::value) stg(t);
      step(wfa, xfa, wfb, xfb, 2 * t + 3);
    }
#pragma unroll
    for (int i = 0; i < 5; ++i)
#pragma unroll
      for (int j = 0; j < 2; ++j) acc[i][j] = mfma16(wfb[j], xfb[i], acc[i][j]);
  };
  if (wid < 4) {
    mainloop(IC<1>{});
  } else if (wid < 7) {
    mainloop(IC<0>{});
  } else {
    stageT(2); stageT(3);
    asm volatile("" : "+v"(sa[0]), "+v"(sa[1]), "+v"(sb[0]), "+v"(sb[1]));
#pragma unroll
    for (int i = 0; i < 2; ++i) {
      const float mean = (sa[i][0] + sa[i][2] + sb[i][0] + sb[i][2]) * 0.25f;
      const float d0 = sa[i][0] - mean, d1 = sa[i][2] - mean, d2 = sb[i][0] - mean, d3 = sb[i][2] - mean;
      const float var = ((sa[i][1] + sa[i][3]) + (sb[i][1] + sb[i][3]) + 144.0f * ((d0 * d0 + d1 * d1) + (d2 * d2 + d3 * d3))) * (1.0f / D_);
      const float den = rbf(__builtin_amdgcn_sqrtf(rbf(rbf(var) + LN_EPS)));
      if (i == 0 || lane < 16) ms[i * 64 + lane] = (f32x2){rbf(mean), __builtin_amdgcn_rcpf(den)};
    }
#pragma unroll
    for (int t = 0; t < 8; ++t) { top(t); stg(t); }
  }
  RSTAMP(1);
  WAIT_L0(); BAR();
  RSTAMP(2);
  stageB(4); stageB(5);
  RSTAMP(3);
  if (wid < 7) {
    f32x2 mr[5];
#pragma unroll
    for (int i = 0; i < 5; ++i) mr[i] = ms[i * 16 + m];
    f32x4 cs[2];
#pragma unroll
    for (int j = 0; j < 2; ++j)
#pragma unroll
      for (int r = 0; r < 4; ++r) cs[j][r] = dpp<0x150>(acc[0][j][r]);
    LAS char* qp = lds + m * K1_QS + (wid * 32 + g * 4) * 2;
    const bool ok1 = wid * 32 + 16 + g * 4 < 216;
#pragma unroll
    for (int i = 0; i < 5; ++i) {
      const float nm = -mr[i].x * mr[i].y;
#pragma unroll
      for (int j = 0; j < 2; ++j) {
        const f32x4 v = acc[i][j] * mr[i].y + cs[j] * nm;
        if (j == 0 || ok1) *(LAS u32x2*)(qp + i * 16 * K1_QS + j * 32) = (u32x2){pk2(v[0], v[1]), pk2(v[2], v[3])};
      }
    }
  }
  RSTAMP(4);
  WAIT_L0(); BAR();
  RSTAMP(5);
  {
    const int half = lane & 1, hh = wid & 3, tok = (wid >> 2) * 32 + (lane >> 1);
    const int row = tok + 16;
    const int gpos = t0 + row;
    const LAS unsigned* bp = (const LAS unsigned*)(lds + row * K1_QS + hh * 36 + half * 20);
    unsigned qv[5], kv[8][5], vv[8][5];
#pragma unroll
    for (int d = 0; d < 5; ++d) qv[d] = bp[72 + (d < 4 ? d : (half ? 0 : 4))];
#pragma unroll
    for (int w = 0; w < 8; ++w)
#pragma unroll
      for (int d = 0; d < 5; ++d) kv[w][d] = bp[d - w * (2 * K1_QS / 4)];
#pragma unroll
    for (int w = 0; w < 8; ++w)
#pragma unroll
      for (int d = 0; d < 5; ++d) vv[w][d] = bp[36 + d - w * (2 * K1_QS / 4)];
    __builtin_amdgcn_sched_barrier(0);
    if (half) qv[4] = 0u;
    float lg[8];
#pragma unroll
    for (int w = 0; w < 8; ++w) {
      float s = 0.f;
#pragma unroll
      for (int d = 0; d < 5; ++d) s = dot2bf(qv[d], kv[w][d], s);
      s += dpp<0xB1>(s);
      lg[w] = (gpos - 2 * w >= 0) ? s * (0.23570226039551584f * 1.4426950408889634f) : -1e30f;
    }
    float mx = lg[0];
#pragma unroll
    for (int w = 1; w < 8; ++w) mx = fmaxf(mx, lg[w]);
    float den = 0.f;
#pragma unroll
    for (int w = 0; w < 8; ++w) { lg[w] = __builtin_amdgcn_exp2f(lg[w] - mx); den += lg[w]; }
    const float inv = __builtin_amdgcn_rcpf(den);
    f32x2 ov2[5];
#pragma unroll
    for (int d = 0; d < 5; ++d) ov2[d] = (f32x2){0.f, 0.f};
#pragma unroll
    for (int w = 0; w < 8; ++w) {
      const f32x2 pw = {lg[w], lg[w]};
#pragma unroll
      for (int d = 0; d < 5; ++d) ov2[d] += pw * (f32x2){bflo(vv[w][d]), bfhi(vv[w][d])};
    }
    float ov[10];
#pragma unroll
    for (int d = 0; d < 5; ++d) { ov[2 * d] = ov2[d].x * inv; ov[2 * d + 1] = ov2[d].y * inv; }
    LAS unsigned* op = (LAS unsigned*)(lds + K1_OB + tok * K1_OS + hh * 36 + half * 20);
#pragma unroll
    for (int d = 0; d < 5; ++d) if (d < 4 || !half) op[d] = pk2(ov[2 * d], ov[2 * d + 1]);
  }
  RSTAMP(6);
  asm volatile("s_waitcnt vmcnt(6) lgkmcnt(0)" ::: "memory");
  BAR();
  RSTAMP(7);
  {
    const __amdgpu_buffer_rsrc_t rso = __builtin_amdgcn_make_buffer_rsrc((void*)OB, 0, S_ * D_ * 2, 0x00020000);
#pragma unroll
    for (int i = 0; i < 2; ++i) {
      const int ci = i * 512 + tid;
      if (ci < 576) {
        const int r = ci & 31, q = ci >> 5, hf = q / 9, cc = q - hf * 9;
        const u32x4 v = *(const LAS u32x4*)(lds + K1_OB + (hf * 32 + r) * K1_OS + cc * 16);
        __builtin_amdgcn_raw_buffer_store_b128(v, rso, (((tt * 2 + hf) * 72 + hg * 9 + cc) * 32 + r) * 16, 0, 16);
      }
    }
  }
  const int prow = tid >> 4, sub = tid & 15;
  const size_t gofs = (size_t)(tt * 64 + th * 32 + prow) * D_ + ct * 144;
  u32x4 rv[2];
  rv[0] = *(const u32x4*)(X + gofs + sub * 8);
  rv[1] = *(const u32x4*)(X + gofs + (sub < 2 ? 16 + sub : sub) * 8);
  {
    const int wm = wid & 1, wk = (wid >> 1) & 1;
    const int obase = (tt * 2 + th) * 72 * 32 * 16;
    if (wid < 4) k2_gemm<5, 0>(lds, lane, wm, wk, (const char*)Wo, w2off, w2dst, OB, obase, cnt + tt * 32, tid, wid);
    else k2_gemm<4, 5>(lds, lane, wm, wk, (const char*)Wo, w2off, w2dst, OB, obase, cnt + tt * 32, tid, wid);
  }
  WAIT_L0(); BAR();
  RSTAMP(15);
  {
    const LAS float* rt = (const LAS float*)(lds + K2_RT);
    u32x4 xo[2];
#pragma unroll
    for (int c = 0; c < 2; ++c) {
      const bool ok = c == 0 || sub < 2;
      const int ch = ok ? c * 16 + sub : sub;
      const LAS float* p0 = rt + prow * 148 + ch * 8;
      const f32x4 a0 = *(const LAS f32x4*)p0 + *(const LAS f32x4*)(p0 + 32 * 148);
      const f32x4 a1 = *(const LAS f32x4*)(p0 + 4) + *(const LAS f32x4*)(p0 + 32 * 148 + 4);
      const u32x4 y = {pk2(a0[0], a0[1]), pk2(a0[2], a0[3]), pk2(a1[0], a1[1]), pk2(a1[2], a1[3])};
#pragma unroll
      for (int e = 0; e < 4; ++e) xo[c][e] = pk2(bflo(rv[c][e]) + bflo(y[e]), bfhi(rv[c][e]) + bfhi(y[e]));
      if (ok) *(u32x4*)(Y + gofs + ch * 8) = xo[c];
    }
    row_stats(xo[0], xo[1], sub, stats + (tt * 64 + th * 32 + prow) * 4 + ct);
  }
  RSTAMP(16);
  STAMPEND(1, 1);
  RSTAMP(17);
  RDUMP(18);
}

constexpr int K3_XS = 32768, K3_UOFF = 9216 + 128, K3_WS = K3_UOFF + 9216;
constexpr int K3_WBASE = 2 * K3_XS, K3_AUX = K3_WBASE + 4 * K3_WS;
constexpr int K3_CS = K3_AUX + 8192;
constexpr int K3_DUMMY = K3_CS + 3584;
constexpr int K3_LDS = K3_DUMMY + 4096;

template <int NJ, int J0>
DEVI void k3_body(LAS char* lds, const char* X, const char* wsrc, bf16_t* __restrict__ H, const unsigned (&xoff)[4], const unsigned (&woff)[3],
                  unsigned xdst, unsigned wdst, int lane, int wr, int pm, int pf, const char* Wd, unsigned pfoff) {
  auto stageX = [&](int t) { glds_4(X + t * 128, xoff[0], xoff[1], xoff[2], xoff[3], xdst + (t & 1) * K3_XS); };
  auto stageW = [&](int s) { glds_2q(wsrc + (size_t)s * 32 * F_ * 2, woff[0], woff[1], woff[2], wdst + (s & 3) * K3_WS); };
  const int tk = lane & 31, h = lane >> 5, q4 = (lane >> 2) & 3, p4 = lane & 3, isup = (lane >> 4) & 1;
  const int xa = (wr * 64 + tk) * 128 + ((h ^ ((tk >> 1) & 7)) * 16);
  const int wa = K3_WBASE + isup * K3_UOFF + (h * 8 + q4) * 288 + J0 * 32 + p4 * 8;
  auto rdW = [&](int wo, int kk, int j) -> bf16x8 {
    const LAS char* p = lds + (wa + wo + kk * (16 * 288) + j * 32);
    return lds_tr2(p, p + 4 * 288);
  };
  auto rdX = [&](int xo, int k4, int i) -> bf16x8 {
    return lds_b128(lds + ((xa ^ (k4 * 32)) + xo + i * 4096));
  };
  f32x16 acc[2][NJ];
#pragma unroll
  for (int i = 0; i < 2; ++i)
#pragma unroll
    for (int j = 0; j < NJ; ++j)
#pragma unroll
      for (int r = 0; r < 16; ++r) acc[i][j][r] = 0.f;
  auto correction = [&]() {
    bf16x8 mf[2];
#pragma unroll
    for (int i = 0; i < 2; ++i) {
      const LAS f32x4* sp = (const LAS f32x4*)(lds + K3_AUX + (wr * 64 + i * 32 + tk) * 32);
      const f32x4 s01 = sp[0], s23 = sp[1];
      const float mean = rbf((s01[0] + s01[2] + s23[0] + s23[2]) * 0.25f);
      const float m0 = rbf(-mean), m1 = rbf(-mean - m0), m2_ = (-mean - m0) - m1;
      u32x4 w = {pk2(m0, m0), pk2(m0, m1), pk2(m1, m2_), 0u};
      if (h) w = (u32x4){0u, 0u, 0u, 0u};
      mf[i] = __builtin_bit_cast(bf16x8, w);
    }
#pragma unroll
    for (int j = 0; j < NJ; ++j) {
      const LAS float* cp = (const LAS float*)(lds + K3_CS) + isup * 144 + (J0 + j) * 16 + (lane & 15);
      const float c = (cp[0] + cp[288]) + cp[576];
      const float c0 = rbf(c), c1 = rbf(c - c0), c2 = (c - c0) - c1;
      const u32x4 w = {pk2(c0, c1), pk2(c2, c0), pk2(c1, c0), 0u};
      const bf16x8 cf = __builtin_bit_cast(bf16x8, w);
#pragma unroll
      for (int i = 0; i < 2; ++i) acc[i][j] = __builtin_amdgcn_mfma_f32_32x32x16_bf16(cf, mf[i], acc[i][j], 0, 0, 0);
    }
  };
  WAIT_V(10); BAR();
  correction();
  bf16x8 xfa[2], xfb[2], xfc[2], wf[4];
  WAIT_V(3); BAR();
  STAMP(1);
  xfa[0] = rdX(0, 0, 0); xfa[1] = rdX(0, 0, 1);
  wf[0] = rdW(0, 0, 0); wf[1] = rdW(0, 0, 1);
  stageX(1); stageW(2);

  if (PRIO == 1 && NJ == 5) __builtin_amdgcn_s_setprio(1);
  if (PRIO == 2 && NJ == 4) __builtin_amdgcn_s_setprio(1);
#pragma unroll 1
  for (int t = 0; t < 9; ++t) {
    const int tp = t & 1;
    const int wo = tp * (2 * K3_WS), won = (1 - tp) * (2 * K3_WS);
    const int xo = tp * K3_XS, xon = (1 - tp) * K3_XS;
#pragma unroll
    for (int k4 = 0; k4 < 4; ++k4) {
      if (k4 == 0) {
        if (t < 8) { WAIT_V(7); }
        ABL_B(BAR());
      }
      if (k4 == 2) {
        if (t < 7) { WAIT_V(3); } else if (t == 7) { WAIT_V(0); }
        WAIT_L0();
        ABL_B(BAR());
        if (t == 7) {
          glds_4(Wd, pfoff, pfoff, pfoff, pfoff, ldsa(lds) + K3_DUMMY);
          glds_2(Wd + 3136, pfoff, pfoff, ldsa(lds) + K3_DUMMY);
        }
      }
      __builtin_amdgcn_sched_barrier(0);
#pragma unroll
      for (int j = 0; j < NJ; ++j) {
        const int n = k4 * NJ + j;
        if (j == 0) {
          if (k4 == (NJ == 4 ? 0 : 1) && t < 8) stageW(2 * t + 3);
          if (k4 == (NJ == 4 ? 2 : 3) && t < 7) { stageW(2 * t + 4); stageX(t + 2); }
        }
        {
          const int n2 = n + 2, k42 = n2 / NJ, j2 = n2 % NJ;
          if (k42 < 4) { ABL_L(wf[n2 & 3] = rdW(wo + (k42 >> 1) * K3_WS, k42 & 1, j2);) }
          else { ABL_L(wf[n2 & 3] = rdW(won, 0, j2);) }
        }
        if (j == 1) {
          if (k4 == 0) { ABL_L(xfb[0] = rdX(xo, 1, 0); xfb[1] = rdX(xo, 1, 1);) }
          if (k4 == 1) { ABL_L(xfa[0] = rdX(xo, 2, 0); xfa[1] = rdX(xo, 2, 1);) }
          if (k4 == 3) { ABL_L(xfa[0] = rdX(xon, 0, 0); xfa[1] = rdX(xon, 0, 1);) }
        }
        if (j == 2 && k4 == 1) { ABL_L(xfc[0] = rdX(xo, 3, 0); xfc[1] = rdX(xo, 3, 1);) }
#pragma unroll
        for (int i = 0; i < 2; ++i)
          acc[i][j] = __builtin_amdgcn_mfma_f32_32x32x16_bf16(wf[n & 3], k4 == 1 ? xfb[i] : (k4 == 3 ? xfc[i] : xfa[i]), acc[i][j], 0, 0, 0);
        __builtin_amdgcn_sched_barrier(0);
      }
    }
  }
  STAMP(2);
  float rs[2];
#pragma unroll
  for (int i = 0; i < 2; ++i) {
    const LAS f32x4* sp = (const LAS f32x4*)(lds + K3_AUX + (wr * 64 + i * 32 + tk) * 32);
    const f32x4 s01 = sp[0], s23 = sp[1];
    const float mean = (s01[0] + s01[2] + s23[0] + s23[2]) * 0.25f;
    const float d0 = s01[0] - mean, d1 = s01[2] - mean, d2 = s23[0] - mean, d3 = s23[2] - mean;
    const float m2 = (s01[1] + s01[3]) + (s23[1] + s23[3]) + 144.0f * ((d0 * d0 + d1 * d1) + (d2 * d2 + d3 * d3));
    { const float den = rbf(__builtin_amdgcn_sqrtf(rbf(rbf(m2 * (1.0f / D_)) + LN_EPS))); rs[i] = den * den; }
  }
  const __amdgpu_buffer_rsrc_t rsh = __builtin_amdgcn_make_buffer_rsrc((void*)H, 0, S_ * F_ * 2, 0x00020000);
  const int hofs = (((pf * 9 + J0) * 2 + h) * S_ + pm * 256 + wr * 64 + tk) * 16;
#pragma unroll
  for (int i = 0; i < 2; ++i) {
    const float c1 = -1.4426950408889634f * __builtin_amdgcn_rsqf(rs[i]);
    const f32x2 c1v = {c1, c1}, inv = {rs[i], rs[i]};
#pragma unroll
    for (int j = 0; j < NJ; ++j) {
      u32x4 hw;
#pragma unroll
      for (int r = 0; r < 4; ++r) {
        const f32x2 ag = {acc[i][j][2 * r], acc[i][j][2 * r + 1]}, au = {acc[i][j][8 + 2 * r], acc[i][j][8 + 2 * r + 1]};
        const f32x2 z = ag * c1v;
        const f32x2 e = {__builtin_amdgcn_exp2f(z.x), __builtin_amdgcn_exp2f(z.y)};
        const f32x2 d = e * inv + inv;
        const f32x2 rc = {__builtin_amdgcn_rcpf(d.x), __builtin_amdgcn_rcpf(d.y)};
        const f32x2 hv = (ag * au) * rc;
        hw[r] = pk2(hv.x, hv.y);
      }
      __builtin_amdgcn_raw_buffer_store_b128(hw, rsh, hofs + j * (2 * S_ * 16) + i * 512, 0, HAUX);
    }
  }
  STAMP(3);
}

__global__ __launch_bounds__(512, 2) void k3_gateup(const bf16_t* __restrict__ X, const bf16_t* __restrict__ Wg, const bf16_t* __restrict__ Wu,
                                                     const float* __restrict__ csum, const f32x2* __restrict__ stats, bf16_t* __restrict__ H,
                                                     const bf16_t* __restrict__ Wd) {
  __shared__ __attribute__((aligned(16))) char lds_[K3_LDS];
  LAS char* lds = (LAS char*)lds_;
  const unsigned lbase = ldsa(lds);
  const int tid = threadIdx.x, lane = tid & 63, wid = __builtin_amdgcn_readfirstlane(tid >> 6);
  const int bid = blockIdx.x, xcd = bid & 7, idx = bid >> 3;
  const int pm = (xcd >> 2) * 4 + (idx & 3), pf = (xcd & 3) * 8 + (idx >> 2);
  const int wr = wid & 3, wc = wid >> 2;
  STAMP(0);
  glds16((const char*)(stats + pm * 256 * 4), (unsigned)(tid * 16), lbase + K3_AUX + wid * 1024);
  {
    const int l36 = lane < 36 ? lane : 35;
    const int cw = wid < 6 ? wid : 0;
    if (lane < 36) glds16((const char*)(csum + cw * F_ + pf * 144), (unsigned)(l36 * 16), lbase + K3_CS + cw * 576);
  }
  unsigned xoff[4], woff[3];
#pragma unroll
  for (int i = 0; i < 4; ++i) {
    const int row = (wid * 4 + i) * 8 + (lane >> 3);
    const int c = (lane & 7) ^ ((row >> 1) & 7);
    xoff[i] = (unsigned)(((pm * 256 + row) * D_ + c * 8) * 2 - i * 1024);
  }
  const unsigned xdst = lbase + wid * 4096;
  glds_4((const char*)X, xoff[0], xoff[1], xoff[2], xoff[3], xdst);
#pragma unroll
  for (int i = 0; i < 3; ++i) {
    int ci = i * 64 + lane; if (ci > 143) ci = 143;
    const int kk = ci / 18, cp = ci - kk * 18;
    woff[i] = (unsigned)((((wid & 3) * 8 + kk) * F_ + pf * 144 + cp * 8) * 2 - i * 1024);
  }
  const char* wsrc = (const char*)((wid >> 2) ? Wu : Wg);
  const unsigned wdst = lbase + K3_WBASE + (wid >> 2) * K3_UOFF + (wid & 3) * 2304;
  glds_2q(wsrc, woff[0], woff[1], woff[2], wdst);
  glds_2q(wsrc + 32 * F_ * 2, woff[0], woff[1], woff[2], wdst + K3_WS);
  const unsigned pfoff = (unsigned)((xcd & 3) * (1152 * D_ * 2) + (idx * 8 + wid) * 5184 + lane * 16);
  if (wc == 0) k3_body<5, 0>(lds, (const char*)X, wsrc, H, xoff, woff, xdst, wdst, lane, wr, pm, pf, (const char*)Wd, pfoff);
  else k3_body<4, 5>(lds, (const char*)X, wsrc, H, xoff, woff, xdst, wdst, lane, wr, pm, pf, (const char*)Wd, pfoff);
  STAMPEND(3, 4);
}

constexpr int K4_XB = 16384, K4_STG = K4_XB + 18432, K4_LDS = 4 * K4_STG;

template <int NJ, int J0>
DEVI void k4_body(LAS char* lds, const char* X, const char* W, unsigned xoff, const unsigned (&woff)[3], unsigned sdst, unsigned wdst,
                  int lane, int wm, int split, int tile, float* __restrict__ P, unsigned* __restrict__ cnt) {
  auto stage = [&](int kt, int buf) {
    glds_2(X + (size_t)kt * (8 * S_ * 16), xoff, xoff, sdst + buf * K4_STG);
    glds_2q(W + (size_t)kt * 64 * D_ * 2, woff[0], woff[1], woff[2], wdst + buf * K4_STG);
  };
  const int g = lane >> 4, m = lane & 15, q4 = (lane >> 2) & 3, p4 = lane & 3;
  const int xa = g * 2048 + (wm * 32 + m) * 16;
  const int wa = K4_XB + ((g >> 1) * 16 + (g & 1) * 4 + q4) * 288 + J0 * 32 + p4 * 8;
  f32x4 acc[2][NJ];
#pragma unroll
  for (int i = 0; i < 2; ++i)
#pragma unroll
    for (int j = 0; j < NJ; ++j) acc[i][j] = (f32x4){0, 0, 0, 0};
  bf16x8 xa_[2], wa_[NJ], xb_[2], wb_[NJ];
  auto rd = [&](bf16x8 (&xf)[2], bf16x8 (&wf)[NJ], const LAS char* sb, int ks) {
#pragma unroll
    for (int i = 0; i < 2; ++i) xf[i] = lds_b128(sb + xa + ks * 8192 + i * 256);
#pragma unroll
    for (int j = 0; j < NJ; ++j) wf[j] = lds_tr2(sb + wa + ks * (32 * 288) + j * 32, sb + wa + ks * (32 * 288) + j * 32 + 8 * 288);
  };
  auto step = [&](bf16x8 (&xc)[2], bf16x8 (&wc)[NJ], bf16x8 (&xn)[2], bf16x8 (&wn)[NJ], const LAS char* sb, int ks) {
#pragma unroll
    for (int j = 0; j < NJ; ++j) {
      if (j < 2) xn[j] = lds_b128(sb + xa + ks * 8192 + j * 256);
      wn[j] = lds_tr2(sb + wa + ks * (32 * 288) + j * 32, sb + wa + ks * (32 * 288) + j * 32 + 8 * 288);
#pragma unroll
      for (int i = 0; i < 2; ++i) acc[i][j] = mfma16(wc[j], xc[i], acc[i][j]);
      __builtin_amdgcn_sched_barrier(0);
    }
  };
  WAIT_V(5); BAR();
  STAMPK(4, 1);
  rd(xa_, wa_, lds, 0);
  stage(2, 2);
  step(xa_, wa_, xb_, wb_, lds, 1);
  stage(3, 3);
  auto iter = [&](auto INFL, auto MORE, int t) {
    asm volatile("s_waitcnt vmcnt(%0) lgkmcnt(0)" ::"n"(decltype(INFL)::value * 5) : "memory");
    BAR();
    __builtin_amdgcn_sched_barrier(0);
    const LAS char* sn = lds + ((t + 1) & 3) * K4_STG;
    if (decltype(MORE)::value && NJ == 4) stage(t + 4, t & 3);
    step(xb_, wb_, xa_, wa_, sn, 0);
    if (decltype(MORE)::value && NJ == 5) stage(t + 4, t & 3);
    step(xa_, wa_, xb_, wb_, sn, 1);
  };
#pragma unroll 1
  for (int t = 0; t < 14; ++t) iter(IC<2>{}, IC<1>{}, t);
  iter(IC<2>{}, IC<0>{}, 14);
  iter(IC<1>{}, IC<0>{}, 15);
  iter(IC<0>{}, IC<0>{}, 16);
#pragma unroll
  for (int j = 0; j < NJ; ++j)
#pragma unroll
    for (int i = 0; i < 2; ++i) acc[i][j] = mfma16(wb_[j], xb_[i], acc[i][j]);
  STAMPK(4, 2);
  const __amdgpu_buffer_rsrc_t rs = __builtin_amdgcn_make_buffer_rsrc((void*)P, 0, 64 * 16 * 9 * 1024, 0x00020000);
  if (wm != split) {
    const int sb = ((tile * 4 + wm) * 4 + split) * 9 * 1024 + lane * 16;
#pragma unroll
    for (int j = 0; j < NJ; ++j) {
      const u32x4 v = {pk2(acc[0][j][0], acc[0][j][1]), pk2(acc[0][j][2], acc[0][j][3]), pk2(acc[1][j][0], acc[1][j][1]), pk2(acc[1][j][2], acc[1][j][3])};
      __builtin_amdgcn_raw_buffer_store_b128(v, rs, sb + (J0 + j) * 1024, 0, 16);
    }
    WAIT_V(0);
    if (lane == 0) __hip_atomic_fetch_add(cnt + (tile * 4 + wm) * 32, 1u, __ATOMIC_RELAXED, __HIP_MEMORY_SCOPE_AGENT);
  } else {
    LAS f32x4* own = (LAS f32x4*)(lds + 2 * K4_STG);
#pragma unroll
    for (int i = 0; i < 2; ++i)
#pragma unroll
      for (int j = 0; j < NJ; ++j) own[(i * 9 + J0 + j) * 64 + lane] = acc[i][j];
    if (NJ == 5) {
      unsigned spins = 0;
      while ((unsigned)__builtin_amdgcn_readfirstlane(__hip_atomic_load(cnt + (tile * 4 + split) * 32, __ATOMIC_RELAXED, __HIP_MEMORY_SCOPE_AGENT)) < 6u) {
        __builtin_amdgcn_s_sleep(1);
        if (++spins > (1u << 22)) break;
      }
    }
  }
}

__global__ __launch_bounds__(512, 2) void k4_down(const bf16_t* __restrict__ X, const bf16_t* __restrict__ W, const bf16_t* __restrict__ R,
                                                   bf16_t* __restrict__ Y, unsigned* __restrict__ cnt, float* __restrict__ P, f32x2* __restrict__ xstats) {
  __shared__ __attribute__((aligned(16))) char lds_[K4_LDS];
  LAS char* lds = (LAS char*)lds_;
  const unsigned lbase = ldsa(lds);
  const int tid = threadIdx.x, lane = tid & 63, wid = __builtin_amdgcn_readfirstlane(tid >> 6);
  const int bid = blockIdx.x, xcd = bid & 7, idx = bid >> 3;
  const int split = xcd & 3;
  const int m0 = (xcd >> 2) * 1024 + (idx >> 2) * 128, n0 = (idx & 3) * 144, k0 = split * 1152;
  const int wm = wid & 3, wn = wid >> 2;
  STAMPK(4, 0);
  const unsigned xoff = (unsigned)((((k0 >> 3) + wid) * S_ + m0 + lane) * 16);
  unsigned woff[3];
#pragma unroll
  for (int i = 0; i < 3; ++i) {
    int ci = i * 64 + lane; if (ci > 143) ci = 143;
    const int rr = ci / 18, c = ci - rr * 18;
    woff[i] = (unsigned)(((k0 + wid * 8 + rr) * D_ + n0 + c * 8) * 2 - i * 1024);
  }
  const unsigned sdst = lbase + wid * 2048, wdst = lbase + K4_XB + wid * 2304;
#pragma unroll
  for (int s = 0; s < 2; ++s) {
    glds_2((const char*)X + (size_t)s * (8 * S_ * 16), xoff, xoff, sdst + s * K4_STG);
    glds_2q((const char*)W + (size_t)s * 64 * D_ * 2, woff[0], woff[1], woff[2], wdst + s * K4_STG);
  }
  const int tile = (m0 >> 7) * 4 + (idx & 3);
  if (wn == 0) k4_body<5, 0>(lds, (const char*)X, (const char*)W, xoff, woff, sdst, wdst, lane, wm, split, tile, P, cnt);
  else k4_body<4, 5>(lds, (const char*)X, (const char*)W, xoff, woff, sdst, wdst, lane, wm, split, tile, P, cnt);
  const int row = tid >> 4, sub = tid & 15;
  const size_t gofs = (size_t)(m0 + split * 32 + row) * D_ + n0;
  u32x4 rv[2];
  rv[0] = *(const u32x4*)(R + gofs + sub * 8);
  rv[1] = *(const u32x4*)(R + gofs + (sub < 2 ? 16 + sub : sub) * 8);
  WAIT_L0(); BAR();
  STAMPK(4, 3);
  const int g = lane >> 4, m = lane & 15;
  LAS f32x4* own = (LAS f32x4*)(lds + 2 * K4_STG);
  LAS float* rt = (LAS float*)lds;
  {
    const __amdgpu_buffer_rsrc_t rs = __builtin_amdgcn_make_buffer_rsrc((void*)P, 0, 64 * 16 * 9 * 1024, 0x00020000);
    u32x4 pv[2][3];
#pragma unroll
    for (int n = 0; n < 2; ++n) {
      const int j = wid + 8 * n < 9 ? wid + 8 * n : wid;
#pragma unroll
      for (int sx = 0; sx < 3; ++sx) {
        const int src = sx + (sx >= split);
        pv[n][sx] = __builtin_amdgcn_raw_buffer_load_b128(rs, (((tile * 4 + split) * 4 + src) * 9 + j) * 1024 + lane * 16, 0, 16);
      }
    }
#pragma unroll
    for (int n = 0; n < 2; ++n) {
      const int j = wid + 8 * n;
      if (j < 9) {
#pragma unroll
        for (int i = 0; i < 2; ++i) {
          f32x4 v = own[(i * 9 + j) * 64 + lane];
#pragma unroll
          for (int sx = 0; sx < 3; ++sx)
            v += (f32x4){bflo(pv[n][sx][2 * i]), bfhi(pv[n][sx][2 * i]), bflo(pv[n][sx][2 * i + 1]), bfhi(pv[n][sx][2 * i + 1])};
          *(LAS f32x4*)(rt + (i * 16 + m) * 148 + j * 16 + g * 4) = v;
        }
      }
    }
  }
  WAIT_L0(); BAR();
  {
    u32x4 xo[2];
#pragma unroll
    for (int c = 0; c < 2; ++c) {
      const bool ok = c == 0 || sub < 2;
      const int ch = ok ? c * 16 + sub : sub;
      const LAS float* p0 = rt + row * 148 + ch * 8;
      const f32x4 a0 = *(const LAS f32x4*)p0, a1 = *(const LAS f32x4*)(p0 + 4);
      const u32x4 y = {pk2(a0[0], a0[1]), pk2(a0[2], a0[3]), pk2(a1[0], a1[1]), pk2(a1[2], a1[3])};
#pragma unroll
      for (int e = 0; e < 4; ++e) xo[c][e] = pk2(bflo(rv[c][e]) + bflo(y[e]), bfhi(rv[c][e]) + bfhi(y[e]));
      if (ok) *(u32x4*)(Y + gofs + ch * 8) = xo[c];
    }
    row_stats(xo[0], xo[1], sub, xstats + (m0 + split * 32 + row) * 4 + (idx & 3));
  }
  STAMPEND(4, 4);
  TOT1();
}

extern "C" __global__ void k9_end() {}

extern "C" void kernel_launch(void* const* d_in, const int* in_sizes, int n_in,
                              void* d_out, int out_size, void* d_ws, size_t ws_size,
                              hipStream_t stream) {
  const bf16_t* x0 = (const bf16_t*)d_in[0];
  const bf16_t* wqkv = (const bf16_t*)d_in[1];
  const bf16_t* wo = (const bf16_t*)d_in[2];
  const bf16_t* wg = (const bf16_t*)d_in[4];
  const bf16_t* wu = (const bf16_t*)d_in[5];
  const bf16_t* wd = (const bf16_t*)d_in[6];
  bf16_t* out = (bf16_t*)d_out;
  char* ws = (char*)d_ws;
  constexpr size_t XB = (size_t)S_ * D_ * 2, HB = (size_t)S_ * F_ * 2, PB = (size_t)4 * S_ * D_ * 4;
  size_t off = 0;
  float* csum = (float*)(ws + off); off += 131072;
  unsigned* cnt = (unsigned*)(ws + off); off += NCNT * 128;
  char* wp = ws + off; off += (size_t)144 * K1_WS;
  const bf16_t* xin = x0;
  char* o = ws + off; off += XB;
  bf16_t* x1 = (bf16_t*)(ws + off); off += XB;
  f32x2* stats = (f32x2*)(ws + off); off += 65536;
  f32x2* xstats = (f32x2*)(ws + off); off += 65536;
  bf16_t* xm = (bf16_t*)(ws + off); off += XB;
  bf16_t* h = (bf16_t*)(ws + off); off += HB;
  float* P = (float*)(ws + off); off += PB;
  k0_prep<<<144 + 512 + 41, 256, 0, stream>>>(wqkv, wp, cnt, x0, wo, xstats);
  for (int l = 0; l < 2; ++l) {
    bf16_t* x2 = (l == 1) ? out : xm;
    k1_qkv_attn<<<256, l == 0 ? 576 : 512, 0, stream>>>(xin, wp, o, wo, x1, stats, cnt + (l * 512 + 256) * 32, wg, wu, csum, xstats);
    k3_gateup<<<256, 512, 0, stream>>>(x1, wg, wu, csum, stats, h, wd);
    k4_down<<<256, 512, 0, stream>>>(h, wd, x1, x2, cnt + (l * 512) * 32, P, xstats);
    xin = x2;
  }
}
```

```cpp
#include <hip/hip_runtime.h>
#include <hip/hip_bf16.h>
#include <stdint.h>

typedef unsigned short bf16_t;
typedef short bf16x8 __attribute__((ext_vector_type(8)));
typedef short bf16x4 __attribute__((ext_vector_type(4)));
typedef float f32x4 __attribute__((ext_vector_type(4)));
typedef float f32x2 __attribute__((ext_vector_type(2)));
typedef float f32x16 __attribute__((ext_vector_type(16)));
typedef unsigned u32x4 __attribute__((ext_vector_type(4)));
typedef unsigned u32x2 __attribute__((ext_vector_type(2)));
#define LAS __attribute__((address_space(3)))
#define DEVI __device__ __forceinline__

constexpr int S_ = 2048, D_ = 576, F_ = 4608, NH_ = 32, DH_ = 18;
constexpr float LN_EPS = 1e-5f;

DEVI float bf2f(unsigned b) { return __uint_as_float(b << 16); }
DEVI float bflo(unsigned w) { return __uint_as_float(w << 16); }
DEVI float bfhi(unsigned w) { return __uint_as_float(w & 0xffff0000u); }
DEVI unsigned pk2(float lo, float hi) {
  typedef __bf16 bf2v __attribute__((ext_vector_type(2)));
  bf2v v; v.x = (__bf16)lo; v.y = (__bf16)hi;
  return __builtin_bit_cast(unsigned, v);
}
template <int CTRL> DEVI float dpp(float x) { return __builtin_bit_cast(float, __builtin_amdgcn_update_dpp(0, __builtin_bit_cast(int, x), CTRL, 0xf, 0xf, true)); }
DEVI float sum8(float x) {
  x += dpp<0xB1>(x); x += dpp<0x4E>(x); x += dpp<0x141>(x); return x;
}
DEVI float rbf(float x) { return (float)((__bf16)x); }

#define TOT0()
#define TOT1()
#define RSTAMP(i)
#define RDUMP(n)
#define STAMPW(kid, i, cond)
#define STAMPK(kid, i)
#define STAMPEND(kid, i)
#define STAMP(i) STAMPK(3, i)
#define ABL_G(x) x
#define ABL_B(x) x
#define ABL_L(...) __VA_ARGS__
#define LNMIMIC 1
#define TAILPRIO 0
#define NPF 0
#define HAUX 16
#define GV 3
#define PRIO 0
template <int N> struct IC { static constexpr int value = N; };
template <int A, int B, class F> __device__ __forceinline__ void static_for(F&& f) {
  if constexpr (A < B) { f(IC<A>{}); static_for<A + 1, B>(f); }
}
#define WAIT_V(n) asm volatile("s_waitcnt vmcnt(" #n ")" ::: "memory")
#define WAIT_L0() asm volatile("s_waitcnt lgkmcnt(0)" ::: "memory")
#define BAR() do { asm volatile("" ::: "memory"); __builtin_amdgcn_s_barrier(); asm volatile("" ::: "memory"); } while (0)

DEVI void glds16(const char* sbase, unsigned voff, unsigned ldsaddr) {
  asm volatile("s_mov_b32 m0, %2\n\ts_nop 0\n\tglobal_load_lds_dwordx4 %0, %1" ::"v"(voff), "s"(sbase), "s"(ldsaddr) : "memory");
}
DEVI void glds_1(const char* sb, unsigned v0, unsigned la) { glds16(sb, v0, la); }
DEVI void glds_2(const char* sb, unsigned v0, unsigned v1, unsigned la) {
  asm volatile("s_mov_b32 m0, %3\n\ts_nop 0\n\tglobal_load_lds_dwordx4 %0, %2\n\tglobal_load_lds_dwordx4 %1, %2 offset:1024"
               ::"v"(v0), "v"(v1), "s"(sb), "s"(la) : "memory");
}
DEVI void glds_4(const char* sb, unsigned v0, unsigned v1, unsigned v2, unsigned v3, unsigned la) {
  asm volatile("s_mov_b32 m0, %5\n\ts_nop 0\n\tglobal_load_lds_dwordx4 %0, %4\n\tglobal_load_lds_dwordx4 %1, %4 offset:1024\n\t"
               "global_load_lds_dwordx4 %2, %4 offset:2048\n\tglobal_load_lds_dwordx4 %3, %4 offset:3072"
               ::"v"(v0), "v"(v1), "v"(v2), "v"(v3), "s"(sb), "s"(la) : "memory");
}
DEVI void glds_2q(const char* sb, unsigned v0, unsigned v1, unsigned v2, unsigned la) {
  asm volatile("s_mov_b32 m0, %4\n\ts_nop 0\n\tglobal_load_lds_dwordx4 %0, %3\n\tglobal_load_lds_dwordx4 %1, %3 offset:1024\n\t"
               "s_mov_b64 exec, 0xffff\n\tglobal_load_lds_dwordx4 %2, %3 offset:2048\n\ts_mov_b64 exec, -1"
               ::"v"(v0), "v"(v1), "v"(v2), "s"(sb), "s"(la) : "memory");
}
DEVI void glds_2m(const char* sb, unsigned v0, unsigned v1, unsigned la, unsigned long long m0_, unsigned long long m1_) {
  asm volatile("s_mov_b32 m0, %3\n\ts_mov_b64 exec, %4\n\tglobal_load_lds_dwordx4 %0, %2\n\t"
               "s_mov_b64 exec, %5\n\tglobal_load_lds_dwordx4 %1, %2 offset:1024\n\ts_mov_b64 exec, -1"
               ::"v"(v0), "v"(v1), "s"(sb), "s"(la), "s"(m0_), "s"(m1_) : "memory");
}
DEVI unsigned ldsa(const LAS char* p) { return (unsigned)(size_t)p; }
DEVI bf16x8 lds_b128(const LAS char* p) { return *(const LAS bf16x8*)p; }
DEVI bf16x8 lds_tr2(const LAS char* p0, const LAS char* p1) {
  bf16x4 a = __builtin_amdgcn_ds_read_tr16_b64_v4i16((LAS bf16x4*)p0);
  bf16x4 b = __builtin_amdgcn_ds_read_tr16_b64_v4i16((LAS bf16x4*)p1);
  return __builtin_shufflevector(a, b, 0, 1, 2, 3, 4, 5, 6, 7);
}
DEVI f32x4 mfma16(bf16x8 a, bf16x8 b, f32x4 c) { return __builtin_amdgcn_mfma_f32_16x16x32_bf16(a, b, c, 0, 0, 0); }

constexpr int K1_WR = 16 * 448 + 32;
constexpr int K1_WS = 14464;
constexpr int NCNT = 1024;

DEVI void row_stats(const u32x4& v0, const u32x4& v1, int sub, f32x2* __restrict__ dst) {
  float xv[16];
#pragma unroll
  for (int e = 0; e < 4; ++e) {
    xv[2 * e] = bflo(v0[e]); xv[2 * e + 1] = bfhi(v0[e]);
    xv[8 + 2 * e] = sub < 2 ? bflo(v1[e]) : 0.f; xv[8 + 2 * e + 1] = sub < 2 ? bfhi(v1[e]) : 0.f;
  }
  float s = 0.f;
#pragma unroll
  for (int e = 0; e < 16; ++e) s += xv[e];
  s = sum8(s); s += dpp<0x140>(s);
  const float mean = s * (1.0f / 144.0f);
  float q = 0.f;
#pragma unroll
  for (int e = 0; e < 16; ++e) { const float d = xv[e] - mean; q += (e < 8 || sub < 2) ? d * d : 0.f; }
  q = sum8(q); q += dpp<0x140>(q);
  if (sub == 0) *dst = (f32x2){mean, q};
}

__global__ __launch_bounds__(256) void k0_prep(const bf16_t* __restrict__ Wqkv, char* __restrict__ Wp, unsigned* __restrict__ cnt,
                                                const bf16_t* __restrict__ X, const bf16_t* __restrict__ Wo, f32x2* __restrict__ xstats,
                                                bf16_t* __restrict__ WB) {
  STAMPK(0, 0);
  if (blockIdx.x < 8) TOT0();
  const int b = blockIdx.x;
  {
    unsigned long long pc0, pc1;
    asm volatile("s_getpc_b64 %0" : "=s"(pc0));
    asm volatile("s_getpc_b64 s[40:41]\n\ts_add_u32 s40, s40, k9_end@rel32@lo+4\n\ts_addc_u32 s41, s41, k9_end@rel32@hi+12\n\ts_mov_b64 %0, s[40:41]"
                 : "=s"(pc1) : : "s40", "s41", "scc");
    const unsigned long long a = (pc0 & ~15ull) + (unsigned long long)(b >> 3) * 4096 + threadIdx.x * 16;
    if (pc1 > pc0 && a + 16 <= pc1) { const u32x4 v = *(const u32x4*)a; asm volatile("" ::"v"(v)); }
  }
  if (b < 144) {
    const int u = b, hg = u / 18, sl = u - hg * 18;
    char* dst = Wp + (size_t)u * K1_WS;
    u32x4 v[4];
#pragma unroll
    for (int i = 0; i < 4; ++i) {
      const int ci = i * 256 + threadIdx.x;
      const int reg = ci >= 450, cj = ci - reg * 450;
      const int rr = cj / 28;
      int ch = cj - rr * 28; if (ch > 26) ch = 26;
      const int seg = ch / 9, cc = ch - seg * 9;
      const int gcol = (seg == 2 ? 0 : (seg + 1) * D_) + hg * 72 + cc * 8;
      const int rc = rr > 15 ? 15 : rr;
      const int k = sl * 32 + (rc >> 3) * 16 + reg * 8 + (rc & 7);
      v[i] = *(const u32x4*)(Wqkv + (size_t)k * (3 * D_) + gcol);
    }
    if (u < NCNT / 256) cnt[(u * 256 + threadIdx.x) * 32] = 0u;
#pragma unroll
    for (int i = 0; i < 4; ++i) {
      const int ci = i * 256 + threadIdx.x;
      if (ci < 904) *(u32x4*)(dst + ci * 16) = v[i];
    }
  } else if (b < 144 + 512) {
    const int u = (b - 144) * 16 + (threadIdx.x >> 4), sub = threadIdx.x & 15;
    const bf16_t* xr = X + (size_t)(u >> 2) * D_ + (u & 3) * 144;
    const u32x4 v0 = *(const u32x4*)(xr + sub * 8), v1 = *(const u32x4*)(xr + (sub < 2 ? 16 + sub : sub) * 8);
    row_stats(v0, v1, sub, xstats + u);
  } else {
    const int k = (b - 656) * 32 + (threadIdx.x >> 3), sub = threadIdx.x & 7;
    u32x4 v[9];
#pragma unroll
    for (int i = 0; i < 9; ++i) v[i] = *(const u32x4*)(Wo + (size_t)k * D_ + (i * 8 + sub) * 8);
    float t = 0.f;
#pragma unroll
    for (int i = 0; i < 9; ++i)
#pragma unroll
      for (int e = 0; e < 4; ++e) t += bflo(v[i][e]) + bfhi(v[i][e]);
    t = sum8(t) * (1.0f / D_);
    const float hi = rbf(t);
    if (sub < 2) WB[sub * D_ + k] = (bf16_t)(pk2(sub ? t - hi : hi, 0.f) & 0xffffu);
  }
  STAMPEND(0, 1);
}

constexpr int K1_XT = 80 * 128;
constexpr int K1_WBASE = 4 * K1_XT;
constexpr int K1_AUX = 156928;
constexpr int K1_MEAN = K1_AUX + 768;
constexpr int K1_WB = K1_MEAN + 320;
constexpr int K1_DUMMY = K1_WB + 2304;
constexpr int K1_LDS = K1_DUMMY + 1024;
constexpr int K1_QS = 440;
constexpr int K1_OB = 35200, K1_OS = 160;
constexpr int K2_WR = 9216 + 128, K2_WS = K2_WR + 9216, K2_WBASE = 45568;

DEVI float dot2bf(unsigned a, unsigned b, float c) {
  typedef __bf16 bf2v __attribute__((ext_vector_type(2)));
  return __builtin_amdgcn_fdot2_f32_bf16(__builtin_bit_cast(bf2v, a), __builtin_bit_cast(bf2v, b), c, false);
}

constexpr int K2_RT = 44032;

template <int NJ, int J0>
DEVI void k2_gemm(LAS char* lds, int lane, int wm, int wk, const char* Wo, const unsigned (&woff)[3], unsigned wdst,
                  const char* OB, int obase, unsigned* __restrict__ cnt, int tid, int wid) {
  const int g = lane >> 4, m = lane & 15, q4 = (lane >> 2) & 3, p4 = lane & 3;
  const int wa = K2_WBASE + (g & 1) * K2_WR + ((wk * 2 + (g >> 1)) * 8 + q4) * 288 + J0 * 32 + p4 * 8;
  auto rdW = [&](bf16x8 (&wf)[NJ], int slot) {
#pragma unroll
    for (int j = 0; j < NJ; ++j) wf[j] = lds_tr2(lds + wa + slot * K2_WS + j * 32, lds + wa + slot * K2_WS + j * 32 + 4 * 288);
  };
  bf16x8 w[6][NJ];
  rdW(w[0], 2); rdW(w[1], 3); rdW(w[2], 4); rdW(w[3], 5);
  RSTAMP(8);
  WAIT_V(0); WAIT_L0();
  RSTAMP(9);
  BAR();
  if (tid == 0) __hip_atomic_fetch_add(cnt, 1u, __ATOMIC_RELAXED, __HIP_MEMORY_SCOPE_AGENT);
  RSTAMP(10);
#pragma unroll
  for (int t = 6; t < 9; ++t) glds_2q(Wo + (size_t)t * 64 * D_ * 2, woff[0], woff[1], woff[2], wdst + (t - 4) * K2_WS);
  rdW(w[4], 0); rdW(w[5], 1);
  RSTAMP(11);
  if (wid == 0) {
    unsigned spins = 0;
    while ((unsigned)__builtin_amdgcn_readfirstlane(__hip_atomic_load(cnt, __ATOMIC_RELAXED, __HIP_MEMORY_SCOPE_AGENT)) < 8u) {
      __builtin_amdgcn_s_sleep(1);
      if (++spins > (1u << 22)) break;
    }
  }
  WAIT_V(0); WAIT_L0(); BAR();
  RSTAMP(12);
  const __amdgpu_buffer_rsrc_t rso = __builtin_amdgcn_make_buffer_rsrc((void*)OB, 0, S_ * D_ * 2, 0x00020000);
  u32x4 xf[9];
#pragma unroll
  for (int t = 0; t < 9; ++t) xf[t] = __builtin_amdgcn_raw_buffer_load_b128(rso, obase + (wk * 4 + g) * 512 + (wm * 16 + m) * 16 + t * 4096, 0, 16);
  bf16x8 w6[NJ], w7[NJ], w8[NJ];
  rdW(w6, 2); rdW(w7, 3); rdW(w8, 4);
  bf16x8 wb[9];
  if (NJ == 4) {
#pragma unroll
    for (int t = 0; t < 9; ++t) wb[t] = lds_b128(lds + K1_WB + (m & 1) * (D_ * 2) + (wk * 32 + g * 8) * 2 + t * 128);
  }
  RSTAMP(13);
  f32x4 acc[NJ];
#pragma unroll
  for (int j = 0; j < NJ; ++j) acc[j] = (f32x4){0, 0, 0, 0};
#pragma unroll
  for (int t = 0; t < 6; ++t)
#pragma unroll
    for (int j = 0; j < NJ; ++j) acc[j] = mfma16(w[t][j], __builtin_bit_cast(bf16x8, xf[t]), acc[j]);
#pragma unroll
  for (int j = 0; j < NJ; ++j) acc[j] = mfma16(w6[j], __builtin_bit_cast(bf16x8, xf[6]), acc[j]);
#pragma unroll
  for (int j = 0; j < NJ; ++j) acc[j] = mfma16(w7[j], __builtin_bit_cast(bf16x8, xf[7]), acc[j]);
#pragma unroll
  for (int j = 0; j < NJ; ++j) acc[j] = mfma16(w8[j], __builtin_bit_cast(bf16x8, xf[8]), acc[j]);
  RSTAMP(14);
  LAS float* rt = (LAS float*)(lds + K2_RT);
  if (NJ == 4) {
    f32x4 am = {0, 0, 0, 0};
#pragma unroll
    for (int t = 0; t < 9; ++t) am = mfma16(wb[t], __builtin_bit_cast(bf16x8, xf[t]), am);
    if (g == 0) *(LAS f32x4*)(rt + (wk * 32 + wm * 16 + m) * 148 + 144) = am;
  }
#pragma unroll
  for (int j = 0; j < NJ; ++j) *(LAS f32x4*)(rt + (wk * 32 + wm * 16 + m) * 148 + (J0 + j) * 16 + g * 4) = acc[j];
}

__global__ __launch_bounds__(512) void k1_qkv_attn(const bf16_t* __restrict__ X, const char* __restrict__ Wp, char* __restrict__ OB,
                                                       const bf16_t* __restrict__ Wo, bf16_t* __restrict__ Y, f32x2* __restrict__ stats,
                                                       unsigned* __restrict__ cnt, const bf16_t* __restrict__ WB, bf16_t* __restrict__ YC,
                                                       const f32x2* __restrict__ xstats, const bf16_t* __restrict__ Wg, const bf16_t* __restrict__ Wu) {
  __shared__ __attribute__((aligned(16))) char lds_[K1_LDS];
  LAS char* lds = (LAS char*)lds_;
  const unsigned lbase = ldsa(lds);
  const int tid = threadIdx.x, lane = tid & 63, wid = __builtin_amdgcn_readfirstlane(tid >> 6);
  const int bid = blockIdx.x, xcd = bid & 7, idx = bid >> 3;
  const int tt = (xcd >> 1) * 8 + (idx & 7), hg = (xcd & 1) * 4 + (idx >> 3);
  const int t0 = tt * 64 - 16;
  STAMPK(1, 0);

  unsigned xoff[2], woff[2];
#pragma unroll
  for (int i = 0; i < 2; ++i) {
    int ci = i * 64 + lane; if (ci > 79) ci = 79;
    ci += wid * 80;
    const int row = ci >> 3, c = (ci & 7) ^ ((row >> 1) & 7);
    int grow = t0 + row; if (grow < 0) grow = 0;
    xoff[i] = (unsigned)((grow * D_ + c * 8) * 2 + 1024 - i * 1024);
  }
  woff[0] = (unsigned)((wid * 113 + lane) * 16); woff[1] = woff[0];
  const unsigned wdst = lbase + K1_WBASE + wid * 1808;
  const unsigned long long xmask = wid == 0 ? ~0xffull : ~0ull;
  auto stageX = [&](int kt) {
    glds_2m((const char*)X + kt * 128 - 1024, xoff[0], xoff[1], lbase + (kt & 3) * K1_XT + wid * 1280, xmask, 0xffffull);
  };
  auto stageW = [&](int s) {
    glds_2m(Wp + (size_t)(hg * 18 + s) * K1_WS, woff[0], woff[1], wdst + (s & 7) * K1_WS, ~0ull, 0x1ffffffffffffull);
  };
  auto stageT = [&](int t) { stageW(2 * t); stageX(t); stageW(2 * t + 1); };
  asm volatile("s_mov_b32 m0, %2\n\ts_mov_b64 exec, 0x3ffff\n\tglobal_load_lds_dwordx4 %0, %1\n\ts_mov_b64 exec, -1"
               ::"v"(wid * 288 + lane * 16), "s"(WB), "s"(lbase + K1_WB + wid * 288) : "memory");
  f32x4 sa[2], sb[2];
#pragma unroll
  for (int i = 0; i < 2; ++i) {
    int gr = t0 + i * 64 + (i ? (lane & 15) : lane); if (gr < 0) gr = 0;
    asm volatile("global_load_dwordx4 %0, %2, %3\n\tglobal_load_dwordx4 %1, %2, %3 offset:16" : "=&v"(sa[i]), "=&v"(sb[i]) : "v"(gr * 32), "s"(xstats) : "memory");
  }
  stageT(0); stageT(1);
  if (tid >= 64 && tid < 64 + 32) {
    const int c = tid - 64;
    *(LAS u32x4*)(lds + (c >> 3) * K1_XT + (c & 7) * 16) = (u32x4){0x3f803f80u, 0x3f803f80u, 0x3f803f80u, 0x3f803f80u};
  }
  const int th = hg >> 2, ct = hg & 3;
  unsigned w2off[3];
#pragma unroll
  for (int i = 0; i < 3; ++i) {
    int ci = i * 64 + lane; if (ci > 143) ci = 143;
    const int rr = ci / 18, c = ci - rr * 18;
    w2off[i] = (unsigned)(((wid * 8 + rr) * D_ + ct * 144 + c * 8) * 2 - i * 1024);
  }
  const unsigned w2dst = lbase + K2_WBASE + (wid & 1) * K2_WR + (wid >> 1) * 2304;
  auto stageB = [&](int kt) { glds_2q((const char*)Wo + (size_t)kt * 64 * D_ * 2, w2off[0], w2off[1], w2off[2], w2dst + ((kt + 2) % 6) * K2_WS); };

  const int g = lane >> 4, m = lane & 15, q4 = (lane >> 2) & 3, p4 = lane & 3;
  const int xa0 = m * 128 + ((g ^ (m >> 1)) * 16);
  const int wa0 = K1_WBASE + (g & 1) * K1_WR + ((g >> 1) * 8 + q4) * 448 + (wid < 7 ? wid : 0) * 64 + p4 * 8;
  f32x4 acc[5][2];
#pragma unroll
  for (int i = 0; i < 5; ++i) { acc[i][0] = (f32x4){0, 0, 0, 0}; acc[i][1] = (f32x4){0, 0, 0, 0}; }
  LAS f32x2* ms = (LAS f32x2*)(lds + K1_AUX);
  auto top = [&](int t) {
    __builtin_amdgcn_sched_barrier(0);
    if (t < 6) { asm volatile("s_waitcnt vmcnt(12) lgkmcnt(0)" ::: "memory"); } else { asm volatile("s_waitcnt vmcnt(6) lgkmcnt(0)" ::: "memory"); }
    BAR();
    __builtin_amdgcn_sched_barrier(0);
  };
  auto stg = [&](int t) {
    if (t < 5) stageT(t + 4);
    if (t == 6) { stageB(0); stageB(1); }
    if (t == 7) { stageB(2); stageB(3); }
  };
  WAIT_V(6); WAIT_L0(); BAR();
  RSTAMP(0);
  auto rdX = [&](bf16x8 (&xf)[5], int kt, int ks) {
#pragma unroll
    for (int i = 0; i < 5; ++i) xf[i] = lds_b128(lds + ((xa0 ^ (ks * 64)) + (kt & 3) * K1_XT + i * 2048));
  };
  auto mainloop = [&](auto EARLY) {
    bf16x8 xfa[5], xfb[5], wfa[2], wfb[2];
    auto rdW = [&](bf16x8 (&wf)[2], int s) {
#pragma unroll
      for (int j = 0; j < 2; ++j) wf[j] = lds_tr2(lds + (wa0 + (s & 7) * K1_WS + j * 32), lds + (wa0 + (s & 7) * K1_WS + j * 32 + 4 * 448));
    };
    auto step = [&](bf16x8 (&wc)[2], bf16x8 (&xc)[5], bf16x8 (&wn)[2], bf16x8 (&xn)[5], int s) {
#pragma unroll
      for (int i = 0; i < 5; ++i) {
        if (i < 2) wn[i] = lds_tr2(lds + (wa0 + (s & 7) * K1_WS + i * 32), lds + (wa0 + (s & 7) * K1_WS + i * 32 + 4 * 448));
        acc[i][0] = mfma16(wc[0], xc[i], acc[i][0]);
        __builtin_amdgcn_sched_barrier(0);
        xn[i] = lds_b128(lds + ((xa0 ^ ((s & 1) * 64)) + ((s >> 1) & 3) * K1_XT + i * 2048));
        acc[i][1] = mfma16(wc[1], xc[i], acc[i][1]);
        __builtin_amdgcn_sched_barrier(0);
      }
    };
    rdX(xfa, 0, 0); rdW(wfa, 0);
    stageT(2);
    step(wfa, xfa, wfb, xfb, 1);
    stageT(3);
#pragma unroll
    for (int t = 0; t < 8; ++t) {
      top(t);
      if (decltype(EARLY)::value) stg(t);
      step(wfb, xfb, wfa, xfa, 2 * t + 2);
      if (!decltype(EARLY)::value) stg(t);
      step(wfa, xfa, wfb, xfb, 2 * t + 3);
    }
#pragma unroll
    for (int i = 0; i < 5; ++i)
#pragma unroll
      for (int j = 0; j < 2; ++j) acc[i][j] = mfma16(wfb[j], xfb[i], acc[i][j]);
  };
  if (wid < 4) {
    mainloop(IC<1>{});
  } else if (wid < 7) {
    mainloop(IC<0>{});
  } else {
    stageT(2); stageT(3);
    asm volatile("" : "+v"(sa[0]), "+v"(sa[1]), "+v"(sb[0]), "+v"(sb[1]));
#pragma unroll
    for (int i = 0; i < 2; ++i) {
      const float mean = (sa[i][0] + sa[i][2] + sb[i][0] + sb[i][2]) * 0.25f;
      const float d0 = sa[i][0] - mean, d1 = sa[i][2] - mean, d2 = sb[i][0] - mean, d3 = sb[i][2] - mean;
      const float var = ((sa[i][1] + sa[i][3]) + (sb[i][1] + sb[i][3]) + 144.0f * ((d0 * d0 + d1 * d1) + (d2 * d2 + d3 * d3))) * (1.0f / D_);
      const float den = rbf(__builtin_amdgcn_sqrtf(rbf(rbf(var) + LN_EPS)));
      if (i == 0 || lane < 16) { ms[i * 64 + lane] = (f32x2){rbf(mean), __builtin_amdgcn_rcpf(den)}; ((LAS float*)(lds + K1_MEAN))[i * 64 + lane] = mean; }
    }
#pragma unroll
    for (int t = 0; t < 8; ++t) { top(t); stg(t); }
  }
  RSTAMP(1);
  WAIT_L0(); BAR();
  RSTAMP(2);
  stageB(4); stageB(5);
#pragma unroll
  for (int i = 0; i < NPF; ++i) {
    const int c = ((((xcd >> 2) * 32 + idx) * 4 + (wid >> 1)) + i * 256) * 64 + lane;
    const int r = c / 144, cc = c - r * 144;
    glds16((const char*)((wid & 1) ? Wu : Wg), (unsigned)(r * (F_ * 2) + (xcd & 3) * 2304 + cc * 16), lbase + K1_DUMMY);
  }
  RSTAMP(3);
  if (wid < 7) {
    f32x2 mr[5];
#pragma unroll
    for (int i = 0; i < 5; ++i) mr[i] = ms[i * 16 + m];
    f32x4 cs[2];
#pragma unroll
    for (int j = 0; j < 2; ++j)
#pragma unroll
      for (int r = 0; r < 4; ++r) cs[j][r] = dpp<0x150>(acc[0][j][r]);
    LAS char* qp = lds + m * K1_QS + (wid * 32 + g * 4) * 2;
    const bool ok1 = wid * 32 + 16 + g * 4 < 216;
#pragma unroll
    for (int i = 0; i < 5; ++i) {
      const float nm = -mr[i].x * mr[i].y;
#pragma unroll
      for (int j = 0; j < 2; ++j) {
        const f32x4 v = acc[i][j] * mr[i].y + cs[j] * nm;
        if (j == 0 || ok1) *(LAS u32x2*)(qp + i * 16 * K1_QS + j * 32) = (u32x2){pk2(v[0], v[1]), pk2(v[2], v[3])};
      }
    }
  }
  RSTAMP(4);
  WAIT_L0(); BAR();
  RSTAMP(5);
  {
    const int half = lane & 1, hh = wid & 3, tok = (wid >> 2) * 32 + (lane >> 1);
    const int row = tok + 16;
    const int gpos = t0 + row;
    const LAS unsigned* bp = (const LAS unsigned*)(lds + row * K1_QS + hh * 36 + half * 20);
    unsigned qv[5], kv[8][5], vv[8][5];
#pragma unroll
    for (int d = 0; d < 5; ++d) qv[d] = bp[72 + (d < 4 ? d : (half ? 0 : 4))];
#pragma unroll
    for (int w = 0; w < 8; ++w)
#pragma unroll
      for (int d = 0; d < 5; ++d) kv[w][d] = bp[d - w * (2 * K1_QS / 4)];
#pragma unroll
    for (int w = 0; w < 8; ++w)
#pragma unroll
      for (int d = 0; d < 5; ++d) vv[w][d] = bp[36 + d - w * (2 * K1_QS / 4)];
    __builtin_amdgcn_sched_barrier(0);
    if (half) qv[4] = 0u;
    float lg[8];
#pragma unroll
    for (int w = 0; w < 8; ++w) {
      float s = 0.f;
#pragma unroll
      for (int d = 0; d < 5; ++d) s = dot2bf(qv[d], kv[w][d], s);
      s += dpp<0xB1>(s);
      lg[w] = (gpos - 2 * w >= 0) ? s * (0.23570226039551584f * 1.4426950408889634f) : -1e30f;
    }
    float mx = lg[0];
#pragma unroll
    for (int w = 1; w < 8; ++w) mx = fmaxf(mx, lg[w]);
    float den = 0.f;
#pragma unroll
    for (int w = 0; w < 8; ++w) { lg[w] = __builtin_amdgcn_exp2f(lg[w] - mx); den += lg[w]; }
    const float inv = __builtin_amdgcn_rcpf(den);
    f32x2 ov2[5];
#pragma unroll
    for (int d = 0; d < 5; ++d) ov2[d] = (f32x2){0.f, 0.f};
#pragma unroll
    for (int w = 0; w < 8; ++w) {
      const f32x2 pw = {lg[w], lg[w]};
#pragma unroll
      for (int d = 0; d < 5; ++d) ov2[d] += pw * (f32x2){bflo(vv[w][d]), bfhi(vv[w][d])};
    }
    float ov[10];
#pragma unroll
    for (int d = 0; d < 5; ++d) { ov[2 * d] = ov2[d].x * inv; ov[2 * d + 1] = ov2[d].y * inv; }
    LAS unsigned* op = (LAS unsigned*)(lds + K1_OB + tok * K1_OS + hh * 36 + half * 20);
#pragma unroll
    for (int d = 0; d < 5; ++d) if (d < 4 || !half) op[d] = pk2(ov[2 * d], ov[2 * d + 1]);
  }
  RSTAMP(6);
  asm volatile("s_waitcnt vmcnt(%0) lgkmcnt(0)" ::"n"(6 + NPF) : "memory");
  BAR();
  RSTAMP(7);
  {
    const __amdgpu_buffer_rsrc_t rso = __builtin_amdgcn_make_buffer_rsrc((void*)OB, 0, S_ * D_ * 2, 0x00020000);
#pragma unroll
    for (int i = 0; i < 2; ++i) {
      const int ci = i * 512 + tid;
      if (ci < 576) {
        const int r = ci & 31, q = ci >> 5, hf = q / 9, cc = q - hf * 9;
        const u32x4 v = *(const LAS u32x4*)(lds + K1_OB + (hf * 32 + r) * K1_OS + cc * 16);
        __builtin_amdgcn_raw_buffer_store_b128(v, rso, (((tt * 2 + hf) * 72 + hg * 9 + cc) * 32 + r) * 16, 0, 16);
      }
    }
  }
  const int prow = tid >> 4, sub = tid & 15;
  const size_t gofs = (size_t)(tt * 64 + th * 32 + prow) * D_ + ct * 144;
  u32x4 rv[2];
  rv[0] = *(const u32x4*)(X + gofs + sub * 8);
  rv[1] = *(const u32x4*)(X + gofs + (sub < 2 ? 16 + sub : sub) * 8);
  {
    const int wm = wid & 1, wk = (wid >> 1) & 1;
    const int obase = (tt * 2 + th) * 72 * 32 * 16;
    if (wid < 4) k2_gemm<5, 0>(lds, lane, wm, wk, (const char*)Wo, w2off, w2dst, OB, obase, cnt + tt * 32, tid, wid);
    else k2_gemm<4, 5>(lds, lane, wm, wk, (const char*)Wo, w2off, w2dst, OB, obase, cnt + tt * 32, tid, wid);
  }
  WAIT_L0(); BAR();
  RSTAMP(15);
  {
    const LAS float* rt = (const LAS float*)(lds + K2_RT);
    const f32x2 d0 = *(const LAS f32x2*)(rt + prow * 148 + 144), d1 = *(const LAS f32x2*)(rt + (32 + prow) * 148 + 144);
    const float mean1 = rbf(((const LAS float*)(lds + K1_MEAN))[16 + th * 32 + prow] + ((d0.x + d0.y) + (d1.x + d1.y)));
    u32x4 xo[2];
#pragma unroll
    for (int c = 0; c < 2; ++c) {
      const bool ok = c == 0 || sub < 2;
      const int ch = ok ? c * 16 + sub : sub;
      const LAS float* p0 = rt + prow * 148 + ch * 8;
      const f32x4 a0 = *(const LAS f32x4*)p0 + *(const LAS f32x4*)(p0 + 32 * 148);
      const f32x4 a1 = *(const LAS f32x4*)(p0 + 4) + *(const LAS f32x4*)(p0 + 32 * 148 + 4);
      const u32x4 y = {pk2(a0[0], a0[1]), pk2(a0[2], a0[3]), pk2(a1[0], a1[1]), pk2(a1[2], a1[3])};
#pragma unroll
      for (int e = 0; e < 4; ++e) xo[c][e] = pk2(bflo(rv[c][e]) + bflo(y[e]), bfhi(rv[c][e]) + bfhi(y[e]));
      u32x4 xc;
#pragma unroll
      for (int e = 0; e < 4; ++e) xc[e] = pk2(bflo(xo[c][e]) - mean1, bfhi(xo[c][e]) - mean1);
      if (ok) { *(u32x4*)(YC + gofs + ch * 8) = xc; *(u32x4*)(Y + gofs + ch * 8) = xo[c]; }
    }
    row_stats(xo[0], xo[1], sub, stats + (tt * 64 + th * 32 + prow) * 4 + ct);
  }
  RSTAMP(16);
  STAMPEND(1, 1);
  RSTAMP(17);
  RDUMP(18);
}

constexpr int K3_XS = 32768, K3_UOFF = 9216 + 128, K3_WS = K3_UOFF + 9216;
constexpr int K3_WBASE = 2 * K3_XS, K3_AUX = K3_WBASE + 4 * K3_WS;
constexpr int K3_DUMMY = K3_AUX + 8192;
constexpr int K3_LDS = K3_DUMMY + 4096;

template <int NJ, int J0>
DEVI void k3_body(LAS char* lds, const char* X, const char* wsrc, bf16_t* __restrict__ H, const unsigned (&xoff)[4], const unsigned (&woff)[3],
                  unsigned xdst, unsigned wdst, int lane, int wr, int pm, int pf, const char* Wd, unsigned pfoff) {
  auto stageX = [&](int t) { glds_4(X + t * 128, xoff[0], xoff[1], xoff[2], xoff[3], xdst + (t & 1) * K3_XS); };
  auto stageW = [&](int s) { glds_2q(wsrc + (size_t)s * 32 * F_ * 2, woff[0], woff[1], woff[2], wdst + (s & 3) * K3_WS); };
  const int tk = lane & 31, h = lane >> 5, q4 = (lane >> 2) & 3, p4 = lane & 3, isup = (lane >> 4) & 1;
  const int xa = (wr * 64 + tk) * 128 + ((h ^ ((tk >> 1) & 7)) * 16);
  const int wa = K3_WBASE + isup * K3_UOFF + (h * 8 + q4) * 288 + J0 * 32 + p4 * 8;
  auto rdW = [&](int wo, int kk, int j) -> bf16x8 {
    const LAS char* p = lds + (wa + wo + kk * (16 * 288) + j * 32);
    return lds_tr2(p, p + 4 * 288);
  };
  auto rdX = [&](int xo, int k4, int i) -> bf16x8 {
    return lds_b128(lds + ((xa ^ (k4 * 32)) + xo + i * 4096));
  };
  f32x16 acc[2][NJ];
#pragma unroll
  for (int i = 0; i < 2; ++i)
#pragma unroll
    for (int j = 0; j < NJ; ++j)
#pragma unroll
      for (int r = 0; r < 16; ++r) acc[i][j][r] = 0.f;
  bf16x8 xfa[2], xfb[2], xfc[2], wf[4];
  WAIT_V(4); BAR();
  STAMP(1);
  xfa[0] = rdX(0, 0, 0); xfa[1] = rdX(0, 0, 1);
  wf[0] = rdW(0, 0, 0); wf[1] = rdW(0, 0, 1);
  stageX(1); stageW(2);

  if (PRIO == 1 && NJ == 5) __builtin_amdgcn_s_setprio(1);
  if (PRIO == 2 && NJ == 4) __builtin_amdgcn_s_setprio(1);
#pragma unroll 1
  for (int t = 0; t < 9; ++t) {
    const int tp = t & 1;
    const int wo = tp * (2 * K3_WS), won = (1 - tp) * (2 * K3_WS);
    const int xo = tp * K3_XS, xon = (1 - tp) * K3_XS;
#pragma unroll
    for (int k4 = 0; k4 < 4; ++k4) {
      if (k4 == 0) {
        if (t < 8) { WAIT_V(7); }
        ABL_B(BAR());
      }
      if (k4 == 2) {
        if (t < 7) { WAIT_V(3); } else if (t == 7) { WAIT_V(0); }
        WAIT_L0();
        ABL_B(BAR());
        if (t == 7) {
          glds_4(Wd, pfoff, pfoff, pfoff, pfoff, ldsa(lds) + K3_DUMMY);
          glds_2(Wd + 3136, pfoff, pfoff, ldsa(lds) + K3_DUMMY);
        }
      }
      __builtin_amdgcn_sched_barrier(0);
#pragma unroll
      for (int j = 0; j < NJ; ++j) {
        const int n = k4 * NJ + j;
        if (j == 0) {
          if (k4 == (NJ == 4 ? 0 : 1) && t < 8) stageW(2 * t + 3);
          if (k4 == (NJ == 4 ? 2 : 3) && t < 7) { stageW(2 * t + 4); stageX(t + 2); }
        }
        {
          const int n2 = n + 2, k42 = n2 / NJ, j2 = n2 % NJ;
          if (k42 < 4) { ABL_L(wf[n2 & 3] = rdW(wo + (k42 >> 1) * K3_WS, k42 & 1, j2);) }
          else { ABL_L(wf[n2 & 3] = rdW(won, 0, j2);) }
        }
        if (j == 1) {
          if (k4 == 0) { ABL_L(xfb[0] = rdX(xo, 1, 0); xfb[1] = rdX(xo, 1, 1);) }
          if (k4 == 1) { ABL_L(xfa[0] = rdX(xo, 2, 0); xfa[1] = rdX(xo, 2, 1);) }
          if (k4 == 3) { ABL_L(xfa[0] = rdX(xon, 0, 0); xfa[1] = rdX(xon, 0, 1);) }
        }
        if (j == 2 && k4 == 1) { ABL_L(xfc[0] = rdX(xo, 3, 0); xfc[1] = rdX(xo, 3, 1);) }
#pragma unroll
        for (int i = 0; i < 2; ++i)
          acc[i][j] = __builtin_amdgcn_mfma_f32_32x32x16_bf16(wf[n & 3], k4 == 1 ? xfb[i] : (k4 == 3 ? xfc[i] : xfa[i]), acc[i][j], 0, 0, 0);
        __builtin_amdgcn_sched_barrier(0);
      }
    }
  }
  STAMP(2);
  float rs[2];
#pragma unroll
  for (int i = 0; i < 2; ++i) {
    const LAS f32x4* sp = (const LAS f32x4*)(lds + K3_AUX + (wr * 64 + i * 32 + tk) * 32);
    const f32x4 s01 = sp[0], s23 = sp[1];
    const float mean = (s01[0] + s01[2] + s23[0] + s23[2]) * 0.25f;
    const float d0 = s01[0] - mean, d1 = s01[2] - mean, d2 = s23[0] - mean, d3 = s23[2] - mean;
    const float m2 = (s01[1] + s01[3]) + (s23[1] + s23[3]) + 144.0f * ((d0 * d0 + d1 * d1) + (d2 * d2 + d3 * d3));
    { const float den = rbf(__builtin_amdgcn_sqrtf(rbf(rbf(m2 * (1.0f / D_)) + LN_EPS))); rs[i] = den * den; }
  }
  const __amdgpu_buffer_rsrc_t rsh = __builtin_amdgcn_make_buffer_rsrc((void*)H, 0, S_ * F_ * 2, 0x00020000);
  const int hofs = (((pf * 9 + J0) * 2 + h) * S_ + pm * 256 + wr * 64 + tk) * 16;
#pragma unroll
  for (int i = 0; i < 2; ++i) {
    const float c1 = -1.4426950408889634f * __builtin_amdgcn_rsqf(rs[i]);
    const f32x2 c1v = {c1, c1}, inv = {rs[i], rs[i]};
#pragma unroll
    for (int j = 0; j < NJ; ++j) {
      u32x4 hw;
#pragma unroll
      for (int r = 0; r < 4; ++r) {
        const f32x2 ag = {acc[i][j][2 * r], acc[i][j][2 * r + 1]}, au = {acc[i][j][8 + 2 * r], acc[i][j][8 + 2 * r + 1]};
        const f32x2 z = ag * c1v;
        const f32x2 e = {__builtin_amdgcn_exp2f(z.x), __builtin_amdgcn_exp2f(z.y)};
        const f32x2 d = e * inv + inv;
        const f32x2 rc = {__builtin_amdgcn_rcpf(d.x), __builtin_amdgcn_rcpf(d.y)};
        const f32x2 hv = (ag * au) * rc;
        hw[r] = pk2(hv.x, hv.y);
      }
      __builtin_amdgcn_raw_buffer_store_b128(hw, rsh, hofs + j * (2 * S_ * 16) + i * 512, 0, HAUX);
    }
  }
  STAMP(3);
}

__global__ __launch_bounds__(512, 2) void k3_gateup(const bf16_t* __restrict__ X, const bf16_t* __restrict__ Wg, const bf16_t* __restrict__ Wu,
                                                     const f32x2* __restrict__ stats, bf16_t* __restrict__ H,
                                                     const bf16_t* __restrict__ Wd) {
  __shared__ __attribute__((aligned(16))) char lds_[K3_LDS];
  LAS char* lds = (LAS char*)lds_;
  const unsigned lbase = ldsa(lds);
  const int tid = threadIdx.x, lane = tid & 63, wid = __builtin_amdgcn_readfirstlane(tid >> 6);
  const int bid = blockIdx.x, xcd = bid & 7, idx = bid >> 3;
  const int pm = (xcd >> 2) * 4 + (idx & 3), pf = (xcd & 3) * 8 + (idx >> 2);
  const int wr = wid & 3, wc = wid >> 2;
  STAMP(0);
  unsigned xoff[4], woff[3];
#pragma unroll
  for (int i = 0; i < 4; ++i) {
    const int row = (wid * 4 + i) * 8 + (lane >> 3);
    const int c = (lane & 7) ^ ((row >> 1) & 7);
    xoff[i] = (unsigned)(((pm * 256 + row) * D_ + c * 8) * 2 - i * 1024);
  }
  const unsigned xdst = lbase + wid * 4096;
  glds_4((const char*)X, xoff[0], xoff[1], xoff[2], xoff[3], xdst);
#pragma unroll
  for (int i = 0; i < 3; ++i) {
    int ci = i * 64 + lane; if (ci > 143) ci = 143;
    const int kk = ci / 18, cp = ci - kk * 18;
    woff[i] = (unsigned)((((wid & 3) * 8 + kk) * F_ + pf * 144 + cp * 8) * 2 - i * 1024);
  }
  const char* wsrc = (const char*)((wid >> 2) ? Wu : Wg);
  const unsigned wdst = lbase + K3_WBASE + (wid >> 2) * K3_UOFF + (wid & 3) * 2304;
  glds_2q(wsrc, woff[0], woff[1], woff[2], wdst);
  glds_2q(wsrc + 32 * F_ * 2, woff[0], woff[1], woff[2], wdst + K3_WS);
  glds16((const char*)(stats + pm * 256 * 4), (unsigned)(tid * 16), lbase + K3_AUX + wid * 1024);
  const unsigned pfoff = (unsigned)((xcd & 3) * (1152 * D_ * 2) + (idx * 8 + wid) * 5184 + lane * 16);
  if (wc == 0) k3_body<5, 0>(lds, (const char*)X, wsrc, H, xoff, woff, xdst, wdst, lane, wr, pm, pf, (const char*)Wd, pfoff);
  else k3_body<4, 5>(lds, (const char*)X, wsrc, H, xoff, woff, xdst, wdst, lane, wr, pm, pf, (const char*)Wd, pfoff);
  STAMPEND(3, 4);
}

constexpr int K4_XB = 16384, K4_STG = K4_XB + 18432, K4_LDS = 4 * K4_STG;

template <int NJ, int J0>
DEVI void k4_body(LAS char* lds, const char* X, const char* W, unsigned xoff, const unsigned (&woff)[3], unsigned sdst, unsigned wdst,
                  int lane, int wm, int split, int tile, float* __restrict__ P, unsigned* __restrict__ cnt) {
  auto stage = [&](int kt, int buf) {
    glds_2(X + (size_t)kt * (8 * S_ * 16), xoff, xoff, sdst + buf * K4_STG);
    glds_2q(W + (size_t)kt * 64 * D_ * 2, woff[0], woff[1], woff[2], wdst + buf * K4_STG);
  };
  const int g = lane >> 4, m = lane & 15, q4 = (lane >> 2) & 3, p4 = lane & 3;
  const int xa = g * 2048 + (wm * 32 + m) * 16;
  const int wa = K4_XB + ((g >> 1) * 16 + (g & 1) * 4 + q4) * 288 + J0 * 32 + p4 * 8;
  f32x4 acc[2][NJ];
#pragma unroll
  for (int i = 0; i < 2; ++i)
#pragma unroll
    for (int j = 0; j < NJ; ++j) acc[i][j] = (f32x4){0, 0, 0, 0};
  bf16x8 xa_[2], wa_[NJ], xb_[2], wb_[NJ];
  auto rd = [&](bf16x8 (&xf)[2], bf16x8 (&wf)[NJ], const LAS char* sb, int ks) {
#pragma unroll
    for (int i = 0; i < 2; ++i) xf[i] = lds_b128(sb + xa + ks * 8192 + i * 256);
#pragma unroll
    for (int j = 0; j < NJ; ++j) wf[j] = lds_tr2(sb + wa + ks * (32 * 288) + j * 32, sb + wa + ks * (32 * 288) + j * 32 + 8 * 288);
  };
  auto step = [&](bf16x8 (&xc)[2], bf16x8 (&wc)[NJ], bf16x8 (&xn)[2], bf16x8 (&wn)[NJ], const LAS char* sb, int ks) {
#pragma unroll
    for (int j = 0; j < NJ; ++j) {
      if (j < 2) xn[j] = lds_b128(sb + xa + ks * 8192 + j * 256);
      wn[j] = lds_tr2(sb + wa + ks * (32 * 288) + j * 32, sb + wa + ks * (32 * 288) + j * 32 + 8 * 288);
#pragma unroll
      for (int i = 0; i < 2; ++i) acc[i][j] = mfma16(wc[j], xc[i], acc[i][j]);
      __builtin_amdgcn_sched_barrier(0);
    }
  };
  WAIT_V(5); BAR();
  STAMPK(4, 1);
  rd(xa_, wa_, lds, 0);
  stage(2, 2);
  step(xa_, wa_, xb_, wb_, lds, 1);
  stage(3, 3);
  auto iter = [&](auto INFL, auto MORE, int t) {
    asm volatile("s_waitcnt vmcnt(%0) lgkmcnt(0)" ::"n"(decltype(INFL)::value * 5) : "memory");
    BAR();
    __builtin_amdgcn_sched_barrier(0);
    const LAS char* sn = lds + ((t + 1) & 3) * K4_STG;
    if (decltype(MORE)::value && NJ == 4) stage(t + 4, t & 3);
    step(xb_, wb_, xa_, wa_, sn, 0);
    if (decltype(MORE)::value && NJ == 5) stage(t + 4, t & 3);
    step(xa_, wa_, xb_, wb_, sn, 1);
  };
#pragma unroll 1
  for (int t = 0; t < 14; ++t) iter(IC<2>{}, IC<1>{}, t);
  iter(IC<2>{}, IC<0>{}, 14);
  iter(IC<1>{}, IC<0>{}, 15);
  iter(IC<0>{}, IC<0>{}, 16);
#pragma unroll
  for (int j = 0; j < NJ; ++j)
#pragma unroll
    for (int i = 0; i < 2; ++i) acc[i][j] = mfma16(wb_[j], xb_[i], acc[i][j]);
  STAMPK(4, 2);
  const __amdgpu_buffer_rsrc_t rs = __builtin_amdgcn_make_buffer_rsrc((void*)P, 0, 64 * 16 * 9 * 1024, 0x00020000);
  if (wm != split) {
    const int sb = ((tile * 4 + wm) * 4 + split) * 9 * 1024 + lane * 16;
#pragma unroll
    for (int j = 0; j < NJ; ++j) {
      const u32x4 v = {pk2(acc[0][j][0], acc[0][j][1]), pk2(acc[0][j][2], acc[0][j][3]), pk2(acc[1][j][0], acc[1][j][1]), pk2(acc[1][j][2], acc[1][j][3])};
      __builtin_amdgcn_raw_buffer_store_b128(v, rs, sb + (J0 + j) * 1024, 0, 16);
    }
    WAIT_V(0);
    if (lane == 0) __hip_atomic_fetch_add(cnt + (tile * 4 + wm) * 32, 1u, __ATOMIC_RELAXED, __HIP_MEMORY_SCOPE_AGENT);
  } else {
    LAS f32x4* own = (LAS f32x4*)(lds + 2 * K4_STG);
#pragma unroll
    for (int i = 0; i < 2; ++i)
#pragma unroll
      for (int j = 0; j < NJ; ++j) own[(i * 9 + J0 + j) * 64 + lane] = acc[i][j];
    if (NJ == 5) {
      unsigned spins = 0;
      while ((unsigned)__builtin_amdgcn_readfirstlane(__hip_atomic_load(cnt + (tile * 4 + split) * 32, __ATOMIC_RELAXED, __HIP_MEMORY_SCOPE_AGENT)) < 6u) {
        __builtin_amdgcn_s_sleep(1);
        if (++spins > (1u << 22)) break;
      }
    }
  }
}

__global__ __launch_bounds__(512, 2) void k4_down(const bf16_t* __restrict__ X, const bf16_t* __restrict__ W, const bf16_t* __restrict__ R,
                                                   bf16_t* __restrict__ Y, unsigned* __restrict__ cnt, float* __restrict__ P, f32x2* __restrict__ xstats) {
  __shared__ __attribute__((aligned(16))) char lds_[K4_LDS];
  LAS char* lds = (LAS char*)lds_;
  const unsigned lbase = ldsa(lds);
  const int tid = threadIdx.x, lane = tid & 63, wid = __builtin_amdgcn_readfirstlane(tid >> 6);
  const int bid = blockIdx.x, xcd = bid & 7, idx = bid >> 3;
  const int split = xcd & 3;
  const int m0 = (xcd >> 2) * 1024 + (idx >> 2) * 128, n0 = (idx & 3) * 144, k0 = split * 1152;
  const int wm = wid & 3, wn = wid >> 2;
  STAMPK(4, 0);
  const unsigned xoff = (unsigned)((((k0 >> 3) + wid) * S_ + m0 + lane) * 16);
  unsigned woff[3];
#pragma unroll
  for (int i = 0; i < 3; ++i) {
    int ci = i * 64 + lane; if (ci > 143) ci = 143;
    const int rr = ci / 18, c = ci - rr * 18;
    woff[i] = (unsigned)(((k0 + wid * 8 + rr) * D_ + n0 + c * 8) * 2 - i * 1024);
  }
  const unsigned sdst = lbase + wid * 2048, wdst = lbase + K4_XB + wid * 2304;
#pragma unroll
  for (int s = 0; s < 2; ++s) {
    glds_2((const char*)X + (size_t)s * (8 * S_ * 16), xoff, xoff, sdst + s * K4_STG);
    glds_2q((const char*)W + (size_t)s * 64 * D_ * 2, woff[0], woff[1], woff[2], wdst + s * K4_STG);
  }
  const int tile = (m0 >> 7) * 4 + (idx & 3);
  if (wn == 0) k4_body<5, 0>(lds, (const char*)X, (const char*)W, xoff, woff, sdst, wdst, lane, wm, split, tile, P, cnt);
  else k4_body<4, 5>(lds, (const char*)X, (const char*)W, xoff, woff, sdst, wdst, lane, wm, split, tile, P, cnt);
  const int row = tid >> 4, sub = tid & 15;
  const size_t gofs = (size_t)(m0 + split * 32 + row) * D_ + n0;
  u32x4 rv[2];
  rv[0] = *(const u32x4*)(R + gofs + sub * 8);
  rv[1] = *(const u32x4*)(R + gofs + (sub < 2 ? 16 + sub : sub) * 8);
  WAIT_L0(); BAR();
  STAMPK(4, 3);
  const int g = lane >> 4, m = lane & 15;
  LAS f32x4* own = (LAS f32x4*)(lds + 2 * K4_STG);
  LAS float* rt = (LAS float*)lds;
  {
    const __amdgpu_buffer_rsrc_t rs = __builtin_amdgcn_make_buffer_rsrc((void*)P, 0, 64 * 16 * 9 * 1024, 0x00020000);
    u32x4 pv[2][3];
#pragma unroll
    for (int n = 0; n < 2; ++n) {
      const int j = wid + 8 * n < 9 ? wid + 8 * n : wid;
#pragma unroll
      for (int sx = 0; sx < 3; ++sx) {
        const int src = sx + (sx >= split);
        pv[n][sx] = __builtin_amdgcn_raw_buffer_load_b128(rs, (((tile * 4 + split) * 4 + src) * 9 + j) * 1024 + lane * 16, 0, 16);
      }
    }
#pragma unroll
    for (int n = 0; n < 2; ++n) {
      const int j = wid + 8 * n;
      if (j < 9) {
#pragma unroll
        for (int i = 0; i < 2; ++i) {
          f32x4 v = own[(i * 9 + j) * 64 + lane];
#pragma unroll
          for (int sx = 0; sx < 3; ++sx)
            v += (f32x4){bflo(pv[n][sx][2 * i]), bfhi(pv[n][sx][2 * i]), bflo(pv[n][sx][2 * i + 1]), bfhi(pv[n][sx][2 * i + 1])};
          *(LAS f32x4*)(rt + (i * 16 + m) * 148 + j * 16 + g * 4) = v;
        }
      }
    }
  }
  WAIT_L0(); BAR();
  {
    u32x4 xo[2];
#pragma unroll
    for (int c = 0; c < 2; ++c) {
      const bool ok = c == 0 || sub < 2;
      const int ch = ok ? c * 16 + sub : sub;
      const LAS float* p0 = rt + row * 148 + ch * 8;
      const f32x4 a0 = *(const LAS f32x4*)p0, a1 = *(const LAS f32x4*)(p0 + 4);
      const u32x4 y = {pk2(a0[0], a0[1]), pk2(a0[2], a0[3]), pk2(a1[0], a1[1]), pk2(a1[2], a1[3])};
#pragma unroll
      for (int e = 0; e < 4; ++e) xo[c][e] = pk2(bflo(rv[c][e]) + bflo(y[e]), bfhi(rv[c][e]) + bfhi(y[e]));
      if (ok) *(u32x4*)(Y + gofs + ch * 8) = xo[c];
    }
    row_stats(xo[0], xo[1], sub, xstats + (m0 + split * 32 + row) * 4 + (idx & 3));
  }
  STAMPEND(4, 4);
  TOT1();
}

extern "C" __global__ void k9_end() {}

extern "C" void kernel_launch(void* const* d_in, const int* in_sizes, int n_in,
                              void* d_out, int out_size, void* d_ws, size_t ws_size,
                              hipStream_t stream) {
  const bf16_t* x0 = (const bf16_t*)d_in[0];
  const bf16_t* wqkv = (const bf16_t*)d_in[1];
  const bf16_t* wo = (const bf16_t*)d_in[2];
  const bf16_t* wg = (const bf16_t*)d_in[4];
  const bf16_t* wu = (const bf16_t*)d_in[5];
  const bf16_t* wd = (const bf16_t*)d_in[6];
  bf16_t* out = (bf16_t*)d_out;
  char* ws = (char*)d_ws;
  constexpr size_t XB = (size_t)S_ * D_ * 2, HB = (size_t)S_ * F_ * 2, PB = (size_t)4 * S_ * D_ * 4;
  size_t off = 0;
  bf16_t* wbar = (bf16_t*)(ws + off); off += 4096;
  unsigned* cnt = (unsigned*)(ws + off); off += NCNT * 128;
  char* wp = ws + off; off += (size_t)144 * K1_WS;
  const bf16_t* xin = x0;
  char* o = ws + off; off += XB;
  bf16_t* x1 = (bf16_t*)(ws + off); off += XB;
  bf16_t* x1c = (bf16_t*)(ws + off); off += XB;
  f32x2* stats = (f32x2*)(ws + off); off += 65536;
  f32x2* xstats = (f32x2*)(ws + off); off += 65536;
  bf16_t* xm = (bf16_t*)(ws + off); off += XB;
  bf16_t* h = (bf16_t*)(ws + off); off += HB;
  float* P = (float*)(ws + off); off += PB;
  k0_prep<<<144 + 512 + 18, 256, 0, stream>>>(wqkv, wp, cnt, x0, wo, xstats, wbar);
  for (int l = 0; l < 2; ++l) {
    bf16_t* x2 = (l == 1) ? out : xm;
    k1_qkv_attn<<<256, 512, 0, stream>>>(xin, wp, o, wo, x1, stats, cnt + (l * 512 + 256) * 32, wbar, x1c, xstats, wg, wu);
    k3_gateup<<<256, 512, 0, stream>>>(x1c, wg, wu, stats, h, wd);
    k4_down<<<256, 512, 0, stream>>>(h, wd, x1, x2, cnt + (l * 512) * 32, P, xstats);
    xin = x2;
  }
}
```

```cpp
#define PRA 0
#define PRB 1
#define AMP 0
#include <hip/hip_runtime.h>
#include <hip/hip_bf16.h>
#include <stdint.h>

typedef unsigned short bf16_t;
typedef short bf16x8 __attribute__((ext_vector_type(8)));
typedef short bf16x4 __attribute__((ext_vector_type(4)));
typedef float f32x4 __attribute__((ext_vector_type(4)));
typedef float f32x2 __attribute__((ext_vector_type(2)));
typedef float f32x16 __attribute__((ext_vector_type(16)));
typedef unsigned u32x4 __attribute__((ext_vector_type(4)));
typedef unsigned u32x2 __attribute__((ext_vector_type(2)));
#define LAS __attribute__((address_space(3)))
#define DEVI __device__ __forceinline__

constexpr int S_ = 2048, D_ = 576, F_ = 4608, NH_ = 32, DH_ = 18;
constexpr float LN_EPS = 1e-5f;

DEVI float bf2f(unsigned b) { return __uint_as_float(b << 16); }
DEVI float bflo(unsigned w) { return __uint_as_float(w << 16); }
DEVI float bfhi(unsigned w) { return __uint_as_float(w & 0xffff0000u); }
DEVI unsigned pk2(float lo, float hi) {
  typedef __bf16 bf2v __attribute__((ext_vector_type(2)));
  bf2v v; v.x = (__bf16)lo; v.y = (__bf16)hi;
  return __builtin_bit_cast(unsigned, v);
}
template <int CTRL> DEVI float dpp(float x) { return __builtin_bit_cast(float, __builtin_amdgcn_update_dpp(0, __builtin_bit_cast(int, x), CTRL, 0xf, 0xf, true)); }
DEVI float sum8(float x) {
  x += dpp<0xB1>(x); x += dpp<0x4E>(x); x += dpp<0x141>(x); return x;
}
DEVI float rbf(float x) { return (float)((__bf16)x); }

#define TOT0()
#define TOT1()
#define RSTAMP(i)
#define RDUMP(n)
#define STAMPW(kid, i, cond)
#define STAMPK(kid, i)
#define STAMPEND(kid, i)
#define STAMP(i) STAMPK(3, i)
#define ABL_G(x) x
#define ABL_B(x) x
#define ABL_L(...) __VA_ARGS__
#define LNMIMIC 1
#define TAILPRIO 0
#define NPF 0
#define HAUX 16
#define GV 3
#define PRIO 0
template <int N> struct IC { static constexpr int value = N; };
template <int A, int B, class F> __device__ __forceinline__ void static_for(F&& f) {
  if constexpr (A < B) { f(IC<A>{}); static_for<A + 1, B>(f); }
}
#define WAIT_V(n) asm volatile("s_waitcnt vmcnt(" #n ")" ::: "memory")
#define WAIT_L0() asm volatile("s_waitcnt lgkmcnt(0)" ::: "memory")
#define BAR() do { asm volatile("" ::: "memory"); __builtin_amdgcn_s_barrier(); asm volatile("" ::: "memory"); } while (0)

DEVI void glds16(const char* sbase, unsigned voff, unsigned ldsaddr) {
  asm volatile("s_mov_b32 m0, %2\n\ts_nop 0\n\tglobal_load_lds_dwordx4 %0, %1" ::"v"(voff), "s"(sbase), "s"(ldsaddr) : "memory");
}
DEVI void glds_1(const char* sb, unsigned v0, unsigned la) { glds16(sb, v0, la); }
DEVI void glds_2(const char* sb, unsigned v0, unsigned v1, unsigned la) {
  asm volatile("s_mov_b32 m0, %3\n\ts_nop 0\n\tglobal_load_lds_dwordx4 %0, %2\n\tglobal_load_lds_dwordx4 %1, %2 offset:1024"
               ::"v"(v0), "v"(v1), "s"(sb), "s"(la) : "memory");
}
DEVI void glds_4(const char* sb, unsigned v0, unsigned v1, unsigned v2, unsigned v3, unsigned la) {
  asm volatile("s_mov_b32 m0, %5\n\ts_nop 0\n\tglobal_load_lds_dwordx4 %0, %4\n\tglobal_load_lds_dwordx4 %1, %4 offset:1024\n\t"
               "global_load_lds_dwordx4 %2, %4 offset:2048\n\tglobal_load_lds_dwordx4 %3, %4 offset:3072"
               ::"v"(v0), "v"(v1), "v"(v2), "v"(v3), "s"(sb), "s"(la) : "memory");
}
DEVI void glds_2q(const char* sb, unsigned v0, unsigned v1, unsigned v2, unsigned la) {
  asm volatile("s_mov_b32 m0, %4\n\ts_nop 0\n\tglobal_load_lds_dwordx4 %0, %3\n\tglobal_load_lds_dwordx4 %1, %3 offset:1024\n\t"
               "s_mov_b64 exec, 0xffff\n\tglobal_load_lds_dwordx4 %2, %3 offset:2048\n\ts_mov_b64 exec, -1"
               ::"v"(v0), "v"(v1), "v"(v2), "s"(sb), "s"(la) : "memory");
}
DEVI void glds_2m(const char* sb, unsigned v0, unsigned v1, unsigned la, unsigned long long m0_, unsigned long long m1_) {
  asm volatile("s_mov_b32 m0, %3\n\ts_mov_b64 exec, %4\n\tglobal_load_lds_dwordx4 %0, %2\n\t"
               "s_mov_b64 exec, %5\n\tglobal_load_lds_dwordx4 %1, %2 offset:1024\n\ts_mov_b64 exec, -1"
               ::"v"(v0), "v"(v1), "s"(sb), "s"(la), "s"(m0_), "s"(m1_) : "memory");
}
DEVI unsigned ldsa(const LAS char* p) { return (unsigned)(size_t)p; }
DEVI bf16x8 lds_b128(const LAS char* p) { return *(const LAS bf16x8*)p; }
DEVI bf16x8 lds_tr2(const LAS char* p0, const LAS char* p1) {
  bf16x4 a = __builtin_amdgcn_ds_read_tr16_b64_v4i16((LAS bf16x4*)p0);
  bf16x4 b = __builtin_amdgcn_ds_read_tr16_b64_v4i16((LAS bf16x4*)p1);
  return __builtin_shufflevector(a, b, 0, 1, 2, 3, 4, 5, 6, 7);
}
DEVI f32x4 mfma16(bf16x8 a, bf16x8 b, f32x4 c) { return __builtin_amdgcn_mfma_f32_16x16x32_bf16(a, b, c, 0, 0, 0); }

constexpr int K1_WR = 16 * 448 + 32;
constexpr int K1_WS = 14464;
constexpr int NCNT = 1024;

DEVI void row_stats(const u32x4& v0, const u32x4& v1, int sub, f32x2* __restrict__ dst) {
  float xv[16];
#pragma unroll
  for (int e = 0; e < 4; ++e) {
    xv[2 * e] = bflo(v0[e]); xv[2 * e + 1] = bfhi(v0[e]);
    xv[8 + 2 * e] = sub < 2 ? bflo(v1[e]) : 0.f; xv[8 + 2 * e + 1] = sub < 2 ? bfhi(v1[e]) : 0.f;
  }
  float s = 0.f;
#pragma unroll
  for (int e = 0; e < 16; ++e) s += xv[e];
  s = sum8(s); s += dpp<0x140>(s);
  const float mean = s * (1.0f / 144.0f);
  float q = 0.f;
#pragma unroll
  for (int e = 0; e < 16; ++e) { const float d = xv[e] - mean; q += (e < 8 || sub < 2) ? d * d : 0.f; }
  q = sum8(q); q += dpp<0x140>(q);
  if (sub == 0) *dst = (f32x2){mean, q};
}

__global__ __launch_bounds__(256) void k0_prep(const bf16_t* __restrict__ Wqkv, char* __restrict__ Wp, unsigned* __restrict__ cnt,
                                                const bf16_t* __restrict__ X, const bf16_t* __restrict__ Wo, f32x2* __restrict__ xstats,
                                                bf16_t* __restrict__ WB) {
  if (blockIdx.x == 0 && threadIdx.x == 0) ((unsigned long long*)WB)[400] = wall_clock64();
  if (blockIdx.x < 8) TOT0();
  const int b = blockIdx.x;
  {
    unsigned long long pc0, pc1;
    asm volatile("s_getpc_b64 %0" : "=s"(pc0));
    asm volatile("s_getpc_b64 s[40:41]\n\ts_add_u32 s40, s40, k9_end@rel32@lo+4\n\ts_addc_u32 s41, s41, k9_end@rel32@hi+12\n\ts_mov_b64 %0, s[40:41]"
                 : "=s"(pc1) : : "s40", "s41", "scc");
    const unsigned long long a = (pc0 & ~15ull) + (unsigned long long)(b >> 3) * 4096 + threadIdx.x * 16;
    if (pc1 > pc0 && a + 16 <= pc1) { const u32x4 v = *(const u32x4*)a; asm volatile("" ::"v"(v)); }
  }
  if (b < 144) {
    const int u = b, hg = u / 18, sl = u - hg * 18;
    char* dst = Wp + (size_t)u * K1_WS;
    u32x4 v[4];
#pragma unroll
    for (int i = 0; i < 4; ++i) {
      const int ci = i * 256 + threadIdx.x;
      const int reg = ci >= 450, cj = ci - reg * 450;
      const int rr = cj / 28;
      int ch = cj - rr * 28; if (ch > 26) ch = 26;
      const int seg = ch / 9, cc = ch - seg * 9;
      const int gcol = (seg == 2 ? 0 : (seg + 1) * D_) + hg * 72 + cc * 8;
      const int rc = rr > 15 ? 15 : rr;
      const int k = sl * 32 + (rc >> 3) * 16 + reg * 8 + (rc & 7);
      v[i] = *(const u32x4*)(Wqkv + (size_t)k * (3 * D_) + gcol);
    }
    if (u < NCNT / 256) cnt[(u * 256 + threadIdx.x) * 32] = 0u;
#pragma unroll
    for (int i = 0; i < 4; ++i) {
      const int ci = i * 256 + threadIdx.x;
      if (ci < 904) *(u32x4*)(dst + ci * 16) = v[i];
    }
  } else if (b < 144 + 512) {
    const int u = (b - 144) * 16 + (threadIdx.x >> 4), sub = threadIdx.x & 15;
    const bf16_t* xr = X + (size_t)(u >> 2) * D_ + (u & 3) * 144;
    const u32x4 v0 = *(const u32x4*)(xr + sub * 8), v1 = *(const u32x4*)(xr + (sub < 2 ? 16 + sub : sub) * 8);
    row_stats(v0, v1, sub, xstats + u);
  } else {
    const int k = (b - 656) * 32 + (threadIdx.x >> 3), sub = threadIdx.x & 7;
    u32x4 v[9];
#pragma unroll
    for (int i = 0; i < 9; ++i) v[i] = *(const u32x4*)(Wo + (size_t)k * D_ + (i * 8 + sub) * 8);
    float t = 0.f;
#pragma unroll
    for (int i = 0; i < 9; ++i)
#pragma unroll
      for (int e = 0; e < 4; ++e) t += bflo(v[i][e]) + bfhi(v[i][e]);
    t = sum8(t) * (1.0f / D_);
    const float hi = rbf(t);
    if (sub < 2) WB[sub * D_ + k] = (bf16_t)(pk2(sub ? t - hi : hi, 0.f) & 0xffffu);
  }
  STAMPEND(0, 1);
}

constexpr int K1_XT = 80 * 128;
constexpr int K1_WBASE = 4 * K1_XT;
constexpr int K1_AUX = 156928;
constexpr int K1_MEAN = K1_AUX + 768;
constexpr int K1_WB = K1_MEAN + 320;
constexpr int K1_DUMMY = K1_WB + 2304;
constexpr int K1_LDS = K1_DUMMY + 1024;
constexpr int K1_QS = 440;
constexpr int K1_OB = 35200, K1_OS = 160;
constexpr int K2_WR = 9216 + 128, K2_WS = K2_WR + 9216, K2_WBASE = 45568;

DEVI float dot2bf(unsigned a, unsigned b, float c) {
  typedef __bf16 bf2v __attribute__((ext_vector_type(2)));
  return __builtin_amdgcn_fdot2_f32_bf16(__builtin_bit_cast(bf2v, a), __builtin_bit_cast(bf2v, b), c, false);
}

constexpr int K2_RT = 44032;

template <int NJ, int J0>
DEVI void k2_gemm(LAS char* lds, int lane, int wm, int wk, const char* Wo, const unsigned (&woff)[3], unsigned wdst,
                  const char* OB, int obase, unsigned* __restrict__ cnt, int tid, int wid) {
  const int g = lane >> 4, m = lane & 15, q4 = (lane >> 2) & 3, p4 = lane & 3;
  const int wa = K2_WBASE + (g & 1) * K2_WR + ((wk * 2 + (g >> 1)) * 8 + q4) * 288 + J0 * 32 + p4 * 8;
  auto rdW = [&](bf16x8 (&wf)[NJ], int slot) {
#pragma unroll
    for (int j = 0; j < NJ; ++j) wf[j] = lds_tr2(lds + wa + slot * K2_WS + j * 32, lds + wa + slot * K2_WS + j * 32 + 4 * 288);
  };
  bf16x8 w[6][NJ];
  rdW(w[0], 2); rdW(w[1], 3); rdW(w[2], 4); rdW(w[3], 5);
  RSTAMP(8);
  WAIT_V(0); WAIT_L0();
  RSTAMP(9);
  BAR();
  if (tid == 0) __hip_atomic_fetch_add(cnt, 1u, __ATOMIC_RELAXED, __HIP_MEMORY_SCOPE_AGENT);
  RSTAMP(10);
#pragma unroll
  for (int t = 6; t < 9; ++t) glds_2q(Wo + (size_t)t * 64 * D_ * 2, woff[0], woff[1], woff[2], wdst + (t - 4) * K2_WS);
  rdW(w[4], 0); rdW(w[5], 1);
  RSTAMP(11);
  if (wid == 0) {
    unsigned spins = 0;
    while ((unsigned)__builtin_amdgcn_readfirstlane(__hip_atomic_load(cnt, __ATOMIC_RELAXED, __HIP_MEMORY_SCOPE_AGENT)) < 8u) {
      __builtin_amdgcn_s_sleep(1);
      if (++spins > (1u << 22)) break;
    }
  }
  WAIT_V(0); WAIT_L0(); BAR();
  RSTAMP(12);
  const __amdgpu_buffer_rsrc_t rso = __builtin_amdgcn_make_buffer_rsrc((void*)OB, 0, S_ * D_ * 2, 0x00020000);
  u32x4 xf[9];
#pragma unroll
  for (int t = 0; t < 9; ++t) xf[t] = __builtin_amdgcn_raw_buffer_load_b128(rso, obase + (wk * 4 + g) * 512 + (wm * 16 + m) * 16 + t * 4096, 0, 16);
  bf16x8 w6[NJ], w7[NJ], w8[NJ];
  rdW(w6, 2); rdW(w7, 3); rdW(w8, 4);
  bf16x8 wb[9];
  if (NJ == 4) {
#pragma unroll
    for (int t = 0; t < 9; ++t) wb[t] = lds_b128(lds + K1_WB + (m & 1) * (D_ * 2) + (wk * 32 + g * 8) * 2 + t * 128);
  }
  RSTAMP(13);
  f32x4 acc[NJ];
#pragma unroll
  for (int j = 0; j < NJ; ++j) acc[j] = (f32x4){0, 0, 0, 0};
#pragma unroll
  for (int t = 0; t < 6; ++t)
#pragma unroll
    for (int j = 0; j < NJ; ++j) acc[j] = mfma16(w[t][j], __builtin_bit_cast(bf16x8, xf[t]), acc[j]);
#pragma unroll
  for (int j = 0; j < NJ; ++j) acc[j] = mfma16(w6[j], __builtin_bit_cast(bf16x8, xf[6]), acc[j]);
#pragma unroll
  for (int j = 0; j < NJ; ++j) acc[j] = mfma16(w7[j], __builtin_bit_cast(bf16x8, xf[7]), acc[j]);
#pragma unroll
  for (int j = 0; j < NJ; ++j) acc[j] = mfma16(w8[j], __builtin_bit_cast(bf16x8, xf[8]), acc[j]);
  RSTAMP(14);
  LAS float* rt = (LAS float*)(lds + K2_RT);
  if (NJ == 4) {
    f32x4 am = {0, 0, 0, 0};
#pragma unroll
    for (int t = 0; t < 9; ++t) am = mfma16(wb[t], __builtin_bit_cast(bf16x8, xf[t]), am);
    if (g == 0) *(LAS f32x4*)(rt + (wk * 32 + wm * 16 + m) * 148 + 144) = am;
  }
#pragma unroll
  for (int j = 0; j < NJ; ++j) *(LAS f32x4*)(rt + (wk * 32 + wm * 16 + m) * 148 + (J0 + j) * 16 + g * 4) = acc[j];
}

__global__ __launch_bounds__(512) void k1_qkv_attn(const bf16_t* __restrict__ X, const char* __restrict__ Wp, char* __restrict__ OB,
                                                       const bf16_t* __restrict__ Wo, bf16_t* __restrict__ Y, f32x2* __restrict__ stats,
                                                       unsigned* __restrict__ cnt, const bf16_t* __restrict__ WB, bf16_t* __restrict__ YC,
                                                       const f32x2* __restrict__ xstats, const bf16_t* __restrict__ Wg, const bf16_t* __restrict__ Wu) {
  __shared__ __attribute__((aligned(16))) char lds_[K1_LDS];
  LAS char* lds = (LAS char*)lds_;
  const unsigned lbase = ldsa(lds);
  const int tid = threadIdx.x, lane = tid & 63, wid = __builtin_amdgcn_readfirstlane(tid >> 6);
  const int bid = blockIdx.x, xcd = bid & 7, idx = bid >> 3;
  const int tt = (xcd >> 1) * 8 + (idx & 7), hg = (xcd & 1) * 4 + (idx >> 3);
  const int t0 = tt * 64 - 16;
  if (blockIdx.x == 0 && threadIdx.x == 0) ((unsigned long long*)WB)[(cnt - (unsigned*)WB) > 20000 ? 404 : 401] = wall_clock64();

  unsigned xoff[2], woff[2];
#pragma unroll
  for (int i = 0; i < 2; ++i) {
    int ci = i * 64 + lane; if (ci > 79) ci = 79;
    ci += wid * 80;
    const int row = ci >> 3, c = (ci & 7) ^ ((row >> 1) & 7);
    int grow = t0 + row; if (grow < 0) grow = 0;
    xoff[i] = (unsigned)((grow * D_ + c * 8) * 2 + 1024 - i * 1024);
  }
  woff[0] = (unsigned)((wid * 113 + lane) * 16); woff[1] = woff[0];
  const unsigned wdst = lbase + K1_WBASE + wid * 1808;
  const unsigned long long xmask = wid == 0 ? ~0xffull : ~0ull;
  auto stageX = [&](int kt) {
    glds_2m((const char*)X + kt * 128 - 1024, xoff[0], xoff[1], lbase + (kt & 3) * K1_XT + wid * 1280, xmask, 0xffffull);
  };
  auto stageW = [&](int s) {
    glds_2m(Wp + (size_t)(hg * 18 + s) * K1_WS, woff[0], woff[1], wdst + (s & 7) * K1_WS, ~0ull, 0x1ffffffffffffull);
  };
  auto stageT = [&](int t) { stageW(2 * t); stageX(t); stageW(2 * t + 1); };
  asm volatile("s_mov_b32 m0, %2\n\ts_mov_b64 exec, 0x3ffff\n\tglobal_load_lds_dwordx4 %0, %1\n\ts_mov_b64 exec, -1"
               ::"v"(wid * 288 + lane * 16), "s"(WB), "s"(lbase + K1_WB + wid * 288) : "memory");
  f32x4 sa[2], sb[2];
#pragma unroll
  for (int i = 0; i < 2; ++i) {
    int gr = t0 + i * 64 + (i ? (lane & 15) : lane); if (gr < 0) gr = 0;
    asm volatile("global_load_dwordx4 %0, %2, %3\n\tglobal_load_dwordx4 %1, %2, %3 offset:16" : "=&v"(sa[i]), "=&v"(sb[i]) : "v"(gr * 32), "s"(xstats) : "memory");
  }
  stageT(0); stageT(1);
  if (tid >= 64 && tid < 64 + 32) {
    const int c = tid - 64;
    *(LAS u32x4*)(lds + (c >> 3) * K1_XT + (c & 7) * 16) = (u32x4){0x3f803f80u, 0x3f803f80u, 0x3f803f80u, 0x3f803f80u};
  }
  const int th = hg >> 2, ct = hg & 3;
  unsigned w2off[3];
#pragma unroll
  for (int i = 0; i < 3; ++i) {
    int ci = i * 64 + lane; if (ci > 143) ci = 143;
    const int rr = ci / 18, c = ci - rr * 18;
    w2off[i] = (unsigned)(((wid * 8 + rr) * D_ + ct * 144 + c * 8) * 2 - i * 1024);
  }
  const unsigned w2dst = lbase + K2_WBASE + (wid & 1) * K2_WR + (wid >> 1) * 2304;
  auto stageB = [&](int kt) { glds_2q((const char*)Wo + (size_t)kt * 64 * D_ * 2, w2off[0], w2off[1], w2off[2], w2dst + ((kt + 2) % 6) * K2_WS); };

  const int g = lane >> 4, m = lane & 15, q4 = (lane >> 2) & 3, p4 = lane & 3;
  const int xa0 = m * 128 + ((g ^ (m >> 1)) * 16);
  const int wa0 = K1_WBASE + (g & 1) * K1_WR + ((g >> 1) * 8 + q4) * 448 + (wid < 7 ? wid : 0) * 64 + p4 * 8;
  f32x4 acc[5][2];
#pragma unroll
  for (int i = 0; i < 5; ++i) { acc[i][0] = (f32x4){0, 0, 0, 0}; acc[i][1] = (f32x4){0, 0, 0, 0}; }
  LAS f32x2* ms = (LAS f32x2*)(lds + K1_AUX);
  auto top = [&](int t) {
    __builtin_amdgcn_sched_barrier(0);
    if (t < 6) { asm volatile("s_waitcnt vmcnt(12) lgkmcnt(0)" ::: "memory"); } else { asm volatile("s_waitcnt vmcnt(6) lgkmcnt(0)" ::: "memory"); }
    BAR();
    __builtin_amdgcn_sched_barrier(0);
  };
  auto stg = [&](int t) {
    if (t < 5) stageT(t + 4);
    if (t == 6) { stageB(0); stageB(1); }
    if (t == 7) { stageB(2); stageB(3); }
  };
  WAIT_V(6); WAIT_L0(); BAR();
  RSTAMP(0);
  auto rdX = [&](bf16x8 (&xf)[5], int kt, int ks) {
#pragma unroll
    for (int i = 0; i < 5; ++i) xf[i] = lds_b128(lds + ((xa0 ^ (ks * 64)) + (kt & 3) * K1_XT + i * 2048));
  };
  auto mainloop = [&](auto EARLY) {
    bf16x8 xfa[5], xfb[5], wfa[2], wfb[2];
    auto rdW = [&](bf16x8 (&wf)[2], int s) {
#pragma unroll
      for (int j = 0; j < 2; ++j) wf[j] = lds_tr2(lds + (wa0 + (s & 7) * K1_WS + j * 32), lds + (wa0 + (s & 7) * K1_WS + j * 32 + 4 * 448));
    };
    auto step = [&](bf16x8 (&wc)[2], bf16x8 (&xc)[5], bf16x8 (&wn)[2], bf16x8 (&xn)[5], int s) {
#pragma unroll
      for (int i = 0; i < 5; ++i) {
        if (i < 2) wn[i] = lds_tr2(lds + (wa0 + (s & 7) * K1_WS + i * 32), lds + (wa0 + (s & 7) * K1_WS + i * 32 + 4 * 448));
        acc[i][0] = mfma16(wc[0], xc[i], acc[i][0]);
        __builtin_amdgcn_sched_barrier(0);
        xn[i] = lds_b128(lds + ((xa0 ^ ((s & 1) * 64)) + ((s >> 1) & 3) * K1_XT + i * 2048));
        acc[i][1] = mfma16(wc[1], xc[i], acc[i][1]);
        __builtin_amdgcn_sched_barrier(0);
      }
    };
    rdX(xfa, 0, 0); rdW(wfa, 0);
    stageT(2);
    step(wfa, xfa, wfb, xfb, 1);
    stageT(3);
#pragma unroll
    for (int t = 0; t < 8; ++t) {
      top(t);
      if (decltype(EARLY)::value) stg(t);
      step(wfb, xfb, wfa, xfa, 2 * t + 2);
      if (!decltype(EARLY)::value) stg(t);
      step(wfa, xfa, wfb, xfb, 2 * t + 3);
    }
#pragma unroll
    for (int i = 0; i < 5; ++i)
#pragma unroll
      for (int j = 0; j < 2; ++j) acc[i][j] = mfma16(wfb[j], xfb[i], acc[i][j]);
  };
  if (wid < 4) {
    mainloop(IC<1>{});
  } else if (wid < 7) {
    mainloop(IC<0>{});
  } else {
    stageT(2); stageT(3);
    asm volatile("" : "+v"(sa[0]), "+v"(sa[1]), "+v"(sb[0]), "+v"(sb[1]));
#pragma unroll
    for (int i = 0; i < 2; ++i) {
      const float mean = (sa[i][0] + sa[i][2] + sb[i][0] + sb[i][2]) * 0.25f;
      const float d0 = sa[i][0] - mean, d1 = sa[i][2] - mean, d2 = sb[i][0] - mean, d3 = sb[i][2] - mean;
      const float var = ((sa[i][1] + sa[i][3]) + (sb[i][1] + sb[i][3]) + 144.0f * ((d0 * d0 + d1 * d1) + (d2 * d2 + d3 * d3))) * (1.0f / D_);
      const float den = rbf(__builtin_amdgcn_sqrtf(rbf(rbf(var) + LN_EPS)));
      if (i == 0 || lane < 16) { ms[i * 64 + lane] = (f32x2){rbf(mean), __builtin_amdgcn_rcpf(den)}; ((LAS float*)(lds + K1_MEAN))[i * 64 + lane] = mean; }
    }
#pragma unroll
    for (int t = 0; t < 8; ++t) { top(t); stg(t); }
  }
  RSTAMP(1);
  WAIT_L0(); BAR();
  RSTAMP(2);
  stageB(4); stageB(5);
#pragma unroll
  for (int i = 0; i < NPF; ++i) {
    const int c = ((((xcd >> 2) * 32 + idx) * 4 + (wid >> 1)) + i * 256) * 64 + lane;
    const int r = c / 144, cc = c - r * 144;
    glds16((const char*)((wid & 1) ? Wu : Wg), (unsigned)(r * (F_ * 2) + (xcd & 3) * 2304 + cc * 16), lbase + K1_DUMMY);
  }
  RSTAMP(3);
  if (wid < 7) {
    f32x2 mr[5];
#pragma unroll
    for (int i = 0; i < 5; ++i) mr[i] = ms[i * 16 + m];
    f32x4 cs[2];
#pragma unroll
    for (int j = 0; j < 2; ++j)
#pragma unroll
      for (int r = 0; r < 4; ++r) cs[j][r] = dpp<0x150>(acc[0][j][r]);
    LAS char* qp = lds + m * K1_QS + (wid * 32 + g * 4) * 2;
    const bool ok1 = wid * 32 + 16 + g * 4 < 216;
#pragma unroll
    for (int i = 0; i < 5; ++i) {
      const float nm = -mr[i].x * mr[i].y;
#pragma unroll
      for (int j = 0; j < 2; ++j) {
        const f32x4 v = acc[i][j] * mr[i].y + cs[j] * nm;
        if (j == 0 || ok1) *(LAS u32x2*)(qp + i * 16 * K1_QS + j * 32) = (u32x2){pk2(v[0], v[1]), pk2(v[2], v[3])};
      }
    }
  }
  RSTAMP(4);
  WAIT_L0(); BAR();
  RSTAMP(5);
  {
    const int half = lane & 1, hh = wid & 3, tok = (wid >> 2) * 32 + (lane >> 1);
    const int row = tok + 16;
    const int gpos = t0 + row;
    const LAS unsigned* bp = (const LAS unsigned*)(lds + row * K1_QS + hh * 36 + half * 20);
    unsigned qv[5], kv[8][5], vv[8][5];
#pragma unroll
    for (int d = 0; d < 5; ++d) qv[d] = bp[72 + (d < 4 ? d : (half ? 0 : 4))];
#pragma unroll
    for (int w = 0; w < 8; ++w)
#pragma unroll
      for (int d = 0; d < 5; ++d) kv[w][d] = bp[d - w * (2 * K1_QS / 4)];
#pragma unroll
    for (int w = 0; w < 8; ++w)
#pragma unroll
      for (int d = 0; d < 5; ++d) vv[w][d] = bp[36 + d - w * (2 * K1_QS / 4)];
    __builtin_amdgcn_sched_barrier(0);
    if (half) qv[4] = 0u;
    float lg[8];
#pragma unroll
    for (int w = 0; w < 8; ++w) {
      float s = 0.f;
#pragma unroll
      for (int d = 0; d < 5; ++d) s = dot2bf(qv[d], kv[w][d], s);
      s += dpp<0xB1>(s);
      lg[w] = (gpos - 2 * w >= 0) ? s * (0.23570226039551584f * 1.4426950408889634f) : -1e30f;
    }
    float mx = lg[0];
#pragma unroll
    for (int w = 1; w < 8; ++w) mx = fmaxf(mx, lg[w]);
    float den = 0.f;
#pragma unroll
    for (int w = 0; w < 8; ++w) { lg[w] = __builtin_amdgcn_exp2f(lg[w] - mx); den += lg[w]; }
    const float inv = __builtin_amdgcn_rcpf(den);
    f32x2 ov2[5];
#pragma unroll
    for (int d = 0; d < 5; ++d) ov2[d] = (f32x2){0.f, 0.f};
#pragma unroll
    for (int w = 0; w < 8; ++w) {
      const f32x2 pw = {lg[w], lg[w]};
#pragma unroll
      for (int d = 0; d < 5; ++d) ov2[d] += pw * (f32x2){bflo(vv[w][d]), bfhi(vv[w][d])};
    }
    float ov[10];
#pragma unroll
    for (int d = 0; d < 5; ++d) { ov[2 * d] = ov2[d].x * inv; ov[2 * d + 1] = ov2[d].y * inv; }
    LAS unsigned* op = (LAS unsigned*)(lds + K1_OB + tok * K1_OS + hh * 36 + half * 20);
#pragma unroll
    for (int d = 0; d < 5; ++d) if (d < 4 || !half) op[d] = pk2(ov[2 * d], ov[2 * d + 1]);
  }
  RSTAMP(6);
  asm volatile("s_waitcnt vmcnt(%0) lgkmcnt(0)" ::"n"(6 + NPF) : "memory");
  BAR();
  RSTAMP(7);
  {
    const __amdgpu_buffer_rsrc_t rso = __builtin_amdgcn_make_buffer_rsrc((void*)OB, 0, S_ * D_ * 2, 0x00020000);
#pragma unroll
    for (int i = 0; i < 2; ++i) {
      const int ci = i * 512 + tid;
      if (ci < 576) {
        const int r = ci & 31, q = ci >> 5, hf = q / 9, cc = q - hf * 9;
        const u32x4 v = *(const LAS u32x4*)(lds + K1_OB + (hf * 32 + r) * K1_OS + cc * 16);
        __builtin_amdgcn_raw_buffer_store_b128(v, rso, (((tt * 2 + hf) * 72 + hg * 9 + cc) * 32 + r) * 16, 0, 16);
      }
    }
  }
  const int prow = tid >> 4, sub = tid & 15;
  const size_t gofs = (size_t)(tt * 64 + th * 32 + prow) * D_ + ct * 144;
  u32x4 rv[2];
  rv[0] = *(const u32x4*)(X + gofs + sub * 8);
  rv[1] = *(const u32x4*)(X + gofs + (sub < 2 ? 16 + sub : sub) * 8);
  {
    const int wm = wid & 1, wk = (wid >> 1) & 1;
    const int obase = (tt * 2 + th) * 72 * 32 * 16;
    if (wid < 4) k2_gemm<5, 0>(lds, lane, wm, wk, (const char*)Wo, w2off, w2dst, OB, obase, cnt + tt * 32, tid, wid);
    else k2_gemm<4, 5>(lds, lane, wm, wk, (const char*)Wo, w2off, w2dst, OB, obase, cnt + tt * 32, tid, wid);
  }
  WAIT_L0(); BAR();
  RSTAMP(15);
  {
    const LAS float* rt = (const LAS float*)(lds + K2_RT);
    const f32x2 d0 = *(const LAS f32x2*)(rt + prow * 148 + 144), d1 = *(const LAS f32x2*)(rt + (32 + prow) * 148 + 144);
    const float mean1 = rbf(((const LAS float*)(lds + K1_MEAN))[16 + th * 32 + prow] + ((d0.x + d0.y) + (d1.x + d1.y)));
    u32x4 xo[2];
#pragma unroll
    for (int c = 0; c < 2; ++c) {
      const bool ok = c == 0 || sub < 2;
      const int ch = ok ? c * 16 + sub : sub;
      const LAS float* p0 = rt + prow * 148 + ch * 8;
      const f32x4 a0 = *(const LAS f32x4*)p0 + *(const LAS f32x4*)(p0 + 32 * 148);
      const f32x4 a1 = *(const LAS f32x4*)(p0 + 4) + *(const LAS f32x4*)(p0 + 32 * 148 + 4);
      const u32x4 y = {pk2(a0[0], a0[1]), pk2(a0[2], a0[3]), pk2(a1[0], a1[1]), pk2(a1[2], a1[3])};
#pragma unroll
      for (int e = 0; e < 4; ++e) xo[c][e] = pk2(bflo(rv[c][e]) + bflo(y[e]), bfhi(rv[c][e]) + bfhi(y[e]));
      u32x4 xc;
#pragma unroll
      for (int e = 0; e < 4; ++e) xc[e] = pk2(bflo(xo[c][e]) - mean1, bfhi(xo[c][e]) - mean1);
      if (ok) { *(u32x4*)(YC + gofs + ch * 8) = xc; *(u32x4*)(Y + gofs + ch * 8) = xo[c]; }
    }
    row_stats(xo[0], xo[1], sub, stats + (tt * 64 + th * 32 + prow) * 4 + ct);
  }
  RSTAMP(16);
  STAMPEND(1, 1);
  RSTAMP(17);
  RDUMP(18);
}

constexpr int K3_XS = 32768, K3_UOFF = 9216 + 128, K3_WS = K3_UOFF + 9216;
constexpr int K3_WBASE = 2 * K3_XS, K3_AUX = K3_WBASE + 4 * K3_WS;
constexpr int K3_DUMMY = K3_AUX + 8192;
constexpr int K3_LDS = K3_DUMMY + 4096;

template <int NJ, int J0>
DEVI void k3_body(LAS char* lds, const char* X, const char* wsrc, bf16_t* __restrict__ H, const unsigned (&xoff)[4], const unsigned (&woff)[3],
                  unsigned xdst, unsigned wdst, int lane, int wr, int pm, int pf, const char* Wd, unsigned pfoff) {
  auto stageX = [&](int t) { glds_4(X + t * 128, xoff[0], xoff[1], xoff[2], xoff[3], xdst + (t & 1) * K3_XS); };
  auto stageW = [&](int s) { glds_2q(wsrc + (size_t)s * 32 * F_ * 2, woff[0], woff[1], woff[2], wdst + (s & 3) * K3_WS); };
  const int tk = lane & 31, h = lane >> 5, q4 = (lane >> 2) & 3, p4 = lane & 3, isup = (lane >> 4) & 1;
  const int xa = (wr * 64 + tk) * 128 + ((h ^ ((tk >> 1) & 7)) * 16);
  const int wa = K3_WBASE + isup * K3_UOFF + (h * 8 + q4) * 288 + J0 * 32 + p4 * 8;
  auto rdW = [&](int wo, int kk, int j) -> bf16x8 {
    const LAS char* p = lds + (wa + wo + kk * (16 * 288) + j * 32);
    return lds_tr2(p, p + 4 * 288);
  };
  auto rdX = [&](int xo, int k4, int i) -> bf16x8 {
    return lds_b128(lds + ((xa ^ (k4 * 32)) + xo + i * 4096));
  };
  f32x16 acc[2][NJ];
#pragma unroll
  for (int i = 0; i < 2; ++i)
#pragma unroll
    for (int j = 0; j < NJ; ++j)
#pragma unroll
      for (int r = 0; r < 16; ++r) acc[i][j][r] = 0.f;
  bf16x8 xfa[2], xfb[2], xfc[2], wf[4];
  WAIT_V(4); BAR();
  STAMP(1);
  xfa[0] = rdX(0, 0, 0); xfa[1] = rdX(0, 0, 1);
  wf[0] = rdW(0, 0, 0); wf[1] = rdW(0, 0, 1);
  stageX(1); stageW(2);

  if (PRIO == 1 && NJ == 5) __builtin_amdgcn_s_setprio(1);
  if (PRIO == 2 && NJ == 4) __builtin_amdgcn_s_setprio(1);
#pragma unroll 1
  for (int t = 0; t < 9; ++t) {
    const int tp = t & 1;
    const int wo = tp * (2 * K3_WS), won = (1 - tp) * (2 * K3_WS);
    const int xo = tp * K3_XS, xon = (1 - tp) * K3_XS;
#pragma unroll
    for (int k4 = 0; k4 < 4; ++k4) {
      if (k4 == 0) {
        if (t < 8) { WAIT_V(7); }
        ABL_B(BAR());
      }
      if (k4 == 2) {
        if (t < 7) { WAIT_V(3); } else if (t == 7) { WAIT_V(0); }
        WAIT_L0();
        ABL_B(BAR());
        if (t == 7) {
          glds_4(Wd, pfoff, pfoff, pfoff, pfoff, ldsa(lds) + K3_DUMMY);
          glds_2(Wd + 3136, pfoff, pfoff, ldsa(lds) + K3_DUMMY);
        }
      }
      __builtin_amdgcn_sched_barrier(0);
#pragma unroll
      for (int j = 0; j < NJ; ++j) {
        const int n = k4 * NJ + j;
        if (j == 0) {
          if (k4 == (NJ == 4 ? 0 : 1) && t < 8) stageW(2 * t + 3);
          if (k4 == (NJ == 4 ? 2 : 3) && t < 7) { stageW(2 * t + 4); stageX(t + 2); }
        }
        {
          const int n2 = n + 2, k42 = n2 / NJ, j2 = n2 % NJ;
          if (k42 < 4) { ABL_L(wf[n2 & 3] = rdW(wo + (k42 >> 1) * K3_WS, k42 & 1, j2);) }
          else { ABL_L(wf[n2 & 3] = rdW(won, 0, j2);) }
        }
        if (j == 1) {
          if (k4 == 0) { ABL_L(xfb[0] = rdX(xo, 1, 0); xfb[1] = rdX(xo, 1, 1);) }
          if (k4 == 1) { ABL_L(xfa[0] = rdX(xo, 2, 0); xfa[1] = rdX(xo, 2, 1);) }
          if (k4 == 3) { ABL_L(xfa[0] = rdX(xon, 0, 0); xfa[1] = rdX(xon, 0, 1);) }
        }
        if (j == 2 && k4 == 1) { ABL_L(xfc[0] = rdX(xo, 3, 0); xfc[1] = rdX(xo, 3, 1);) }
#pragma unroll
        for (int i = 0; i < 2; ++i)
          acc[i][j] = __builtin_amdgcn_mfma_f32_32x32x16_bf16(wf[n & 3], k4 == 1 ? xfb[i] : (k4 == 3 ? xfc[i] : xfa[i]), acc[i][j], 0, 0, 0);
        __builtin_amdgcn_sched_barrier(0);
      }
    }
  }
  STAMP(2);
  float rs[2];
#pragma unroll
  for (int i = 0; i < 2; ++i) {
    const LAS f32x4* sp = (const LAS f32x4*)(lds + K3_AUX + (wr * 64 + i * 32 + tk) * 32);
    const f32x4 s01 = sp[0], s23 = sp[1];
    const float mean = (s01[0] + s01[2] + s23[0] + s23[2]) * 0.25f;
    const float d0 = s01[0] - mean, d1 = s01[2] - mean, d2 = s23[0] - mean, d3 = s23[2] - mean;
    const float m2 = (s01[1] + s01[3]) + (s23[1] + s23[3]) + 144.0f * ((d0 * d0 + d1 * d1) + (d2 * d2 + d3 * d3));
    { const float den = rbf(__builtin_amdgcn_sqrtf(rbf(rbf(m2 * (1.0f / D_)) + LN_EPS))); rs[i] = den * den; }
  }
  const __amdgpu_buffer_rsrc_t rsh = __builtin_amdgcn_make_buffer_rsrc((void*)H, 0, S_ * F_ * 2, 0x00020000);
  const int hofs = (((pf * 9 + J0) * 2 + h) * S_ + pm * 256 + wr * 64 + tk) * 16;
#pragma unroll
  for (int i = 0; i < 2; ++i) {
    const float c1 = -1.4426950408889634f * __builtin_amdgcn_rsqf(rs[i]);
    const f32x2 c1v = {c1, c1}, inv = {rs[i], rs[i]};
#pragma unroll
    for (int j = 0; j < NJ; ++j) {
      u32x4 hw;
#pragma unroll
      for (int r = 0; r < 4; ++r) {
        const f32x2 ag = {acc[i][j][2 * r], acc[i][j][2 * r + 1]}, au = {acc[i][j][8 + 2 * r], acc[i][j][8 + 2 * r + 1]};
        const f32x2 z = ag * c1v;
        const f32x2 e = {__builtin_amdgcn_exp2f(z.x), __builtin_amdgcn_exp2f(z.y)};
        const f32x2 d = e * inv + inv;
        const f32x2 rc = {__builtin_amdgcn_rcpf(d.x), __builtin_amdgcn_rcpf(d.y)};
        const f32x2 hv = (ag * au) * rc;
        hw[r] = pk2(hv.x, hv.y);
      }
      __builtin_amdgcn_raw_buffer_store_b128(hw, rsh, hofs + j * (2 * S_ * 16) + i * 512, 0, HAUX);
    }
  }
  STAMP(3);
}

__global__ __launch_bounds__(512, 2) void k3_gateup(const bf16_t* __restrict__ X, const bf16_t* __restrict__ Wg, const bf16_t* __restrict__ Wu,
                                                     const f32x2* __restrict__ stats, bf16_t* __restrict__ H,
                                                     const bf16_t* __restrict__ Wd, unsigned long long* ST) {
  __shared__ __attribute__((aligned(16))) char lds_[K3_LDS];
  LAS char* lds = (LAS char*)lds_;
  const unsigned lbase = ldsa(lds);
  const int tid = threadIdx.x, lane = tid & 63, wid = __builtin_amdgcn_readfirstlane(tid >> 6);
  const int bid = blockIdx.x, xcd = bid & 7, idx = bid >> 3;
  const int pm = (xcd >> 2) * 4 + (idx & 3), pf = (xcd & 3) * 8 + (idx >> 2);
  const int wr = wid & 3, wc = wid >> 2;
  if (blockIdx.x == 0 && threadIdx.x == 0) ST[0] = wall_clock64();
  unsigned xoff[4], woff[3];
#pragma unroll
  for (int i = 0; i < 4; ++i) {
    const int row = (wid * 4 + i) * 8 + (lane >> 3);
    const int c = (lane & 7) ^ ((row >> 1) & 7);
    xoff[i] = (unsigned)(((pm * 256 + row) * D_ + c * 8) * 2 - i * 1024);
  }
  const unsigned xdst = lbase + wid * 4096;
  glds_4((const char*)X, xoff[0], xoff[1], xoff[2], xoff[3], xdst);
#pragma unroll
  for (int i = 0; i < 3; ++i) {
    int ci = i * 64 + lane; if (ci > 143) ci = 143;
    const int kk = ci / 18, cp = ci - kk * 18;
    woff[i] = (unsigned)((((wid & 3) * 8 + kk) * F_ + pf * 144 + cp * 8) * 2 - i * 1024);
  }
  const char* wsrc = (const char*)((wid >> 2) ? Wu : Wg);
  const unsigned wdst = lbase + K3_WBASE + (wid >> 2) * K3_UOFF + (wid & 3) * 2304;
  glds_2q(wsrc, woff[0], woff[1], woff[2], wdst);
  glds_2q(wsrc + 32 * F_ * 2, woff[0], woff[1], woff[2], wdst + K3_WS);
  glds16((const char*)(stats + pm * 256 * 4), (unsigned)(tid * 16), lbase + K3_AUX + wid * 1024);
  const unsigned pfoff = (unsigned)((xcd & 3) * (1152 * D_ * 2) + (idx * 8 + wid) * 5184 + lane * 16);
  if (wc == 0) k3_body<5, 0>(lds, (const char*)X, wsrc, H, xoff, woff, xdst, wdst, lane, wr, pm, pf, (const char*)Wd, pfoff);
  else k3_body<4, 5>(lds, (const char*)X, wsrc, H, xoff, woff, xdst, wdst, lane, wr, pm, pf, (const char*)Wd, pfoff);
  STAMPEND(3, 4);
}

constexpr int K4_XB = 16384, K4_STG = K4_XB + 18432, K4_LDS = 4 * K4_STG;

template <int NJ, int J0>
DEVI void k4_body(LAS char* lds, const char* X, const char* W, unsigned xoff, const unsigned (&woff)[3], unsigned sdst, unsigned wdst,
                  int lane, int wm, int split, int tile, float* __restrict__ P, unsigned* __restrict__ cnt) {
  auto stage = [&](int kt, int buf) {
    glds_2(X + (size_t)kt * (8 * S_ * 16), xoff, xoff, sdst + buf * K4_STG);
    glds_2q(W + (size_t)kt * 64 * D_ * 2, woff[0], woff[1], woff[2], wdst + buf * K4_STG);
  };
  const int g = lane >> 4, m = lane & 15, q4 = (lane >> 2) & 3, p4 = lane & 3;
  const int xa = g * 2048 + (wm * 32 + m) * 16;
  const int wa = K4_XB + ((g >> 1) * 16 + (g & 1) * 4 + q4) * 288 + J0 * 32 + p4 * 8;
  f32x4 acc[2][NJ];
#pragma unroll
  for (int i = 0; i < 2; ++i)
#pragma unroll
    for (int j = 0; j < NJ; ++j) acc[i][j] = (f32x4){0, 0, 0, 0};
  bf16x8 xa_[2], wa_[NJ], xb_[2], wb_[NJ];
  auto rd = [&](bf16x8 (&xf)[2], bf16x8 (&wf)[NJ], const LAS char* sb, int ks) {
#pragma unroll
    for (int i = 0; i < 2; ++i) xf[i] = lds_b128(sb + xa + ks * 8192 + i * 256);
#pragma unroll
    for (int j = 0; j < NJ; ++j) wf[j] = lds_tr2(sb + wa + ks * (32 * 288) + j * 32, sb + wa + ks * (32 * 288) + j * 32 + 8 * 288);
  };
  auto step = [&](bf16x8 (&xc)[2], bf16x8 (&wc)[NJ], bf16x8 (&xn)[2], bf16x8 (&wn)[NJ], const LAS char* sb, int ks) {
#pragma unroll
    for (int j = 0; j < NJ; ++j) {
      if (j < 2) xn[j] = lds_b128(sb + xa + ks * 8192 + j * 256);
      wn[j] = lds_tr2(sb + wa + ks * (32 * 288) + j * 32, sb + wa + ks * (32 * 288) + j * 32 + 8 * 288);
#pragma unroll
      for (int i = 0; i < 2; ++i) acc[i][j] = mfma16(wc[j], xc[i], acc[i][j]);
      __builtin_amdgcn_sched_barrier(0);
    }
  };
  WAIT_V(5); BAR();
  STAMPK(4, 1);
  rd(xa_, wa_, lds, 0);
  stage(2, 2);
  step(xa_, wa_, xb_, wb_, lds, 1);
  stage(3, 3);
  auto iter = [&](auto INFL, auto MORE, int t) {
    asm volatile("s_waitcnt vmcnt(%0) lgkmcnt(0)" ::"n"(decltype(INFL)::value * 5) : "memory");
    BAR();
    __builtin_amdgcn_sched_barrier(0);
    const LAS char* sn = lds + ((t + 1) & 3) * K4_STG;
    if (decltype(MORE)::value && NJ == 4) stage(t + 4, t & 3);
    step(xb_, wb_, xa_, wa_, sn, 0);
    if (decltype(MORE)::value && NJ == 5) stage(t + 4, t & 3);
    step(xa_, wa_, xb_, wb_, sn, 1);
  };
#pragma unroll 1
  for (int t = 0; t < 14; ++t) iter(IC<2>{}, IC<1>{}, t);
  iter(IC<2>{}, IC<0>{}, 14);
  iter(IC<1>{}, IC<0>{}, 15);
  iter(IC<0>{}, IC<0>{}, 16);
#pragma unroll
  for (int j = 0; j < NJ; ++j)
#pragma unroll
    for (int i = 0; i < 2; ++i) acc[i][j] = mfma16(wb_[j], xb_[i], acc[i][j]);
  STAMPK(4, 2);
  const __amdgpu_buffer_rsrc_t rs = __builtin_amdgcn_make_buffer_rsrc((void*)P, 0, 64 * 16 * 9 * 1024, 0x00020000);
  if (wm != split) {
    const int sb = ((tile * 4 + wm) * 4 + split) * 9 * 1024 + lane * 16;
#pragma unroll
    for (int j = 0; j < NJ; ++j) {
      const u32x4 v = {pk2(acc[0][j][0], acc[0][j][1]), pk2(acc[0][j][2], acc[0][j][3]), pk2(acc[1][j][0], acc[1][j][1]), pk2(acc[1][j][2], acc[1][j][3])};
      __builtin_amdgcn_raw_buffer_store_b128(v, rs, sb + (J0 + j) * 1024, 0, 16);
    }
    WAIT_V(0);
    if (lane == 0) __hip_atomic_fetch_add(cnt + (tile * 4 + wm) * 32, 1u, __ATOMIC_RELAXED, __HIP_MEMORY_SCOPE_AGENT);
  } else {
    LAS f32x4* own = (LAS f32x4*)(lds + 2 * K4_STG);
#pragma unroll
    for (int i = 0; i < 2; ++i)
#pragma unroll
      for (int j = 0; j < NJ; ++j) own[(i * 9 + J0 + j) * 64 + lane] = acc[i][j];
    if (NJ == 5) {
      unsigned spins = 0;
      while ((unsigned)__builtin_amdgcn_readfirstlane(__hip_atomic_load(cnt + (tile * 4 + split) * 32, __ATOMIC_RELAXED, __HIP_MEMORY_SCOPE_AGENT)) < 6u) {
        __builtin_amdgcn_s_sleep(1);
        if (++spins > (1u << 22)) break;
      }
    }
  }
}

__global__ __launch_bounds__(512, 2) void k4_down(const bf16_t* __restrict__ X, const bf16_t* __restrict__ W, const bf16_t* __restrict__ R,
                                                   bf16_t* __restrict__ Y, unsigned* __restrict__ cnt, float* __restrict__ P, f32x2* __restrict__ xstats, unsigned long long* ST) {
  __shared__ __attribute__((aligned(16))) char lds_[K4_LDS];
  LAS char* lds = (LAS char*)lds_;
  const unsigned lbase = ldsa(lds);
  const int tid = threadIdx.x, lane = tid & 63, wid = __builtin_amdgcn_readfirstlane(tid >> 6);
  const int bid = blockIdx.x, xcd = bid & 7, idx = bid >> 3;
  const int split = xcd & 3;
  const int m0 = (xcd >> 2) * 1024 + (idx >> 2) * 128, n0 = (idx & 3) * 144, k0 = split * 1152;
  const int wm = wid & 3, wn = wid >> 2;
  if (blockIdx.x == 0 && threadIdx.x == 0) ST[0] = wall_clock64();
  const unsigned xoff = (unsigned)((((k0 >> 3) + wid) * S_ + m0 + lane) * 16);
  unsigned woff[3];
#pragma unroll
  for (int i = 0; i < 3; ++i) {
    int ci = i * 64 + lane; if (ci > 143) ci = 143;
    const int rr = ci / 18, c = ci - rr * 18;
    woff[i] = (unsigned)(((k0 + wid * 8 + rr) * D_ + n0 + c * 8) * 2 - i * 1024);
  }
  const unsigned sdst = lbase + wid * 2048, wdst = lbase + K4_XB + wid * 2304;
#pragma unroll
  for (int s = 0; s < 2; ++s) {
    glds_2((const char*)X + (size_t)s * (8 * S_ * 16), xoff, xoff, sdst + s * K4_STG);
    glds_2q((const char*)W + (size_t)s * 64 * D_ * 2, woff[0], woff[1], woff[2], wdst + s * K4_STG);
  }
  const int tile = (m0 >> 7) * 4 + (idx & 3);
  if (wn == 0) k4_body<5, 0>(lds, (const char*)X, (const char*)W, xoff, woff, sdst, wdst, lane, wm, split, tile, P, cnt);
  else k4_body<4, 5>(lds, (const char*)X, (const char*)W, xoff, woff, sdst, wdst, lane, wm, split, tile, P, cnt);
  const int row = tid >> 4, sub = tid & 15;
  const size_t gofs = (size_t)(m0 + split * 32 + row) * D_ + n0;
  u32x4 rv[2];
  rv[0] = *(const u32x4*)(R + gofs + sub * 8);
  rv[1] = *(const u32x4*)(R + gofs + (sub < 2 ? 16 + sub : sub) * 8);
  WAIT_L0(); BAR();
  STAMPK(4, 3);
  const int g = lane >> 4, m = lane & 15;
  LAS f32x4* own = (LAS f32x4*)(lds + 2 * K4_STG);
  LAS float* rt = (LAS float*)lds;
  {
    const __amdgpu_buffer_rsrc_t rs = __builtin_amdgcn_make_buffer_rsrc((void*)P, 0, 64 * 16 * 9 * 1024, 0x00020000);
    u32x4 pv[2][3];
#pragma unroll
    for (int n = 0; n < 2; ++n) {
      const int j = wid + 8 * n < 9 ? wid + 8 * n : wid;
#pragma unroll
      for (int sx = 0; sx < 3; ++sx) {
        const int src = sx + (sx >= split);
        pv[n][sx] = __builtin_amdgcn_raw_buffer_load_b128(rs, (((tile * 4 + split) * 4 + src) * 9 + j) * 1024 + lane * 16, 0, 16);
      }
    }
#pragma unroll
    for (int n = 0; n < 2; ++n) {
      const int j = wid + 8 * n;
      if (j < 9) {
#pragma unroll
        for (int i = 0; i < 2; ++i) {
          f32x4 v = own[(i * 9 + j) * 64 + lane];
#pragma unroll
          for (int sx = 0; sx < 3; ++sx)
            v += (f32x4){bflo(pv[n][sx][2 * i]), bfhi(pv[n][sx][2 * i]), bflo(pv[n][sx][2 * i + 1]), bfhi(pv[n][sx][2 * i + 1])};
          *(LAS f32x4*)(rt + (i * 16 + m) * 148 + j * 16 + g * 4) = v;
        }
      }
    }
  }
  WAIT_L0(); BAR();
  {
    u32x4 xo[2];
#pragma unroll
    for (int c = 0; c < 2; ++c) {
      const bool ok = c == 0 || sub < 2;
      const int ch = ok ? c * 16 + sub : sub;
      const LAS float* p0 = rt + row * 148 + ch * 8;
      const f32x4 a0 = *(const LAS f32x4*)p0, a1 = *(const LAS f32x4*)(p0 + 4);
      const u32x4 y = {pk2(a0[0], a0[1]), pk2(a0[2], a0[3]), pk2(a1[0], a1[1]), pk2(a1[2], a1[3])};
#pragma unroll
      for (int e = 0; e < 4; ++e) xo[c][e] = pk2(bflo(rv[c][e]) + bflo(y[e]), bfhi(rv[c][e]) + bfhi(y[e]));
      if (ok) *(u32x4*)(Y + gofs + ch * 8) = xo[c];
    }
    row_stats(xo[0], xo[1], sub, xstats + (m0 + split * 32 + row) * 4 + (idx & 3));
  }
  STAMPEND(4, 4);
  TOT1();
}

extern "C" __global__ void k9_end() {}
__global__ void k_delay(const unsigned long long* st) {
  const unsigned long long now = wall_clock64();
  const unsigned long long a = st[400 + PRA], b = PRB == 7 ? now : st[400 + PRB];
  unsigned long long d = (b - a) * AMP; if (d > 100000) d = 100000;
  while (wall_clock64() < now + d) __builtin_amdgcn_s_sleep(2);
}

extern "C" void kernel_launch(void* const* d_in, const int* in_sizes, int n_in,
                              void* d_out, int out_size, void* d_ws, size_t ws_size,
                              hipStream_t stream) {
  const bf16_t* x0 = (const bf16_t*)d_in[0];
  const bf16_t* wqkv = (const bf16_t*)d_in[1];
  const bf16_t* wo = (const bf16_t*)d_in[2];
  const bf16_t* wg = (const bf16_t*)d_in[4];
  const bf16_t* wu = (const bf16_t*)d_in[5];
  const bf16_t* wd = (const bf16_t*)d_in[6];
  bf16_t* out = (bf16_t*)d_out;
  char* ws = (char*)d_ws;
  constexpr size_t XB = (size_t)S_ * D_ * 2, HB = (size_t)S_ * F_ * 2, PB = (size_t)4 * S_ * D_ * 4;
  size_t off = 0;
  bf16_t* wbar = (bf16_t*)(ws + off); off += 4096;
  unsigned* cnt = (unsigned*)(ws + off); off += NCNT * 128;
  char* wp = ws + off; off += (size_t)144 * K1_WS;
  const bf16_t* xin = x0;
  char* o = ws + off; off += XB;
  bf16_t* x1 = (bf16_t*)(ws + off); off += XB;
  bf16_t* x1c = (bf16_t*)(ws + off); off += XB;
  f32x2* stats = (f32x2*)(ws + off); off += 65536;
  f32x2* xstats = (f32x2*)(ws + off); off += 65536;
  bf16_t* xm = (bf16_t*)(ws + off); off += XB;
  bf16_t* h = (bf16_t*)(ws + off); off += HB;
  float* P = (float*)(ws + off); off += PB;
  k0_prep<<<144 + 512 + 18, 256, 0, stream>>>(wqkv, wp, cnt, x0, wo, xstats, wbar);
  for (int l = 0; l < 2; ++l) {
    bf16_t* x2 = (l == 1) ? out : xm;
    k1_qkv_attn<<<256, 512, 0, stream>>>(xin, wp, o, wo, x1, stats, cnt + (l * 512 + 256) * 32, wbar, x1c, xstats, wg, wu);
    k3_gateup<<<256, 512, 0, stream>>>(x1c, wg, wu, stats, h, wd, (unsigned long long*)wbar + 402 + 3 * l);
    k4_down<<<256, 512, 0, stream>>>(h, wd, x1, x2, cnt + (l * 512) * 32, P, xstats, (unsigned long long*)wbar + 403 + 3 * l);
    xin = x2;
  }
  k_delay<<<1, 64, 0, stream>>>((const unsigned long long*)wbar);
}
```

```cpp
#include <hip/hip_runtime.h>
#include <hip/hip_bf16.h>
#include <stdint.h>

typedef unsigned short bf16_t;
typedef short bf16x8 __attribute__((ext_vector_type(8)));
typedef short bf16x4 __attribute__((ext_vector_type(4)));
typedef float f32x4 __attribute__((ext_vector_type(4)));
typedef float f32x2 __attribute__((ext_vector_type(2)));
typedef float f32x16 __attribute__((ext_vector_type(16)));
typedef unsigned u32x4 __attribute__((ext_vector_type(4)));
typedef unsigned u32x2 __attribute__((ext_vector_type(2)));
#define LAS __attribute__((address_space(3)))
#define DEVI __device__ __forceinline__

constexpr int S_ = 2048, D_ = 576, F_ = 4608, NH_ = 32, DH_ = 18;
constexpr float LN_EPS = 1e-5f;

DEVI float bf2f(unsigned b) { return __uint_as_float(b << 16); }
DEVI float bflo(unsigned w) { return __uint_as_float(w << 16); }
DEVI float bfhi(unsigned w) { return __uint_as_float(w & 0xffff0000u); }
DEVI unsigned pk2(float lo, float hi) {
  typedef __bf16 bf2v __attribute__((ext_vector_type(2)));
  bf2v v; v.x = (__bf16)lo; v.y = (__bf16)hi;
  return __builtin_bit_cast(unsigned, v);
}
template <int CTRL> DEVI float dpp(float x) { return __builtin_bit_cast(float, __builtin_amdgcn_update_dpp(0, __builtin_bit_cast(int, x), CTRL, 0xf, 0xf, true)); }
DEVI float sum8(float x) {
  x += dpp<0xB1>(x); x += dpp<0x4E>(x); x += dpp<0x141>(x); return x;
}
DEVI float rbf(float x) { return (float)((__bf16)x); }

#define TOT0()
#define TOT1()
#define RSTAMP(i)
#define RDUMP(n)
#define STAMPW(kid, i, cond)
#define STAMPK(kid, i)
#define STAMPEND(kid, i)
#define STAMP(i) STAMPK(3, i)
#define ABL_G(x) x
#define ABL_B(x) x
#define ABL_L(...) __VA_ARGS__
#define LNMIMIC 1
#define TAILPRIO 0
#define NPF 2
#define HAUX 16
#define GV 3
#define PRIO 0
template <int N> struct IC { static constexpr int value = N; };
template <int A, int B, class F> __device__ __forceinline__ void static_for(F&& f) {
  if constexpr (A < B) { f(IC<A>{}); static_for<A + 1, B>(f); }
}
#define WAIT_V(n) asm volatile("s_waitcnt vmcnt(" #n ")" ::: "memory")
#define WAIT_L0() asm volatile("s_waitcnt lgkmcnt(0)" ::: "memory")
#define BAR() do { asm volatile("" ::: "memory"); __builtin_amdgcn_s_barrier(); asm volatile("" ::: "memory"); } while (0)

DEVI void glds16(const char* sbase, unsigned voff, unsigned ldsaddr) {
  asm volatile("s_mov_b32 m0, %2\n\ts_nop 0\n\tglobal_load_lds_dwordx4 %0, %1" ::"v"(voff), "s"(sbase), "s"(ldsaddr) : "memory");
}
DEVI void glds_1(const char* sb, unsigned v0, unsigned la) { glds16(sb, v0, la); }
DEVI void glds_2(const char* sb, unsigned v0, unsigned v1, unsigned la) {
  asm volatile("s_mov_b32 m0, %3\n\ts_nop 0\n\tglobal_load_lds_dwordx4 %0, %2\n\tglobal_load_lds_dwordx4 %1, %2 offset:1024"
               ::"v"(v0), "v"(v1), "s"(sb), "s"(la) : "memory");
}
DEVI void glds_4(const char* sb, unsigned v0, unsigned v1, unsigned v2, unsigned v3, unsigned la) {
  asm volatile("s_mov_b32 m0, %5\n\ts_nop 0\n\tglobal_load_lds_dwordx4 %0, %4\n\tglobal_load_lds_dwordx4 %1, %4 offset:1024\n\t"
               "global_load_lds_dwordx4 %2, %4 offset:2048\n\tglobal_load_lds_dwordx4 %3, %4 offset:3072"
               ::"v"(v0), "v"(v1), "v"(v2), "v"(v3), "s"(sb), "s"(la) : "memory");
}
DEVI void glds_2q(const char* sb, unsigned v0, unsigned v1, unsigned v2, unsigned la) {
  asm volatile("s_mov_b32 m0, %4\n\ts_nop 0\n\tglobal_load_lds_dwordx4 %0, %3\n\tglobal_load_lds_dwordx4 %1, %3 offset:1024\n\t"
               "s_mov_b64 exec, 0xffff\n\tglobal_load_lds_dwordx4 %2, %3 offset:2048\n\ts_mov_b64 exec, -1"
               ::"v"(v0), "v"(v1), "v"(v2), "s"(sb), "s"(la) : "memory");
}
DEVI void glds_2m(const char* sb, unsigned v0, unsigned v1, unsigned la, unsigned long long m0_, unsigned long long m1_) {
  asm volatile("s_mov_b32 m0, %3\n\ts_mov_b64 exec, %4\n\tglobal_load_lds_dwordx4 %0, %2\n\t"
               "s_mov_b64 exec, %5\n\tglobal_load_lds_dwordx4 %1, %2 offset:1024\n\ts_mov_b64 exec, -1"
               ::"v"(v0), "v"(v1), "s"(sb), "s"(la), "s"(m0_), "s"(m1_) : "memory");
}
DEVI unsigned ldsa(const LAS char* p) { return (unsigned)(size_t)p; }
DEVI bf16x8 lds_b128(const LAS char* p) { return *(const LAS bf16x8*)p; }
DEVI bf16x8 lds_tr2(const LAS char* p0, const LAS char* p1) {
  bf16x4 a = __builtin_amdgcn_ds_read_tr16_b64_v4i16((LAS bf16x4*)p0);
  bf16x4 b = __builtin_amdgcn_ds_read_tr16_b64_v4i16((LAS bf16x4*)p1);
  return __builtin_shufflevector(a, b, 0, 1, 2, 3, 4, 5, 6, 7);
}
DEVI f32x4 mfma16(bf16x8 a, bf16x8 b, f32x4 c) { return __builtin_amdgcn_mfma_f32_16x16x32_bf16(a, b, c, 0, 0, 0); }

constexpr int NCNT = 1024;
constexpr unsigned EMPTY = 0xffffffffu;

DEVI void row_stats(const u32x4& v0, const u32x4& v1, int sub, f32x2* __restrict__ dst) {
  float xv[16];
#pragma unroll
  for (int e = 0; e < 4; ++e) {
    xv[2 * e] = bflo(v0[e]); xv[2 * e + 1] = bfhi(v0[e]);
    xv[8 + 2 * e] = sub < 2 ? bflo(v1[e]) : 0.f; xv[8 + 2 * e + 1] = sub < 2 ? bfhi(v1[e]) : 0.f;
  }
  float s = 0.f;
#pragma unroll
  for (int e = 0; e < 16; ++e) s += xv[e];
  s = sum8(s); s += dpp<0x140>(s);
  const float mean = s * (1.0f / 144.0f);
  float q = 0.f;
#pragma unroll
  for (int e = 0; e < 16; ++e) { const float d = xv[e] - mean; q += (e < 8 || sub < 2) ? d * d : 0.f; }
  q = sum8(q); q += dpp<0x140>(q);
  if (sub == 0) *dst = (f32x2){mean, q};
}

__global__ __launch_bounds__(512) void k0_prep(const bf16_t* __restrict__ Wqkv, char* __restrict__ Wp, unsigned* __restrict__ cnt,
                                                const bf16_t* __restrict__ X, const bf16_t* __restrict__ Wo, f32x2* __restrict__ xstats,
                                                bf16_t* __restrict__ WB, char* __restrict__ OB, char* __restrict__ Wop) {
  STAMPK(0, 0);
  if (blockIdx.x < 8) TOT0();
  const int b = blockIdx.x, tid = threadIdx.x, lane = tid & 63, wid = __builtin_amdgcn_readfirstlane(tid >> 6);
  if (b < 144) {
    const int hg = b / 18, sl = b - hg * 18;
    if (wid < 7) {
      const int m = lane & 15, g = lane >> 4;
      u32x4 v[2];
#pragma unroll
      for (int j = 0; j < 2; ++j) {
        int u = wid * 2 + j, dim = m;
        if (wid == 6) { int e2 = j * 16 + m; if (e2 > 23) e2 = 23; u = e2 >> 1; dim = 16 + (e2 & 1); }
        const int ty = u >> 2;
        const bf16_t* p = Wqkv + (size_t)(sl * 32 + g * 8) * (3 * D_) + (ty == 2 ? 0 : (ty + 1) * D_) + (hg * 4 + (u & 3)) * DH_ + dim;
#pragma unroll
        for (int e = 0; e < 4; ++e) v[j][e] = (unsigned)p[(2 * e) * (3 * D_)] | ((unsigned)p[(2 * e + 1) * (3 * D_)] << 16);
      }
      char* dst = Wp + (size_t)(((hg * 7 + wid) * 18 + sl) * 2) * 1024 + lane * 16;
      *(u32x4*)dst = v[0]; *(u32x4*)(dst + 1024) = v[1];
    } else {
      if (b < NCNT / 64) cnt[(b * 64 + lane) * 32] = 0u;
      unsigned long long pc0, pc1;
      asm volatile("s_getpc_b64 %0" : "=s"(pc0));
      asm volatile("s_getpc_b64 s[40:41]\n\ts_add_u32 s40, s40, k9_end@rel32@lo+4\n\ts_addc_u32 s41, s41, k9_end@rel32@hi+12\n\ts_mov_b64 %0, s[40:41]"
                   : "=s"(pc1) : : "s40", "s41", "scc");
      u32x4 v[4];
#pragma unroll
      for (int i = 0; i < 4; ++i) {
        const unsigned long long a = (pc0 & ~15ull) + (unsigned long long)(b >> 3) * 4096 + i * 1024 + lane * 16;
        v[i] = *(const u32x4*)((pc1 > pc0 && a + 16 <= pc1) ? a : (pc0 & ~15ull));
      }
#pragma unroll
      for (int i = 0; i < 4; ++i) asm volatile("" ::"v"(v[i]));
    }
  } else if (b < 144 + 256) {
    const int u = (b - 144) * 32 + (tid >> 4), sub = tid & 15;
    const bf16_t* xr = X + (size_t)(u >> 2) * D_ + (u & 3) * 144;
    const u32x4 v0 = *(const u32x4*)(xr + sub * 8), v1 = *(const u32x4*)(xr + (sub < 2 ? 16 + sub : sub) * 8);
    char* ob = OB + (size_t)(b - 144) * 18432 + tid * 16;
    const u32x4 em = {EMPTY, EMPTY, EMPTY, EMPTY};
    *(u32x4*)ob = em; *(u32x4*)(ob + 8192) = em;
    if (tid < 128) *(u32x4*)(ob + 16384) = em;
    row_stats(v0, v1, sub, xstats + u);
  } else if (b >= 409) {
    const int u = b - 409, ct = u / 18, sg = u - ct * 18;
    const int m = lane & 15, C = sg * 4 + (lane >> 4);
    const int r0 = C < 64 ? ((C >> 3) * 4 + ((C >> 2) & 1) * 2) * DH_ + (C & 3) * 4 : (C - 64) * 72 + 16;
    const int sa = C < 64 ? 2 * D_ : DH_ * D_, sb = C < 64 ? DH_ * D_ : 2 * DH_ * D_;
    for (int j = wid; j < 9; j += 8) {
      const bf16_t* p = Wo + (size_t)r0 * D_ + ct * 144 + j * 16 + m;
      u32x4 v;
#pragma unroll
      for (int e = 0; e < 4; ++e) {
        const bf16_t* q = p + (e & 1) * sa + (e >> 1) * sb;
        v[e] = (unsigned)q[0] | ((unsigned)q[D_] << 16);
      }
      *(u32x4*)(Wop + (size_t)(u * 9 + j) * 1024 + lane * 16) = v;
    }
  } else {
    const int k = (b - 400) * 64 + (tid >> 3), sub = tid & 7;
    u32x4 v[9];
#pragma unroll
    for (int i = 0; i < 9; ++i) v[i] = *(const u32x4*)(Wo + (size_t)k * D_ + (i * 8 + sub) * 8);
    float t = 0.f;
#pragma unroll
    for (int i = 0; i < 9; ++i)
#pragma unroll
      for (int e = 0; e < 4; ++e) t += bflo(v[i][e]) + bfhi(v[i][e]);
    t = sum8(t) * (1.0f / D_);
    const float hi = rbf(t);
    const int hd = k / DH_, dim = k - hd * DH_, hg = hd >> 2, hh = hd & 3;
    const int pos = dim < 16 ? (hg * 8 + (hh >> 1) * 4 + (dim >> 2)) * 8 + (hh & 1) * 4 + (dim & 3) : (64 + hg) * 8 + hh * 2 + (dim - 16);
    if (sub < 2) WB[sub * D_ + pos] = (bf16_t)(pk2(sub ? t - hi : hi, 0.f) & 0xffffu);
  }
  STAMPEND(0, 1);
}

constexpr int K1_XT = 80 * 128;
constexpr int K1_QS = 592;
constexpr int K2_RT = 47488;
constexpr int K1_AUX = K2_RT + 4 * 32 * 148 * 4;
constexpr int K1_MEAN = K1_AUX + 768;
constexpr int K1_LDS = K1_MEAN + 320;
#define K1_D 6

template <bool MAX> DEVI void xrow2(float& a, float& b) {
  auto op = [](unsigned x, unsigned y) { return MAX ? fmaxf(__uint_as_float(x), __uint_as_float(y)) : __uint_as_float(x) + __uint_as_float(y); };
  auto r = __builtin_amdgcn_permlane32_swap(__float_as_uint(a), __float_as_uint(b), false, false);
  const unsigned t = __float_as_uint(op(r[0], r[1]));
  auto q = __builtin_amdgcn_permlane16_swap(t, t, false, false);
  const unsigned u = __float_as_uint(op(q[0], q[1]));
  auto w = __builtin_amdgcn_permlane32_swap(u, u, false, false);
  a = __uint_as_float(w[0]); b = __uint_as_float(w[1]);
}
DEVI unsigned nonempty(unsigned w) { return w == EMPTY ? 0x7fff7fffu : w; }

DEVI void gload2(u32x4& a, u32x4& b, unsigned va, unsigned vb, const char* sa, const char* sb) {
  asm volatile("global_load_dwordx4 %0, %2, %4\n\tglobal_load_dwordx4 %1, %3, %5" : "=&v"(a), "=&v"(b) : "v"(va), "v"(vb), "s"(sa), "s"(sb) : "memory");
}
template <int OFF> DEVI void gload1(u32x4& a, unsigned va, const char* sa) {
  asm volatile("global_load_dwordx4 %0, %1, %2 offset:%3" : "=&v"(a) : "v"(va), "s"(sa), "n"(OFF) : "memory");
}

__global__ __launch_bounds__(512) void k1_qkv_attn(const bf16_t* __restrict__ X, const char* __restrict__ Wp, char* __restrict__ OB,
                                                       const char* __restrict__ Wop, bf16_t* __restrict__ Y, f32x2* __restrict__ stats,
                                                       const bf16_t* __restrict__ WB, bf16_t* __restrict__ YC,
                                                       const f32x2* __restrict__ xstats) {
  __shared__ __attribute__((aligned(16))) char lds_[K1_LDS];
  LAS char* lds = (LAS char*)lds_;
  const unsigned lbase = ldsa(lds);
  const int tid = threadIdx.x, lane = tid & 63, wid = __builtin_amdgcn_readfirstlane(tid >> 6);
  const int bid = blockIdx.x, xcd = bid & 7, idx = bid >> 3;
  const int tt = (xcd >> 1) * 8 + (idx & 7), hg = (xcd & 1) * 4 + (idx >> 3);
  const int t0 = tt * 64 - 16;
  STAMPK(1, 0);
  const int g = lane >> 4, m = lane & 15, q4 = (lane >> 2) & 3, p4 = lane & 3;
  const unsigned wv = lane * 16;

  const int th = hg >> 2, ct = hg & 3;
  const int cg = wid >> 2, kq = cg ? 3 - (wid & 3) : wid & 3;
  const int sg0 = kq < 2 ? kq * 5 : 2 + kq * 4;
  const char* w2src = Wop + (size_t)((ct * 18 + sg0) * 9 + cg * 5) * 1024;
  const char* w2src4 = cg ? (const char*)WB + sg0 * 64 : w2src + 4096;
  const unsigned w2v4 = cg ? (unsigned)((m & 1) * (D_ * 2) + g * 16) : wv;
  const int w2st4 = cg ? 64 : 9216;
  u32x4 w2[5][5];
  auto loadB = [&](auto N) {
    constexpr int n = decltype(N)::value, st = n / 5, j = n % 5;
    const int se = (st == 4 && kq >= 2) ? 3 : st;
    if constexpr (j < 4) gload1<j * 1024>(w2[st][j], wv, w2src + se * 9216);
    else gload1<0>(w2[st][j], w2v4, w2src4 + se * w2st4);
  };

  const int tb = wid & 3, hp = wid >> 2;
  f32x4 bias[2];
  auto mkbias = [&]() {
#pragma unroll
    for (int bk = 0; bk < 2; ++bk)
#pragma unroll
      for (int r = 0; r < 4; ++r) {
        const int d = 16 + m - (bk * 16 + g * 4 + r);
        bias[bk][r] = ((d & 1) == 0 && d >= 0 && d <= 14 && tt * 64 + tb * 16 + m - d >= 0) ? 0.f : -1e30f;
      }
    asm volatile("" : "+v"(bias[0]), "+v"(bias[1]));
  };
  f32x4 acc[5][2];
  LAS f32x2* ms = (LAS f32x2*)(lds + K1_AUX);
  constexpr int NB0 = 2 * K1_D;
#define NB1 25
  if (wid < 7) {
    const char* wsrc = Wp + (size_t)((hg * 7 + wid) * 18) * 2048;
    u32x4 wf[18][2];
    auto loadW = [&](auto S, auto J) {
      constexpr int s = decltype(S)::value, j = decltype(J)::value;
      gload1<(s & 1) * 2048 + j * 1024>(wf[s][j], wv, wsrc + (s >> 1) * 4096);
    };
#define K1_D0 2
    static_for<0, K1_D0>([&](auto S) { loadW(S, IC<0>{}); loadW(S, IC<1>{}); });
    if (wid == 1 && lane < 36) {
#pragma unroll
      for (int i = 0; i < 2; ++i) {
        const int c = lane * 2 + i;
        *(LAS u32x4*)(lds + (c >> 3) * K1_XT + (c & 7) * 16) = (u32x4){0x3f803f80u, 0x3f803f80u, 0x3f803f80u, 0x3f803f80u};
      }
    }
#pragma unroll
    for (int i = 0; i < 5; ++i) { acc[i][0] = (f32x4){0, 0, 0, 0}; acc[i][1] = (f32x4){0, 0, 0, 0}; }
    const int xa0 = m * 128 + ((g ^ (m >> 1)) * 16);
    bf16x8 xf[2][5];
    mkbias();
    static_for<K1_D0, K1_D>([&](auto S) { loadW(S, IC<0>{}); loadW(S, IC<1>{}); });
    WAIT_L0(); BAR();
    RSTAMP(0);
#pragma unroll
    for (int i = 0; i < 5; ++i) xf[0][i] = lds_b128(lds + (xa0 + i * 2048));
    static_for<0, 18>([&](auto S) {
      constexpr int s = decltype(S)::value;
      __builtin_amdgcn_sched_barrier(0);
      if (s == 1 || s == 3 || s == 7 || s == 13) BAR();
      asm volatile("s_waitcnt vmcnt(%2)" : "+v"(wf[s][0]), "+v"(wf[s][1]) : "n"(2 * (K1_D - 1)) : "memory");
      __builtin_amdgcn_sched_barrier(0);
      static_for<0, 5>([&](auto I) {
        constexpr int i = decltype(I)::value;
        if (s < 17) xf[(s + 1) & 1][i] = lds_b128(lds + ((xa0 ^ (((s + 1) & 1) * 64)) + ((s + 1) >> 1) * K1_XT + i * 2048));
        acc[i][0] = mfma16(__builtin_bit_cast(bf16x8, wf[s][0]), xf[s & 1][i], acc[i][0]);
        if constexpr (i == 1 || i == 3) {
          if constexpr (s + K1_D < 18) loadW(IC<(s + K1_D < 18 ? s + K1_D : 0)>{}, IC<i / 2>{});
          else loadB(IC<(s + K1_D - 18) * 2 + i / 2>{});
        }
        acc[i][1] = mfma16(__builtin_bit_cast(bf16x8, wf[s][1]), xf[s & 1][i], acc[i][1]);
        __builtin_amdgcn_sched_barrier(0);
      });
    });
  } else {
    unsigned xoff[10];
#pragma unroll
    for (int i = 0; i < 10; ++i) {
      const int row = i * 8 + (lane >> 3), c = (lane & 7) ^ ((row >> 1) & 7);
      int grow = t0 + row; if (i < 2 && grow < 0) grow = 0;
      xoff[i] = (unsigned)((grow * D_ + c * 8) * 2 + 4096 - (i & 3) * 1024);
    }
    auto stageX = [&](int kt) {
      const char* sb = (const char*)X + kt * 128 - 4096;
      const unsigned la = lbase + kt * K1_XT;
      asm volatile("s_mov_b32 m0, %5\n\ts_mov_b32 exec_lo, 0xffffff00\n\tglobal_load_lds_dwordx4 %0, %4\n\ts_mov_b32 exec_lo, -1\n\tglobal_load_lds_dwordx4 %1, %4 offset:1024\n\t"
                   "global_load_lds_dwordx4 %2, %4 offset:2048\n\tglobal_load_lds_dwordx4 %3, %4 offset:3072"
                   ::"v"(xoff[0]), "v"(xoff[1]), "v"(xoff[2]), "v"(xoff[3]), "s"(sb), "s"(la) : "memory");
      glds_4(sb, xoff[4], xoff[5], xoff[6], xoff[7], la + 4096);
      glds_2(sb, xoff[8], xoff[9], la + 8192);
    };
    stageX(0); stageX(1);
    mkbias();
    WAIT_V(10); BAR();
    stageX(2); stageX(3);
    WAIT_V(20); BAR();
    stageX(4); stageX(5); stageX(6);
    WAIT_V(30); BAR();
    f32x4 sa[2], sb[2];
#pragma unroll
    for (int i = 0; i < 2; ++i) {
      int gr = t0 + i * 64 + (i ? (lane & 15) : lane); if (gr < 0) gr = 0;
      asm volatile("global_load_dwordx4 %0, %2, %3\n\tglobal_load_dwordx4 %1, %2, %3 offset:16" : "=&v"(sa[i]), "=&v"(sb[i]) : "v"(gr * 32), "s"(xstats) : "memory");
    }
    stageX(7); stageX(8);
    WAIT_V(24); BAR();
    asm volatile("s_waitcnt vmcnt(0)" : "+v"(sa[0]), "+v"(sa[1]), "+v"(sb[0]), "+v"(sb[1]) : : "memory");
#pragma unroll
    for (int i = 0; i < 2; ++i) {
      const float mean = (sa[i][0] + sa[i][2] + sb[i][0] + sb[i][2]) * 0.25f;
      const float d0 = sa[i][0] - mean, d1 = sa[i][2] - mean, d2 = sb[i][0] - mean, d3 = sb[i][2] - mean;
      const float var = ((sa[i][1] + sa[i][3]) + (sb[i][1] + sb[i][3]) + 144.0f * ((d0 * d0 + d1 * d1) + (d2 * d2 + d3 * d3))) * (1.0f / D_);
      const float den = rbf(__builtin_amdgcn_sqrtf(rbf(rbf(var) + LN_EPS)));
      if (i == 0 || lane < 16) { ms[i * 64 + lane] = (f32x2){rbf(mean), __builtin_amdgcn_rcpf(den)}; ((LAS float*)(lds + K1_MEAN))[i * 64 + lane] = mean; }
    }
    BAR();
    static_for<0, NB0>([&](auto N) { loadB(N); });
  }
  RSTAMP(1);
  static_for<NB0, NB0 + 3>([&](auto N) { loadB(N); });
  WAIT_L0(); BAR();
  RSTAMP(2);
  if (wid < 7) {
    f32x2 mr[5];
#pragma unroll
    for (int i = 0; i < 5; ++i) mr[i] = ms[i * 16 + m];
    f32x4 cs[2];
#pragma unroll
    for (int j = 0; j < 2; ++j)
#pragma unroll
      for (int r = 0; r < 4; ++r) cs[j][r] = dpp<0x150>(acc[0][j][r]);
    u32x2 qw[5][2];
#pragma unroll
    for (int i = 0; i < 5; ++i) {
      const float nm = -mr[i].x * mr[i].y;
#pragma unroll
      for (int j = 0; j < 2; ++j) {
        const f32x4 v = acc[i][j] * mr[i].y + cs[j] * nm;
        qw[i][j] = (u32x2){pk2(v[0], v[1]), pk2(v[2], v[3])};
      }
    }
    if (wid < 6) {
      LAS char* qp = lds + m * K1_QS + wid * 96 + g * 8;
#pragma unroll
      for (int i = 0; i < 5; ++i)
#pragma unroll
        for (int j = 0; j < 2; ++j) *(LAS u32x2*)(qp + i * 16 * K1_QS + j * 48) = qw[i][j];
    } else {
      LAS unsigned* qp = (LAS unsigned*)(lds + m * K1_QS + g * 96 + 32);
#pragma unroll
      for (int i = 0; i < 5; ++i) { qp[i * 4 * K1_QS] = qw[i][0][0]; qp[i * 4 * K1_QS + 12] = qw[i][0][1]; }
      if (g < 2) {
#pragma unroll
        for (int i = 0; i < 5; ++i) { qp[i * 4 * K1_QS + 96] = qw[i][1][0]; qp[i * 4 * K1_QS + 108] = qw[i][1][1]; }
      }
    }
  }
  RSTAMP(3);
  static_for<NB0 + 3, NB0 + 8>([&](auto N) { loadB(N); });
  const int prow = tid >> 4, sub = tid & 15;
  const size_t gofs = (size_t)(tt * 64 + th * 32 + prow) * D_ + ct * 144;
  RSTAMP(4);
  WAIT_L0(); BAR();
  RSTAMP(5);
  const __amdgpu_buffer_rsrc_t rso = __builtin_amdgcn_make_buffer_rsrc((void*)OB, 0, S_ * D_ * 2, 0x00020000);
  {
    const LAS char* ka = lds + (tb * 16 + m) * K1_QS + hp * 96 + (g < 3 ? g : 2) * 16;
    const LAS char* va = lds + (tb * 16 + g * 4 + q4) * K1_QS + (4 + hp * 2) * 48 + p4 * 8;
    const unsigned mk0 = g < 3 ? ~0u : 0u, mk1 = g < 2 ? ~0u : 0u;
    bf16x8 qf[2], kf[2][2], vf[2][2];
#pragma unroll
    for (int a = 0; a < 2; ++a) {
      const u32x4 q = *(const LAS u32x4*)(ka + a * 48 + 16 * K1_QS + 384);
      qf[a] = __builtin_bit_cast(bf16x8, (u32x4){q[0] & mk0, q[1] & mk1, q[2] & mk1, q[3] & mk1});
      kf[a][0] = lds_b128(ka + a * 48); kf[a][1] = lds_b128(ka + a * 48 + 16 * K1_QS);
    }
#pragma unroll
    for (int a = 0; a < 2; ++a)
#pragma unroll
      for (int x = 0; x < 2; ++x) vf[a][x] = lds_tr2(va + a * 48 + x * 32, va + a * 48 + x * 32 + 16 * K1_QS);
    static_for<NB0 + 8, NB1>([&](auto N) { loadB(N); });
    constexpr float SC = 0.23570226039551584f * 1.4426950408889634f;
    f32x4 sc[2][2], lg[2];
    float mx[2], inv[2];
#pragma unroll
    for (int a = 0; a < 2; ++a) { sc[a][0] = mfma16(kf[a][0], qf[a], bias[0]); sc[a][1] = mfma16(kf[a][1], qf[a], bias[1]); }
#pragma unroll
    for (int a = 0; a < 2; ++a) {
      lg[a] = (f32x4){fmaxf(sc[a][0][0], sc[a][0][1]), fmaxf(sc[a][0][2], sc[a][0][3]), fmaxf(sc[a][1][0], sc[a][1][1]), fmaxf(sc[a][1][2], sc[a][1][3])};
      mx[a] = fmaxf(fmaxf(lg[a][0], lg[a][1]), fmaxf(lg[a][2], lg[a][3]));
    }
    xrow2<true>(mx[0], mx[1]);
#pragma unroll
    for (int a = 0; a < 2; ++a) {
      const f32x4 z = lg[a] * SC - mx[a] * SC;
#pragma unroll
      for (int r = 0; r < 4; ++r) lg[a][r] = __builtin_amdgcn_exp2f(z[r]);
      inv[a] = (lg[a][0] + lg[a][1]) + (lg[a][2] + lg[a][3]);
    }
    xrow2<false>(inv[0], inv[1]);
    u32x2 ow[2]; unsigned ox[2];
    f32x4 o0[2], o1[2];
#pragma unroll
    for (int a = 0; a < 2; ++a) {
      const float iv = __builtin_amdgcn_rcpf(inv[a]);
      const f32x2 i2 = {(m & 1) ? 0.f : iv, (m & 1) ? iv : 0.f};
      u32x4 pw;
#pragma unroll
      for (int r = 0; r < 4; ++r) { const f32x2 p = i2 * lg[a][r]; pw[r] = pk2(p.x, p.y); }
      const f32x4 z = {0, 0, 0, 0};
      o0[a] = mfma16(vf[a][0], __builtin_bit_cast(bf16x8, pw), z); o1[a] = mfma16(vf[a][1], __builtin_bit_cast(bf16x8, pw), z);
    }
#pragma unroll
    for (int a = 0; a < 2; ++a) {
      ow[a] = (u32x2){nonempty(pk2(o0[a][0], o0[a][1])), nonempty(pk2(o0[a][2], o0[a][3]))};
      ox[a] = nonempty(pk2(o1[a][0], o1[a][1]));
    }
    const int ob = (tt * 2 + (tb >> 1)) * (72 * 512) + ((tb & 1) * 16 + m) * 16;
    __builtin_amdgcn_raw_buffer_store_b128((u32x4){ow[0][0], ow[0][1], ow[1][0], ow[1][1]}, rso, ob + (hg * 8 + hp * 4 + g) * 512, 0, 16);
    if (g == 0) __builtin_amdgcn_raw_buffer_store_b64((u32x2){ox[0], ox[1]}, rso, ob + (64 + hg) * 512 + hp * 8, 0, 16);
  }
  RSTAMP(6);
  static_for<NB1, 25>([&](auto N) { loadB(N); });
  u32x4 rv; unsigned rv1;
  asm volatile("global_load_dwordx4 %0, %2, %4\n\tglobal_load_ushort %1, %3, %4 offset:256" : "=&v"(rv), "=&v"(rv1)
               : "v"((unsigned)(gofs + sub * 8) * 2u), "v"((unsigned)(gofs + sub) * 2u), "s"(X) : "memory");
  {
    const int oa = (tt * 2 + th) * (72 * 512) + (sg0 * 4 + g) * 512 + m * 16;
    const int o4 = kq < 2 ? 4 * 2048 : 3 * 2048;
    u32x4 xf[5][2];
    unsigned spins = 0;
    WAIT_V(0);
    RSTAMP(13);
    while (true) {
      asm volatile("" ::: "memory");
#pragma unroll
      for (int t = 0; t < 5; ++t)
#pragma unroll
        for (int i = 0; i < 2; ++i) xf[t][i] = __builtin_amdgcn_raw_buffer_load_b128(rso, oa + i * 256, t < 4 ? t * 2048 : o4, 16);
      unsigned mxw = 0;
#pragma unroll
      for (int t = 0; t < 5; ++t)
#pragma unroll
        for (int i = 0; i < 2; ++i) mxw = max(max(mxw, max(xf[t][i][0], xf[t][i][1])), max(xf[t][i][2], xf[t][i][3]));
      if (__builtin_amdgcn_ballot_w64(mxw == EMPTY) == 0ull || ++spins > (1u << 18)) break;
    }
    RSTAMP(7);
    asm volatile("s_waitcnt vmcnt(0)" : "+v"(rv), "+v"(rv1) : : "memory");
#pragma unroll
    for (int t = 0; t < 5; ++t) asm volatile("" : "+v"(w2[t][0]), "+v"(w2[t][1]), "+v"(w2[t][2]), "+v"(w2[t][3]), "+v"(w2[t][4]));
    RSTAMP(8);
    f32x4 ac[2][5];
#pragma unroll
    for (int i = 0; i < 2; ++i)
#pragma unroll
      for (int j = 0; j < 5; ++j) ac[i][j] = (f32x4){0, 0, 0, 0};
#pragma unroll
    for (int t = 0; t < 5; ++t)
      if (t < 4 || kq < 2) {
#pragma unroll
        for (int j = 0; j < 5; ++j)
#pragma unroll
          for (int i = 0; i < 2; ++i) ac[i][j] = mfma16(__builtin_bit_cast(bf16x8, w2[t][j]), __builtin_bit_cast(bf16x8, xf[t][i]), ac[i][j]);
      }
    RSTAMP(9);
    LAS float* rt = (LAS float*)(lds + K2_RT) + (kq * 32 + m) * 148 + cg * 80 + g * 4;
#pragma unroll
    for (int i = 0; i < 2; ++i)
#pragma unroll
      for (int j = 0; j < 5; ++j) if (j < 4 || cg == 0 || g == 0) *(LAS f32x4*)(rt + i * 16 * 148 + j * 16) = ac[i][j];
  }
  WAIT_L0(); BAR();
  RSTAMP(10);
  {
    const LAS float* rt = (const LAS float*)(lds + K2_RT) + prow * 148;
    f32x2 dm = *(const LAS f32x2*)(rt + 144);
#pragma unroll
    for (int k = 1; k < 4; ++k) dm += *(const LAS f32x2*)(rt + k * 32 * 148 + 144);
    const float mean1 = rbf(((const LAS float*)(lds + K1_MEAN))[16 + th * 32 + prow] + (dm.x + dm.y));
    const LAS float* p0 = rt + sub * 8;
    const f32x4 a0 = (*(const LAS f32x4*)p0 + *(const LAS f32x4*)(p0 + 32 * 148)) + (*(const LAS f32x4*)(p0 + 64 * 148) + *(const LAS f32x4*)(p0 + 96 * 148));
    const f32x4 a1 = (*(const LAS f32x4*)(p0 + 4) + *(const LAS f32x4*)(p0 + 32 * 148 + 4)) + (*(const LAS f32x4*)(p0 + 64 * 148 + 4) + *(const LAS f32x4*)(p0 + 96 * 148 + 4));
    const LAS float* p1 = rt + 128 + sub;
    const float a2 = (p1[0] + p1[32 * 148]) + (p1[64 * 148] + p1[96 * 148]);
    const u32x4 y = {pk2(a0[0], a0[1]), pk2(a0[2], a0[3]), pk2(a1[0], a1[1]), pk2(a1[2], a1[3])};
    u32x4 xo, xc;
    const float x2 = rbf(bflo(rv1) + rbf(a2)), d2 = x2 - mean1;
    f32x2 s1 = {d2, 0.f}, s2 = {d2 * d2, 0.f};
#pragma unroll
    for (int e = 0; e < 4; ++e) {
      xo[e] = pk2(bflo(rv[e]) + bflo(y[e]), bfhi(rv[e]) + bfhi(y[e]));
      const f32x2 d = (f32x2){bflo(xo[e]), bfhi(xo[e])} - mean1;
      xc[e] = pk2(d.x, d.y);
      s1 += d; s2 += d * d;
    }
    *(u32x4*)(YC + gofs + sub * 8) = xc; *(u32x4*)(Y + gofs + sub * 8) = xo;
    YC[gofs + 128 + sub] = (bf16_t)(pk2(d2, 0.f) & 0xffffu); Y[gofs + 128 + sub] = (bf16_t)(__float_as_uint(x2) >> 16);
    float t1 = s1.x + s1.y, t2 = s2.x + s2.y;
    t1 = sum8(t1); t2 = sum8(t2); t1 += dpp<0x140>(t1); t2 += dpp<0x140>(t2);
    if (sub == 0) stats[(tt * 64 + th * 32 + prow) * 4 + ct] = (f32x2){mean1 + t1 * (1.0f / 144.0f), t2 - t1 * t1 * (1.0f / 144.0f)};
  }
  RSTAMP(11);
  STAMPEND(1, 1);
  RSTAMP(12);
  RDUMP(15);
}

constexpr int K3_XS = 32768, K3_UOFF = 9216 + 128, K3_WS = K3_UOFF + 9216;
constexpr int K3_WBASE = 2 * K3_XS, K3_AUX = K3_WBASE + 4 * K3_WS;
constexpr int K3_DUMMY = K3_AUX + 8192;
constexpr int K3_LDS = K3_DUMMY + 4096;

template <int NJ, int J0>
DEVI void k3_body(LAS char* lds, const char* X, const char* wsrc, bf16_t* __restrict__ H, const unsigned (&xoff)[4], const unsigned (&woff)[3],
                  unsigned xdst, unsigned wdst, int lane, int wr, int pm, int pf, const char* Wd, unsigned pfoff) {
  auto stageX = [&](int t) { glds_4(X + t * 128, xoff[0], xoff[1], xoff[2], xoff[3], xdst + (t & 1) * K3_XS); };
  auto stageW = [&](int s) { glds_2q(wsrc + (size_t)s * 32 * F_ * 2, woff[0], woff[1], woff[2], wdst + (s & 3) * K3_WS); };
  const int tk = lane & 31, h = lane >> 5, q4 = (lane >> 2) & 3, p4 = lane & 3, isup = (lane >> 4) & 1;
  const int xa = (wr * 64 + tk) * 128 + ((h ^ ((tk >> 1) & 7)) * 16);
  const int wa = K3_WBASE + isup * K3_UOFF + (h * 8 + q4) * 288 + J0 * 32 + p4 * 8;
  auto rdW = [&](int wo, int kk, int j) -> bf16x8 {
    const LAS char* p = lds + (wa + wo + kk * (16 * 288) + j * 32);
    return lds_tr2(p, p + 4 * 288);
  };
  auto rdX = [&](int xo, int k4, int i) -> bf16x8 {
    return lds_b128(lds + ((xa ^ (k4 * 32)) + xo + i * 4096));
  };
  f32x16 acc[2][NJ];
#pragma unroll
  for (int i = 0; i < 2; ++i)
#pragma unroll
    for (int j = 0; j < NJ; ++j)
#pragma unroll
      for (int r = 0; r < 16; ++r) acc[i][j][r] = 0.f;
  bf16x8 xfa[2], xfb[2], xfc[2], wf[4];
  WAIT_V(4); BAR();
  STAMP(1);
  xfa[0] = rdX(0, 0, 0); xfa[1] = rdX(0, 0, 1);
  wf[0] = rdW(0, 0, 0); wf[1] = rdW(0, 0, 1);
  stageX(1); stageW(2);

  if (PRIO == 1 && NJ == 5) __builtin_amdgcn_s_setprio(1);
  if (PRIO == 2 && NJ == 4) __builtin_amdgcn_s_setprio(1);
#pragma unroll 1
  for (int t = 0; t < 9; ++t) {
    const int tp = t & 1;
    const int wo = tp * (2 * K3_WS), won = (1 - tp) * (2 * K3_WS);
    const int xo = tp * K3_XS, xon = (1 - tp) * K3_XS;
#pragma unroll
    for (int k4 = 0; k4 < 4; ++k4) {
      if (k4 == 0) {
        if (t < 8) { WAIT_V(7); }
        ABL_B(BAR());
      }
      if (k4 == 2) {
        if (t < 7) { WAIT_V(3); } else if (t == 7) { WAIT_V(0); }
        WAIT_L0();
        ABL_B(BAR());
        if (t == 7) {
          glds_4(Wd, pfoff, pfoff, pfoff, pfoff, ldsa(lds) + K3_DUMMY);
          glds_2(Wd + 3136, pfoff, pfoff, ldsa(lds) + K3_DUMMY);
        }
      }
      __builtin_amdgcn_sched_barrier(0);
#pragma unroll
      for (int j = 0; j < NJ; ++j) {
        const int n = k4 * NJ + j;
        if (j == 0) {
          if (k4 == (NJ == 4 ? 0 : 1) && t < 8) stageW(2 * t + 3);
          if (k4 == (NJ == 4 ? 2 : 3) && t < 7) { stageW(2 * t + 4); stageX(t + 2); }
        }
        {
          const int n2 = n + 2, k42 = n2 / NJ, j2 = n2 % NJ;
          if (k42 < 4) { ABL_L(wf[n2 & 3] = rdW(wo + (k42 >> 1) * K3_WS, k42 & 1, j2);) }
          else { ABL_L(wf[n2 & 3] = rdW(won, 0, j2);) }
        }
        if (j == 1) {
          if (k4 == 0) { ABL_L(xfb[0] = rdX(xo, 1, 0); xfb[1] = rdX(xo, 1, 1);) }
          if (k4 == 1) { ABL_L(xfa[0] = rdX(xo, 2, 0); xfa[1] = rdX(xo, 2, 1);) }
          if (k4 == 3) { ABL_L(xfa[0] = rdX(xon, 0, 0); xfa[1] = rdX(xon, 0, 1);) }
        }
        if (j == 2 && k4 == 1) { ABL_L(xfc[0] = rdX(xo, 3, 0); xfc[1] = rdX(xo, 3, 1);) }
#pragma unroll
        for (int i = 0; i < 2; ++i)
          acc[i][j] = __builtin_amdgcn_mfma_f32_32x32x16_bf16(wf[n & 3], k4 == 1 ? xfb[i] : (k4 == 3 ? xfc[i] : xfa[i]), acc[i][j], 0, 0, 0);
        __builtin_amdgcn_sched_barrier(0);
      }
    }
  }
  STAMP(2);
  float rs[2];
#pragma unroll
  for (int i = 0; i < 2; ++i) {
    const LAS f32x4* sp = (const LAS f32x4*)(lds + K3_AUX + (wr * 64 + i * 32 + tk) * 32);
    const f32x4 s01 = sp[0], s23 = sp[1];
    const float mean = (s01[0] + s01[2] + s23[0] + s23[2]) * 0.25f;
    const float d0 = s01[0] - mean, d1 = s01[2] - mean, d2 = s23[0] - mean, d3 = s23[2] - mean;
    const float m2 = (s01[1] + s01[3]) + (s23[1] + s23[3]) + 144.0f * ((d0 * d0 + d1 * d1) + (d2 * d2 + d3 * d3));
    { const float den = rbf(__builtin_amdgcn_sqrtf(rbf(rbf(m2 * (1.0f / D_)) + LN_EPS))); rs[i] = den * den; }
  }
  const __amdgpu_buffer_rsrc_t rsh = __builtin_amdgcn_make_buffer_rsrc((void*)H, 0, S_ * F_ * 2, 0x00020000);
  const int hofs = (((pf * 9 + J0) * 2 + h) * S_ + pm * 256 + wr * 64 + tk) * 16;
#pragma unroll
  for (int i = 0; i < 2; ++i) {
    const float c1 = -1.4426950408889634f * __builtin_amdgcn_rsqf(rs[i]);
    const f32x2 c1v = {c1, c1}, inv = {rs[i], rs[i]};
#pragma unroll
    for (int j = 0; j < NJ; ++j) {
      u32x4 hw;
#pragma unroll
      for (int r = 0; r < 4; ++r) {
        const f32x2 ag = {acc[i][j][2 * r], acc[i][j][2 * r + 1]}, au = {acc[i][j][8 + 2 * r], acc[i][j][8 + 2 * r + 1]};
        const f32x2 z = ag * c1v;
        const f32x2 e = {__builtin_amdgcn_exp2f(z.x), __builtin_amdgcn_exp2f(z.y)};
        const f32x2 d = e * inv + inv;
        const f32x2 rc = {__builtin_amdgcn_rcpf(d.x), __builtin_amdgcn_rcpf(d.y)};
        const f32x2 hv = (ag * au) * rc;
        hw[r] = pk2(hv.x, hv.y);
      }
      __builtin_amdgcn_raw_buffer_store_b128(hw, rsh, hofs + j * (2 * S_ * 16) + i * 512, 0, HAUX);
    }
  }
  STAMP(3);
}

__global__ __launch_bounds__(512, 2) void k3_gateup(const bf16_t* __restrict__ X, const bf16_t* __restrict__ Wg, const bf16_t* __restrict__ Wu,
                                                     const f32x2* __restrict__ stats, bf16_t* __restrict__ H,
                                                     const bf16_t* __restrict__ Wd) {
  __shared__ __attribute__((aligned(16))) char lds_[K3_LDS];
  LAS char* lds = (LAS char*)lds_;
  const unsigned lbase = ldsa(lds);
  const int tid = threadIdx.x, lane = tid & 63, wid = __builtin_amdgcn_readfirstlane(tid >> 6);
  const int bid = blockIdx.x, xcd = bid & 7, idx = bid >> 3;
  const int pm = (xcd >> 2) * 4 + (idx & 3), pf = (xcd & 3) * 8 + (idx >> 2);
  const int wr = wid & 3, wc = wid >> 2;
  STAMP(0);
  unsigned xoff[4], woff[3];
#pragma unroll
  for (int i = 0; i < 4; ++i) {
    const int row = (wid * 4 + i) * 8 + (lane >> 3);
    const int c = (lane & 7) ^ ((row >> 1) & 7);
    xoff[i] = (unsigned)(((pm * 256 + row) * D_ + c * 8) * 2 - i * 1024);
  }
  const unsigned xdst = lbase + wid * 4096;
  glds_4((const char*)X, xoff[0], xoff[1], xoff[2], xoff[3], xdst);
#pragma unroll
  for (int i = 0; i < 3; ++i) {
    int ci = i * 64 + lane; if (ci > 143) ci = 143;
    const int kk = ci / 18, cp = ci - kk * 18;
    woff[i] = (unsigned)((((wid & 3) * 8 + kk) * F_ + pf * 144 + cp * 8) * 2 - i * 1024);
  }
  const char* wsrc = (const char*)((wid >> 2) ? Wu : Wg);
  const unsigned wdst = lbase + K3_WBASE + (wid >> 2) * K3_UOFF + (wid & 3) * 2304;
  glds_2q(wsrc, woff[0], woff[1], woff[2], wdst);
  glds_2q(wsrc + 32 * F_ * 2, woff[0], woff[1], woff[2], wdst + K3_WS);
  glds16((const char*)(stats + pm * 256 * 4), (unsigned)(tid * 16), lbase + K3_AUX + wid * 1024);
  const unsigned pfoff = (unsigned)((xcd & 3) * (1152 * D_ * 2) + (idx * 8 + wid) * 5184 + lane * 16);
  if (wc == 0) k3_body<5, 0>(lds, (const char*)X, wsrc, H, xoff, woff, xdst, wdst, lane, wr, pm, pf, (const char*)Wd, pfoff);
  else k3_body<4, 5>(lds, (const char*)X, wsrc, H, xoff, woff, xdst, wdst, lane, wr, pm, pf, (const char*)Wd, pfoff);
  STAMPEND(3, 4);
}

constexpr int K4_XB = 16384, K4_STG = K4_XB + 18432, K4_LDS = 4 * K4_STG;

template <int NJ, int J0>
DEVI void k4_body(LAS char* lds, const char* X, const char* W, unsigned xoff, const unsigned (&woff)[3], unsigned sdst, unsigned wdst,
                  int lane, int wm, int split, int tile, float* __restrict__ P, unsigned* __restrict__ cnt) {
  auto stage = [&](int kt, int buf) {
    glds_2(X + (size_t)kt * (8 * S_ * 16), xoff, xoff, sdst + buf * K4_STG);
    glds_2q(W + (size_t)kt * 64 * D_ * 2, woff[0], woff[1], woff[2], wdst + buf * K4_STG);
  };
  const int g = lane >> 4, m = lane & 15, q4 = (lane >> 2) & 3, p4 = lane & 3;
  const int xa = g * 2048 + (wm * 32 + m) * 16;
  const int wa = K4_XB + ((g >> 1) * 16 + (g & 1) * 4 + q4) * 288 + J0 * 32 + p4 * 8;
  f32x4 acc[2][NJ];
#pragma unroll
  for (int i = 0; i < 2; ++i)
#pragma unroll
    for (int j = 0; j < NJ; ++j) acc[i][j] = (f32x4){0, 0, 0, 0};
  bf16x8 xa_[2], wa_[NJ], xb_[2], wb_[NJ];
  auto rd = [&](bf16x8 (&xf)[2], bf16x8 (&wf)[NJ], const LAS char* sb, int ks) {
#pragma unroll
    for (int i = 0; i < 2; ++i) xf[i] = lds_b128(sb + xa + ks * 8192 + i * 256);
#pragma unroll
    for (int j = 0; j < NJ; ++j) wf[j] = lds_tr2(sb + wa + ks * (32 * 288) + j * 32, sb + wa + ks * (32 * 288) + j * 32 + 8 * 288);
  };
  auto step = [&](bf16x8 (&xc)[2], bf16x8 (&wc)[NJ], bf16x8 (&xn)[2], bf16x8 (&wn)[NJ], const LAS char* sb, int ks) {
#pragma unroll
    for (int j = 0; j < NJ; ++j) {
      if (j < 2) xn[j] = lds_b128(sb + xa + ks * 8192 + j * 256);
      wn[j] = lds_tr2(sb + wa + ks * (32 * 288) + j * 32, sb + wa + ks * (32 * 288) + j * 32 + 8 * 288);
#pragma unroll
      for (int i = 0; i < 2; ++i) acc[i][j] = mfma16(wc[j], xc[i], acc[i][j]);
      __builtin_amdgcn_sched_barrier(0);
    }
  };
  WAIT_V(5); BAR();
  STAMPK(4, 1);
  rd(xa_, wa_, lds, 0);
  stage(2, 2);
  step(xa_, wa_, xb_, wb_, lds, 1);
  stage(3, 3);
  auto iter = [&](auto INFL, auto MORE, int t) {
    asm volatile("s_waitcnt vmcnt(%0) lgkmcnt(0)" ::"n"(decltype(INFL)::value * 5) : "memory");
    BAR();
    __builtin_amdgcn_sched_barrier(0);
    const LAS char* sn = lds + ((t + 1) & 3) * K4_STG;
    if (decltype(MORE)::value && NJ == 4) stage(t + 4, t & 3);
    step(xb_, wb_, xa_, wa_, sn, 0);
    if (decltype(MORE)::value && NJ == 5) stage(t + 4, t & 3);
    step(xa_, wa_, xb_, wb_, sn, 1);
  };
#pragma unroll 1
  for (int t = 0; t < 14; ++t) iter(IC<2>{}, IC<1>{}, t);
  iter(IC<2>{}, IC<0>{}, 14);
  iter(IC<1>{}, IC<0>{}, 15);
  iter(IC<0>{}, IC<0>{}, 16);
#pragma unroll
  for (int j = 0; j < NJ; ++j)
#pragma unroll
    for (int i = 0; i < 2; ++i) acc[i][j] = mfma16(wb_[j], xb_[i], acc[i][j]);
  STAMPK(4, 2);
  const __amdgpu_buffer_rsrc_t rs = __builtin_amdgcn_make_buffer_rsrc((void*)P, 0, 64 * 16 * 9 * 1024, 0x00020000);
  if (wm != split) {
    const int sb = ((tile * 4 + wm) * 4 + split) * 9 * 1024 + lane * 16;
#pragma unroll
    for (int j = 0; j < NJ; ++j) {
      const u32x4 v = {pk2(acc[0][j][0], acc[0][j][1]), pk2(acc[0][j][2], acc[0][j][3]), pk2(acc[1][j][0], acc[1][j][1]), pk2(acc[1][j][2], acc[1][j][3])};
      __builtin_amdgcn_raw_buffer_store_b128(v, rs, sb + (J0 + j) * 1024, 0, 16);
    }
    WAIT_V(0);
    if (lane == 0) __hip_atomic_fetch_add(cnt + (tile * 4 + wm) * 32, 1u, __ATOMIC_RELAXED, __HIP_MEMORY_SCOPE_AGENT);
  } else {
    LAS f32x4* own = (LAS f32x4*)(lds + 2 * K4_STG);
#pragma unroll
    for (int i = 0; i < 2; ++i)
#pragma unroll
      for (int j = 0; j < NJ; ++j) own[(i * 9 + J0 + j) * 64 + lane] = acc[i][j];
    if (NJ == 5) {
      unsigned spins = 0;
      while ((unsigned)__builtin_amdgcn_readfirstlane(__hip_atomic_load(cnt + (tile * 4 + split) * 32, __ATOMIC_RELAXED, __HIP_MEMORY_SCOPE_AGENT)) < 6u) {
        __builtin_amdgcn_s_sleep(1);
        if (++spins > (1u << 22)) break;
      }
    }
  }
}

__global__ __launch_bounds__(512, 2) void k4_down(const bf16_t* __restrict__ X, const bf16_t* __restrict__ W, const bf16_t* __restrict__ R,
                                                   bf16_t* __restrict__ Y, unsigned* __restrict__ cnt, float* __restrict__ P, f32x2* __restrict__ xstats) {
  __shared__ __attribute__((aligned(16))) char lds_[K4_LDS];
  LAS char* lds = (LAS char*)lds_;
  const unsigned lbase = ldsa(lds);
  const int tid = threadIdx.x, lane = tid & 63, wid = __builtin_amdgcn_readfirstlane(tid >> 6);
  const int bid = blockIdx.x, xcd = bid & 7, idx = bid >> 3;
  const int split = xcd & 3;
  const int m0 = (xcd >> 2) * 1024 + (idx >> 2) * 128, n0 = (idx & 3) * 144, k0 = split * 1152;
  const int wm = wid & 3, wn = wid >> 2;
  STAMPK(4, 0);
  const unsigned xoff = (unsigned)((((k0 >> 3) + wid) * S_ + m0 + lane) * 16);
  unsigned woff[3];
#pragma unroll
  for (int i = 0; i < 3; ++i) {
    int ci = i * 64 + lane; if (ci > 143) ci = 143;
    const int rr = ci / 18, c = ci - rr * 18;
    woff[i] = (unsigned)(((k0 + wid * 8 + rr) * D_ + n0 + c * 8) * 2 - i * 1024);
  }
  const unsigned sdst = lbase + wid * 2048, wdst = lbase + K4_XB + wid * 2304;
#pragma unroll
  for (int s = 0; s < 2; ++s) {
    glds_2((const char*)X + (size_t)s * (8 * S_ * 16), xoff, xoff, sdst + s * K4_STG);
    glds_2q((const char*)W + (size_t)s * 64 * D_ * 2, woff[0], woff[1], woff[2], wdst + s * K4_STG);
  }
  const int tile = (m0 >> 7) * 4 + (idx & 3);
  if (wn == 0) k4_body<5, 0>(lds, (const char*)X, (const char*)W, xoff, woff, sdst, wdst, lane, wm, split, tile, P, cnt);
  else k4_body<4, 5>(lds, (const char*)X, (const char*)W, xoff, woff, sdst, wdst, lane, wm, split, tile, P, cnt);
  const int row = tid >> 4, sub = tid & 15;
  const size_t gofs = (size_t)(m0 + split * 32 + row) * D_ + n0;
  u32x4 rv[2];
  rv[0] = *(const u32x4*)(R + gofs + sub * 8);
  rv[1] = *(const u32x4*)(R + gofs + (sub < 2 ? 16 + sub : sub) * 8);
  WAIT_L0(); BAR();
  STAMPK(4, 3);
  const int g = lane >> 4, m = lane & 15;
  LAS f32x4* own = (LAS f32x4*)(lds + 2 * K4_STG);
  LAS float* rt = (LAS float*)lds;
  {
    const __amdgpu_buffer_rsrc_t rs = __builtin_amdgcn_make_buffer_rsrc((void*)P, 0, 64 * 16 * 9 * 1024, 0x00020000);
    u32x4 pv[2][3];
#pragma unroll
    for (int n = 0; n < 2; ++n) {
      const int j = wid + 8 * n < 9 ? wid + 8 * n : wid;
#pragma unroll
      for (int sx = 0; sx < 3; ++sx) {
        const int src = sx + (sx >= split);
        pv[n][sx] = __builtin_amdgcn_raw_buffer_load_b128(rs, (((tile * 4 + split) * 4 + src) * 9 + j) * 1024 + lane * 16, 0, 16);
      }
    }
#pragma unroll
    for (int n = 0; n < 2; ++n) {
      const int j = wid + 8 * n;
      if (j < 9) {
#pragma unroll
        for (int i = 0; i < 2; ++i) {
          f32x4 v = own[(i * 9 + j) * 64 + lane];
#pragma unroll
          for (int sx = 0; sx < 3; ++sx)
            v += (f32x4){bflo(pv[n][sx][2 * i]), bfhi(pv[n][sx][2 * i]), bflo(pv[n][sx][2 * i + 1]), bfhi(pv[n][sx][2 * i + 1])};
          *(LAS f32x4*)(rt + (i * 16 + m) * 148 + j * 16 + g * 4) = v;
        }
      }
    }
  }
  WAIT_L0(); BAR();
  {
    u32x4 xo[2];
#pragma unroll
    for (int c = 0; c < 2; ++c) {
      const bool ok = c == 0 || sub < 2;
      const int ch = ok ? c * 16 + sub : sub;
      const LAS float* p0 = rt + row * 148 + ch * 8;
      const f32x4 a0 = *(const LAS f32x4*)p0, a1 = *(const LAS f32x4*)(p0 + 4);
      const u32x4 y = {pk2(a0[0], a0[1]), pk2(a0[2], a0[3]), pk2(a1[0], a1[1]), pk2(a1[2], a1[3])};
#pragma unroll
      for (int e = 0; e < 4; ++e) xo[c][e] = pk2(bflo(rv[c][e]) + bflo(y[e]), bfhi(rv[c][e]) + bfhi(y[e]));
      if (ok) *(u32x4*)(Y + gofs + ch * 8) = xo[c];
    }
    row_stats(xo[0], xo[1], sub, xstats + (m0 + split * 32 + row) * 4 + (idx & 3));
  }
  STAMPEND(4, 4);
  TOT1();
}

extern "C" __global__ void k9_end() {}

extern "C" void kernel_launch(void* const* d_in, const int* in_sizes, int n_in,
                              void* d_out, int out_size, void* d_ws, size_t ws_size,
                              hipStream_t stream) {
  const bf16_t* x0 = (const bf16_t*)d_in[0];
  const bf16_t* wqkv = (const bf16_t*)d_in[1];
  const bf16_t* wo = (const bf16_t*)d_in[2];
  const bf16_t* wg = (const bf16_t*)d_in[4];
  const bf16_t* wu = (const bf16_t*)d_in[5];
  const bf16_t* wd = (const bf16_t*)d_in[6];
  bf16_t* out = (bf16_t*)d_out;
  char* ws = (char*)d_ws;
  constexpr size_t XB = (size_t)S_ * D_ * 2, HB = (size_t)S_ * F_ * 2, PB = (size_t)4 * S_ * D_ * 4;
  size_t off = 0;
  bf16_t* wbar = (bf16_t*)(ws + off); off += 4096;
  unsigned* cnt = (unsigned*)(ws + off); off += NCNT * 128;
  char* wp = ws + off; off += (size_t)8 * 7 * 18 * 2048;
  char* wop = ws + off; off += (size_t)72 * 9 * 1024;
  const bf16_t* xin = x0;
  char* o = ws + off; off += 2 * XB;
  bf16_t* x1 = (bf16_t*)(ws + off); off += XB;
  bf16_t* x1c = (bf16_t*)(ws + off); off += XB;
  f32x2* stats = (f32x2*)(ws + off); off += 65536;
  f32x2* xstats = (f32x2*)(ws + off); off += 65536;
  bf16_t* xm = (bf16_t*)(ws + off); off += XB;
  bf16_t* h = (bf16_t*)(ws + off); off += HB;
  float* P = (float*)(ws + off); off += PB;
  k0_prep<<<144 + 256 + 9 + 72, 512, 0, stream>>>(wqkv, wp, cnt, x0, wo, xstats, wbar, o, wop);
  for (int l = 0; l < 2; ++l) {
    bf16_t* x2 = (l == 1) ? out : xm;
    k1_qkv_attn<<<256, 512, 0, stream>>>(xin, wp, o + l * XB, wop, x1, stats, wbar, x1c, xstats);
    k3_gateup<<<256, 512, 0, stream>>>(x1c, wg, wu, stats, h, wd);
    k4_down<<<256, 512, 0, stream>>>(h, wd, x1, x2, cnt + (l * 512) * 32, P, xstats);
    xin = x2;
  }
}
```

```cpp
#include <hip/hip_runtime.h>
#include <hip/hip_bf16.h>
#include <stdint.h>

typedef unsigned short bf16_t;
typedef short bf16x8 __attribute__((ext_vector_type(8)));
typedef short bf16x4 __attribute__((ext_vector_type(4)));
typedef float f32x4 __attribute__((ext_vector_type(4)));
typedef float f32x2 __attribute__((ext_vector_type(2)));
typedef float f32x16 __attribute__((ext_vector_type(16)));
typedef unsigned u32x4 __attribute__((ext_vector_type(4)));
typedef unsigned u32x2 __attribute__((ext_vector_type(2)));
#define LAS __attribute__((address_space(3)))
#define DEVI __device__ __forceinline__
#define MAP1 0
#define MAP3 0

constexpr int S_ = 2048, D_ = 576, F_ = 4608, NH_ = 32, DH_ = 18;
constexpr float LN_EPS = 1e-5f;

DEVI float bf2f(unsigned b) { return __uint_as_float(b << 16); }
DEVI float bflo(unsigned w) { return __uint_as_float(w << 16); }
DEVI float bfhi(unsigned w) { return __uint_as_float(w & 0xffff0000u); }
DEVI unsigned pk2(float lo, float hi) {
  typedef __bf16 bf2v __attribute__((ext_vector_type(2)));
  bf2v v; v.x = (__bf16)lo; v.y = (__bf16)hi;
  return __builtin_bit_cast(unsigned, v);
}
template <int CTRL> DEVI float dpp(float x) { return __builtin_bit_cast(float, __builtin_amdgcn_update_dpp(0, __builtin_bit_cast(int, x), CTRL, 0xf, 0xf, true)); }
DEVI float sum8(float x) {
  x += dpp<0xB1>(x); x += dpp<0x4E>(x); x += dpp<0x141>(x); return x;
}
DEVI float rbf(float x) { return (float)((__bf16)x); }

#define TOT0()
#define TOT1()
#define RSTAMP(i)
#define RDUMP(n)
#define STAMPW(kid, i, cond)
#define STAMPK(kid, i)
#define STAMPEND(kid, i)
#define STAMP(i) STAMPK(3, i)
template <int N> struct IC { static constexpr int value = N; };
template <int A, int B, class F> __device__ __forceinline__ void static_for(F&& f) {
  if constexpr (A < B) { f(IC<A>{}); static_for<A + 1, B>(f); }
}
#define WAIT_V(n) asm volatile("s_waitcnt vmcnt(" #n ")" ::: "memory")
#define WAIT_L0() asm volatile("s_waitcnt lgkmcnt(0)" ::: "memory")
#define BAR() do { asm volatile("" ::: "memory"); __builtin_amdgcn_s_barrier(); asm volatile("" ::: "memory"); } while (0)

DEVI void glds16(const char* sbase, unsigned voff, unsigned ldsaddr) {
  asm volatile("s_mov_b32 m0, %2\n\ts_nop 0\n\tglobal_load_lds_dwordx4 %0, %1" ::"v"(voff), "s"(sbase), "s"(ldsaddr) : "memory");
}
DEVI void glds_2(const char* sb, unsigned v0, unsigned v1, unsigned la) {
  asm volatile("s_mov_b32 m0, %3\n\ts_nop 0\n\tglobal_load_lds_dwordx4 %0, %2\n\tglobal_load_lds_dwordx4 %1, %2 offset:1024"
               ::"v"(v0), "v"(v1), "s"(sb), "s"(la) : "memory");
}
DEVI void glds_2s(const char* sb, unsigned v0, unsigned v1, unsigned la) {
  asm volatile("s_mov_b32 m0, %3\n\ts_nop 0\n\tglobal_load_lds_dwordx4 %0, %2 sc1\n\tglobal_load_lds_dwordx4 %1, %2 offset:1024 sc1"
               ::"v"(v0), "v"(v1), "s"(sb), "s"(la) : "memory");
}
DEVI void glds_4s(const char* sb, unsigned v0, unsigned v1, unsigned v2, unsigned v3, unsigned la) {
  asm volatile("s_mov_b32 m0, %5\n\ts_nop 0\n\tglobal_load_lds_dwordx4 %0, %4 sc1\n\tglobal_load_lds_dwordx4 %1, %4 offset:1024 sc1\n\t"
               "global_load_lds_dwordx4 %2, %4 offset:2048 sc1\n\tglobal_load_lds_dwordx4 %3, %4 offset:3072 sc1"
               ::"v"(v0), "v"(v1), "v"(v2), "v"(v3), "s"(sb), "s"(la) : "memory");
}
DEVI void glds_1s(const char* sbase, unsigned voff, unsigned ldsaddr) {
  asm volatile("s_mov_b32 m0, %2\n\ts_nop 0\n\tglobal_load_lds_dwordx4 %0, %1 sc1" ::"v"(voff), "s"(sbase), "s"(ldsaddr) : "memory");
}
DEVI void glds_4(const char* sb, unsigned v0, unsigned v1, unsigned v2, unsigned v3, unsigned la) {
  asm volatile("s_mov_b32 m0, %5\n\ts_nop 0\n\tglobal_load_lds_dwordx4 %0, %4\n\tglobal_load_lds_dwordx4 %1, %4 offset:1024\n\t"
               "global_load_lds_dwordx4 %2, %4 offset:2048\n\tglobal_load_lds_dwordx4 %3, %4 offset:3072"
               ::"v"(v0), "v"(v1), "v"(v2), "v"(v3), "s"(sb), "s"(la) : "memory");
}
DEVI void glds_2q(const char* sb, unsigned v0, unsigned v1, unsigned v2, unsigned la) {
  asm volatile("s_mov_b32 m0, %4\n\ts_nop 0\n\tglobal_load_lds_dwordx4 %0, %3\n\tglobal_load_lds_dwordx4 %1, %3 offset:1024\n\t"
               "s_mov_b64 exec, 0xffff\n\tglobal_load_lds_dwordx4 %2, %3 offset:2048\n\ts_mov_b64 exec, -1"
               ::"v"(v0), "v"(v1), "v"(v2), "s"(sb), "s"(la) : "memory");
}
typedef unsigned long long u64x8 __attribute__((ext_vector_type(8)));
#define GAS __attribute__((address_space(1)))
DEVI u64x8 kargs_issue() { u64x8 v; asm volatile("s_load_dwordx16 %0, %1, 0x0" : "=s"(v) : "s"(__builtin_amdgcn_kernarg_segment_ptr())); __builtin_amdgcn_sched_barrier(0); return v; }
DEVI void kargs_wait(u64x8& v) { asm volatile("s_waitcnt lgkmcnt(0)" : "+s"(v)); }
DEVI unsigned apoll(GAS unsigned* p, bool act) {
  unsigned v = 0x7fff7fffu;
  if (act) asm volatile("global_atomic_or %0, %1, %2, off sc0\n\ts_waitcnt vmcnt(0)" : "=&v"(v) : "v"(p), "v"(0u) : "memory");
  return v;
}
DEVI unsigned ldsa(const LAS char* p) { return (unsigned)(size_t)p; }
DEVI bf16x8 lds_b128(const LAS char* p) { return *(const LAS bf16x8*)p; }
DEVI bf16x8 lds_tr2(const LAS char* p0, const LAS char* p1) {
  bf16x4 a = __builtin_amdgcn_ds_read_tr16_b64_v4i16((LAS bf16x4*)p0);
  bf16x4 b = __builtin_amdgcn_ds_read_tr16_b64_v4i16((LAS bf16x4*)p1);
  return __builtin_shufflevector(a, b, 0, 1, 2, 3, 4, 5, 6, 7);
}
DEVI f32x4 mfma16(bf16x8 a, bf16x8 b, f32x4 c) { return __builtin_amdgcn_mfma_f32_16x16x32_bf16(a, b, c, 0, 0, 0); }

constexpr int NCNT = 2048;
constexpr unsigned EMPTY = 0xffffffffu;
constexpr int XB_ = S_ * D_ * 2;
constexpr int O_WB = 0, O_WP = 4096, O_WOP = O_WP + 8 * 7 * 18 * 2048, O_L = O_WOP + 72 * 9 * 1024;
constexpr int XPL = S_ * 128, XPE = 8 * XPL;
constexpr int CS = 32;
constexpr int L_CNT = 0, L_XIN = 1024 * CS * 4, L_XST = L_XIN + XB_, L_OB = L_XST + 65536, L_SZ = L_OB + XB_;
constexpr int O_X1 = O_L + 2 * L_SZ, O_X1C = O_X1 + XB_, O_ST = O_X1C + XB_, O_H = O_ST + 65536, O_P = O_H + S_ * F_ * 2;

__global__ __launch_bounds__(512) void k0_prep(const bf16_t* Wqkv_, char* Wp_, const bf16_t* X_, const bf16_t* Wo_, f32x2* xstats_, bf16_t* WB_,
                                                char* OB_, char* Wop_) {
  u64x8 ka = kargs_issue();
  STAMPK(0, 0);
  if (blockIdx.x < 8) TOT0();
  const int b = blockIdx.x, tid = threadIdx.x, lane = tid & 63, wid = __builtin_amdgcn_readfirstlane(tid >> 6);
  const int m = lane & 15, g = lane >> 4;
  const int su = b * 32 + (tid >> 4);
  unsigned xo = (su >> 2) * D_ + (su & 3) * 144;
  asm volatile("" : "+v"(xo));
  kargs_wait(ka);
  const GAS bf16_t* Wqkv = (const GAS bf16_t*)ka[0]; GAS char* Wp = (GAS char*)ka[1]; const GAS bf16_t* Wo = (const GAS bf16_t*)ka[3];
  GAS f32x2* xstats = (GAS f32x2*)ka[4]; GAS bf16_t* WB = (GAS bf16_t*)ka[5]; GAS unsigned* cnt = (GAS unsigned*)(ka[5] + O_L);
  GAS char* Wop = (GAS char*)ka[7];
  const GAS bf16_t* xr = (const GAS bf16_t*)ka[2] + xo;
  const u32x4 x0 = *(const GAS u32x4*)(xr + m * 8);
  const unsigned x1 = xr[128 + m];
  {
    const __amdgpu_buffer_rsrc_t rf = __builtin_amdgcn_make_buffer_rsrc((void*)ka[6], 0, S_ * D_ * 2, 0x00020000);
    const u32x4 em = {EMPTY, EMPTY, EMPTY, EMPTY};
    __builtin_amdgcn_raw_buffer_store_b128(em, rf, b * 9216 + tid * 16, 0, 0);
    if (tid < 64) __builtin_amdgcn_raw_buffer_store_b128(em, rf, b * 9216 + 8192 + tid * 16, 0, 0);
  }
  if (b < 144) {
    const int hg = b / 18, sl = b - hg * 18;
    if (wid < 7) {
      u32x4 v[2];
#pragma unroll
      for (int j = 0; j < 2; ++j) {
        int u = wid * 2 + j, dim = m;
        if (wid == 6) { int e2 = j * 16 + m; if (e2 > 23) e2 = 23; u = e2 >> 1; dim = 16 + (e2 & 1); }
        const int ty = u >> 2;
        const int k0 = sl < 16 ? (sl >> 2) * 144 + (sl & 3) * 32 + g * 8 : ((sl - 16) * 2 + (g >> 1)) * 144 + 128 + (g & 1) * 8;
        const GAS bf16_t* p = Wqkv + (size_t)k0 * (3 * D_) + (ty == 2 ? 0 : (ty + 1) * D_) + (hg * 4 + (u & 3)) * DH_ + dim;
#pragma unroll
        for (int e = 0; e < 4; ++e) v[j][e] = (unsigned)p[(2 * e) * (3 * D_)] | ((unsigned)p[(2 * e + 1) * (3 * D_)] << 16);
      }
      GAS char* dst = Wp + (size_t)(((hg * 7 + wid) * 18 + sl) * 2) * 1024 + lane * 16;
      *(GAS u32x4*)dst = v[0]; *(GAS u32x4*)(dst + 1024) = v[1];
    } else {
      if (b < NCNT / 64) cnt[(b >> 4) * (L_SZ / 4) + ((b & 15) * 64 + lane) * CS] = 0u;
      unsigned long long pc0, pc1;
      asm volatile("s_getpc_b64 %0" : "=s"(pc0));
      asm volatile("s_getpc_b64 s[40:41]\n\ts_add_u32 s40, s40, k9_end@rel32@lo+4\n\ts_addc_u32 s41, s41, k9_end@rel32@hi+12\n\ts_mov_b64 %0, s[40:41]"
                   : "=s"(pc1) : : "s40", "s41", "scc");
      u32x4 v[4];
#pragma unroll
      for (int i = 0; i < 4; ++i) {
        const unsigned long long a = (pc0 & ~15ull) + (unsigned long long)(b >> 3) * 4096 + i * 1024 + lane * 16;
        v[i] = *(const GAS u32x4*)((pc1 > pc0 && a + 16 <= pc1) ? a : (pc0 & ~15ull));
      }
#pragma unroll
      for (int i = 0; i < 4; ++i) asm volatile("" ::"v"(v[i]));
    }
  } else if (b < 225) {
    const int f = (b - 144) * 8 + wid, sg = f / 36, cj = f - sg * 36, ct = cj / 9;
    const int C = sg * 4 + g;
    const int r0 = C < 64 ? ((C >> 3) * 4 + ((C >> 2) & 1) * 2) * DH_ + (C & 3) * 4 : (C - 64) * 72 + 16;
    const int sa = C < 64 ? 2 * D_ : DH_ * D_, sb = C < 64 ? DH_ * D_ : 2 * DH_ * D_;
    const GAS bf16_t* p = Wo + (size_t)r0 * D_ + cj * 16 + m;
    u32x4 v;
#pragma unroll
    for (int e = 0; e < 4; ++e) {
      const GAS bf16_t* q = p + (e & 1) * sa + (e >> 1) * sb;
      v[e] = (unsigned)q[0] | ((unsigned)q[D_] << 16);
    }
    *(GAS u32x4*)(Wop + (size_t)((ct * 18 + sg) * 9 + (cj - ct * 9)) * 1024 + lane * 16) = v;
  } else if (b < 234) {
    const int k = (b - 225) * 64 + (tid >> 3), sub = tid & 7;
    u32x4 v[9];
#pragma unroll
    for (int i = 0; i < 9; ++i) v[i] = *(const GAS u32x4*)(Wo + (size_t)k * D_ + (i * 8 + sub) * 8);
    float t = 0.f;
#pragma unroll
    for (int i = 0; i < 9; ++i)
#pragma unroll
      for (int e = 0; e < 4; ++e) t += bflo(v[i][e]) + bfhi(v[i][e]);
    t = sum8(t) * (1.0f / D_);
    const float hi = rbf(t);
    const int hd = k / DH_, dim = k - hd * DH_, hg = hd >> 2, hh = hd & 3;
    const int pos = dim < 16 ? (hg * 8 + (hh >> 1) * 4 + (dim >> 2)) * 8 + (hh & 1) * 4 + (dim & 3) : (64 + hg) * 8 + hh * 2 + (dim - 16);
    if (sub < 2) WB[sub * D_ + pos] = (bf16_t)(pk2(sub ? t - hi : hi, 0.f) & 0xffffu);
  }
  {
    const float x2 = bflo(x1), sh = dpp<0x150>(x2), d2 = x2 - sh;
    f32x2 s1 = {d2, 0.f}, s2 = {d2 * d2, 0.f};
#pragma unroll
    for (int e = 0; e < 4; ++e) {
      const f32x2 d = (f32x2){bflo(x0[e]), bfhi(x0[e])} - sh;
      s1 += d; s2 += d * d;
    }
    float t1 = s1.x + s1.y, t2 = s2.x + s2.y;
    t1 = sum8(t1); t2 = sum8(t2); t1 += dpp<0x140>(t1); t2 += dpp<0x140>(t2);
    GAS char* xp = (GAS char*)xstats - XB_;
    *(GAS u32x4*)(xp + ((su & 3) * 2 + (m >> 3)) * XPL + (su >> 2) * 128 + (m & 7) * 16) = x0;
    *(GAS bf16_t*)(xp + XPE + (su & 3) * (S_ * 32) + (su >> 2) * 32 + m * 2) = (bf16_t)x1;
    if (m == 0) xstats[(su & 3) * S_ + (su >> 2)] = (f32x2){sh + t1 * (1.0f / 144.0f), t2 - t1 * t1 * (1.0f / 144.0f)};
  }
  STAMPEND(0, 1);
}

constexpr int K1_XT = 80 * 128;
constexpr int K1_QS = 592;
constexpr int K2_RT = 47488, K2_RS = 164;
constexpr int K1_AUX = K2_RT + 4 * 32 * K2_RS * 4;
constexpr int K1_MEAN = K1_AUX + 768;
constexpr int K1_LDS = K1_MEAN + 320;
constexpr int K1_D = 6, K1_D0 = 2;

DEVI float mx2(float a, float b) { return __builtin_elementwise_maximum(a, b); }
template <bool MAX> DEVI void xrow2(float& a, float& b) {
  auto op = [](unsigned x, unsigned y) { return MAX ? mx2(__uint_as_float(x), __uint_as_float(y)) : __uint_as_float(x) + __uint_as_float(y); };
  auto r = __builtin_amdgcn_permlane32_swap(__float_as_uint(a), __float_as_uint(b), false, false);
  const unsigned t = __float_as_uint(op(r[0], r[1]));
  auto q = __builtin_amdgcn_permlane16_swap(t, t, false, false);
  const unsigned u = __float_as_uint(op(q[0], q[1]));
  auto w = __builtin_amdgcn_permlane32_swap(u, u, false, false);
  a = __uint_as_float(w[0]); b = __uint_as_float(w[1]);
}
DEVI unsigned nonempty(unsigned w) { return min(w, EMPTY - 1u); }

template <int OFF> DEVI void gload1(u32x4& a, unsigned va, const char* sa) {
  asm volatile("global_load_dwordx4 %0, %1, %2 offset:%3" : "=&v"(a) : "v"(va), "s"(sa), "n"(OFF) : "memory");
}

DEVI void k1_phase(LAS char* lds, const char* ws, const char* Lb, const char* Lo, int layer, int tid, int bid) {
  const unsigned lbase = ldsa(lds);
  const int lane = tid & 63, wid = __builtin_amdgcn_readfirstlane(tid >> 6);
  const int xcd = bid & 7, idx = bid >> 3;
  const int tt = (xcd >> 1) * 8 + (idx & 7), hg = (xcd & 1) * 4 + (idx >> 3);
  const int t0 = tt * 64 - 16;
  STAMPK(1, 0);
  const int g = lane >> 4, m = lane & 15, q4 = (lane >> 2) & 3, p4 = lane & 3;
  const unsigned wv = lane * 16;

  const int th = hg >> 2, ct = hg & 3;
  const int cg = wid >> 2, kq = cg ? 3 - (wid & 3) : wid & 3;
  const int sg0 = kq * 5 - (kq == 3);
  const char *w2src, *w2src4;
  const int w2st4 = cg ? 64 : 9216;
  const int tb = wid & 3, hp = wid >> 2;
  f32x4 bias[2];
  const int prow = tid >> 4, sub = tid & 15;
  int ob0, ob1, oa, kao, vao;
  unsigned yo0, yo1, yc0, yc1, mk0, mk1, w2v4;
  auto mkbias = [&]() {
    int ln = lane;
    asm volatile("" : "+v"(ln));
    const int g = ln >> 4, m = ln & 15;
#pragma unroll
    for (int bk = 0; bk < 2; ++bk)
#pragma unroll
      for (int r = 0; r < 4; ++r) {
        const int d = 16 + m - (bk * 16 + g * 4 + r);
        bias[bk][r] = ((d & 1) == 0 && d >= 0 && d <= 14 && tt * 64 + tb * 16 + m - d >= 0) ? 0.f : -1e30f;
      }
    const int obr = (tt * 2 + (tb >> 1)) * (72 * 512) + ((tb & 1) * 16 + m) * 16;
    ob0 = obr + (hg * 8 + hp * 4 + g) * 512; ob1 = obr + (64 + hg) * 512 + hp * 8;
    oa = (tt * 2 + th) * (72 * 512) + (sg0 * 4 + g) * 512 + m * 16;
    const int orow = tt * 64 + th * 32 + wid * 4 + g;
    yo0 = (ct * 2 + (m >> 3)) * XPL + orow * 128 + (m & 7) * 16; yo1 = XPE + ct * (S_ * 32) + orow * 32 + m * 2;
    const int ck = ct * 18 + m, ce = ct * 18 + 16 + (m >> 3), fr = (orow >> 8) * (18 * 16384) + ((orow >> 4) & 15) * 1024 + (orow & 15) * 16;
    yc0 = (ck >> 2) * 16384 + (ck & 3) * 256 + fr; yc1 = (ce >> 2) * 16384 + (ce & 3) * 256 + fr + (m & 7) * 2;
    kao = (tb * 16 + m) * K1_QS + hp * 96 + (g < 3 ? g : 2) * 16; vao = (tb * 16 + g * 4 + (m >> 2)) * K1_QS + (4 + hp * 2) * 48 + (m & 3) * 8;
    mk0 = g < 3 ? ~0u : 0u; mk1 = g < 2 ? ~0u : 0u;
    w2v4 = cg ? (unsigned)((m & 1) * (D_ * 2) + g * 16) : (unsigned)ln * 16u;
    w2src = ws + (O_WOP + ((ct * 18 + sg0) * 9 + cg * 5) * 1024);
    w2src4 = cg ? ws + (O_WB + sg0 * 64) : w2src + 4096;
    asm volatile("" : "+v"(bias[0]), "+v"(bias[1]));
    asm volatile("" : "+v"(ob0), "+v"(ob1), "+v"(oa), "+v"(kao), "+v"(vao), "+v"(mk0), "+v"(mk1), "+v"(w2v4), "+v"(yo0), "+v"(yo1), "+v"(yc0), "+v"(yc1));
  };
  u32x4 w2[5][5];
  auto loadB = [&](auto N) {
    constexpr int n = decltype(N)::value, st = n / 5, j = n % 5;
    if constexpr (j < 4) gload1<j * 1024>(w2[st][j], wv, w2src + st * 9216);
    else gload1<0>(w2[st][j], w2v4, w2src4 + st * w2st4);
  };

  f32x4 acc[5][2];
  LAS f32x2* ms = (LAS f32x2*)(lds + K1_AUX);
  constexpr int NB0 = 2 * K1_D;
  constexpr int NBA = 15, NBB = 20;
  if (wid < 7) {
    const char* wsrc = ws + (O_WP + (hg * 7 + wid) * 18 * 2048);
    u32x4 wf[18][2];
    auto loadW = [&](auto S, auto J) {
      constexpr int s = decltype(S)::value, j = decltype(J)::value;
      gload1<(s & 1) * 2048 + j * 1024>(wf[s][j], wv, wsrc + (s >> 1) * 4096);
    };
    static_for<0, K1_D0>([&](auto S) { loadW(S, IC<0>{}); loadW(S, IC<1>{}); });
    if (wid == 1 && lane < 36) {
#pragma unroll
      for (int i = 0; i < 2; ++i) {
        const int c = lane * 2 + i;
        *(LAS u32x4*)(lds + (c >> 3) * K1_XT + (c & 7) * 16) = (u32x4){0x3f803f80u, 0x3f803f80u, 0x3f803f80u, 0x3f803f80u};
      }
    }
#pragma unroll
    for (int i = 0; i < 5; ++i) { acc[i][0] = (f32x4){0, 0, 0, 0}; acc[i][1] = (f32x4){0, 0, 0, 0}; }
    const int xa0 = m * 128 + ((g ^ (m >> 1)) * 16);
    bf16x8 xf[2][5];
    mkbias();
    static_for<K1_D0, K1_D>([&](auto S) { loadW(S, IC<0>{}); loadW(S, IC<1>{}); });
    WAIT_L0(); BAR();
    RSTAMP(0);
#pragma unroll
    for (int i = 0; i < 5; ++i) xf[0][i] = lds_b128(lds + (xa0 + i * 2048));
    static_for<0, 18>([&](auto S) {
      constexpr int s = decltype(S)::value;
      __builtin_amdgcn_sched_barrier(0);
      if (s == 1 || s == 3 || s == 7 || s == 13) BAR();
      asm volatile("s_waitcnt vmcnt(%2)" : "+v"(wf[s][0]), "+v"(wf[s][1]) : "n"(2 * (K1_D - 1)) : "memory");
      __builtin_amdgcn_sched_barrier(0);
      static_for<0, 5>([&](auto I) {
        constexpr int i = decltype(I)::value;
        if (s < 17) xf[(s + 1) & 1][i] = lds_b128(lds + ((xa0 ^ (((s + 1) & 1) * 64)) + ((s + 1) >> 1) * K1_XT + i * 2048));
        acc[i][0] = mfma16(__builtin_bit_cast(bf16x8, wf[s][0]), xf[s & 1][i], acc[i][0]);
        if constexpr (i == 1 || i == 3) {
          if constexpr (s + K1_D < 18) loadW(IC<(s + K1_D < 18 ? s + K1_D : 0)>{}, IC<i / 2>{});
          else loadB(IC<(s + K1_D - 18) * 2 + i / 2>{});
        }
        acc[i][1] = mfma16(__builtin_bit_cast(bf16x8, wf[s][1]), xf[s & 1][i], acc[i][1]);
        __builtin_amdgcn_sched_barrier(0);
      });
    });
  } else {
    unsigned xoff[10], xofe[10];
#pragma unroll
    for (int i = 0; i < 10; ++i) {
      const int row = i * 8 + (lane >> 3), c = (lane & 7) ^ ((row >> 1) & 7);
      int grow = t0 + row; if (i < 2 && grow < 0) grow = 0;
      xoff[i] = (unsigned)(grow * 128 + c * 16 + 4096 - (i & 3) * 1024);
      xofe[i] = (unsigned)((c >> 1) * (S_ * 32) + grow * 32 + (c & 1) * 16 + 4096 - (i & 3) * 1024);
    }
    auto stage10 = [&](const char* sb, const unsigned (&xo)[10], unsigned la) {
      asm volatile("s_mov_b32 m0, %5\n\ts_mov_b32 exec_lo, 0xffffff00\n\tglobal_load_lds_dwordx4 %0, %4 sc1\n\ts_mov_b32 exec_lo, -1\n\tglobal_load_lds_dwordx4 %1, %4 offset:1024 sc1\n\t"
                   "global_load_lds_dwordx4 %2, %4 offset:2048 sc1\n\tglobal_load_lds_dwordx4 %3, %4 offset:3072 sc1"
                   ::"v"(xo[0]), "v"(xo[1]), "v"(xo[2]), "v"(xo[3]), "s"(sb), "s"(la) : "memory");
      glds_4s(sb, xo[4], xo[5], xo[6], xo[7], la + 4096);
      glds_2s(sb, xo[8], xo[9], la + 8192);
    };
    auto stageX = [&](int kt) {
      if (kt < 8) stage10(Lb + (L_XIN + kt * XPL - 4096), xoff, lbase + kt * K1_XT);
      else stage10(Lb + (L_XIN + XPE - 4096), xofe, lbase + kt * K1_XT);
    };
    if (layer) {
      const int gi = 2 * tt - 1 + (lane < 3 ? lane : 2);
      const GAS unsigned* fp = (const GAS unsigned*)(Lb + L_CNT) + (384 + (gi < 0 ? 0 : gi)) * CS;
      unsigned spins = 0;
      while (__builtin_amdgcn_ballot_w64(apoll((GAS unsigned*)fp, lane < 3) < 4u) != 0ull) {
        __builtin_amdgcn_s_sleep(1);
        if (++spins > (1u << 22)) break;
      }
    }
    stageX(0); stageX(1);
    mkbias();
    WAIT_V(10); BAR();
    stageX(2); stageX(3);
    WAIT_V(20); BAR();
    stageX(4); stageX(5); stageX(6);
    WAIT_V(30); BAR();
    f32x2 st[2][4];
#pragma unroll
    for (int i = 0; i < 2; ++i) {
      int gr = t0 + i * 64 + (i ? (lane & 15) : lane); if (gr < 0) gr = 0;
#pragma unroll
      for (int c = 0; c < 4; ++c)
        asm volatile("global_load_dwordx2 %0, %1, %2 sc1" : "=&v"(st[i][c]) : "v"(gr * 8 + c * (S_ * 8)), "s"(Lb + L_XST) : "memory");
    }
    stageX(7); stageX(8);
    WAIT_V(28); BAR();
    asm volatile("s_waitcnt vmcnt(0)" : "+v"(st[0][0]), "+v"(st[0][1]), "+v"(st[0][2]), "+v"(st[0][3]), "+v"(st[1][0]), "+v"(st[1][1]), "+v"(st[1][2]), "+v"(st[1][3]) : : "memory");
#pragma unroll
    for (int i = 0; i < 2; ++i) {
      const float mean = (st[i][0].x + st[i][1].x + st[i][2].x + st[i][3].x) * 0.25f;
      const float d0 = st[i][0].x - mean, d1 = st[i][1].x - mean, d2 = st[i][2].x - mean, d3 = st[i][3].x - mean;
      const float var = ((st[i][0].y + st[i][1].y) + (st[i][2].y + st[i][3].y) + 144.0f * ((d0 * d0 + d1 * d1) + (d2 * d2 + d3 * d3))) * (1.0f / D_);
      const float den = rbf(__builtin_amdgcn_sqrtf(rbf(rbf(var) + LN_EPS)));
      if (i == 0 || lane < 16) { ms[i * 64 + lane] = (f32x2){rbf(mean), __builtin_amdgcn_rcpf(den)}; ((LAS float*)(lds + K1_MEAN))[i * 64 + lane] = mean; }
    }
    {
      const __amdgpu_buffer_rsrc_t rf = __builtin_amdgcn_make_buffer_rsrc((void*)(Lo + L_OB), 0, S_ * D_ * 2, 0x00020000);
      const u32x4 em = {EMPTY, EMPTY, EMPTY, EMPTY};
#pragma unroll
      for (int i = 0; i < 8; ++i) __builtin_amdgcn_raw_buffer_store_b128(em, rf, tt * 2 * (72 * 512) + hg * 4096 + lane * 16, (i >> 2) * (72 * 512) + (i & 3) * 1024, 16);
      __builtin_amdgcn_raw_buffer_store_b128(em, rf, (tt * 2 + (lane >> 5)) * (72 * 512) + (64 + hg) * 512 + (lane & 31) * 16, 0, 16);
    }
    WAIT_L0(); BAR();
    static_for<0, NB0>([&](auto N) { loadB(N); });
  }
  RSTAMP(1);
  static_for<NB0, NBA>([&](auto N) { loadB(N); });
  RSTAMP(2);
  if (wid < 7) {
    f32x2 mr[5];
#pragma unroll
    for (int i = 0; i < 5; ++i) mr[i] = ms[i * 16 + m];
    f32x4 cs[2];
#pragma unroll
    for (int j = 0; j < 2; ++j)
#pragma unroll
      for (int r = 0; r < 4; ++r) cs[j][r] = dpp<0x150>(acc[0][j][r]);
    u32x2 qw[5][2];
#pragma unroll
    for (int i = 0; i < 5; ++i) {
      const float nm = -mr[i].x * mr[i].y;
      f32x2 r2 = {mr[i].y, mr[i].y};
      asm volatile("" : "+v"(r2));
      const f32x4 r4 = {r2.x, r2.y, r2.x, r2.y};
#pragma unroll
      for (int j = 0; j < 2; ++j) {
        const f32x4 v = acc[i][j] * r4 + cs[j] * nm;
        qw[i][j] = (u32x2){pk2(v[0], v[1]), pk2(v[2], v[3])};
      }
    }
    if (wid < 6) {
      LAS char* qp = lds + m * K1_QS + wid * 96 + g * 8;
#pragma unroll
      for (int i = 0; i < 5; ++i)
#pragma unroll
        for (int j = 0; j < 2; ++j) *(LAS u32x2*)(qp + i * 16 * K1_QS + j * 48) = qw[i][j];
    } else {
      LAS unsigned* qp = (LAS unsigned*)(lds + m * K1_QS + g * 96 + 32);
#pragma unroll
      for (int i = 0; i < 5; ++i) { qp[i * 4 * K1_QS] = qw[i][0][0]; qp[i * 4 * K1_QS + 12] = qw[i][0][1]; }
      if (g < 2) {
#pragma unroll
        for (int i = 0; i < 5; ++i) { qp[i * 4 * K1_QS + 96] = qw[i][1][0]; qp[i * 4 * K1_QS + 108] = qw[i][1][1]; }
      }
    }
  }
  RSTAMP(3);
  static_for<NBA, NBB>([&](auto N) { loadB(N); });
  RSTAMP(4);
  WAIT_L0(); BAR();
  RSTAMP(5);
  const __amdgpu_buffer_rsrc_t rso = __builtin_amdgcn_make_buffer_rsrc((void*)(Lb + L_OB), 0, S_ * D_ * 2, 0x00020000);
  {
    const LAS char* kp = lds + kao;
    const LAS char* va = lds + vao;
    bf16x8 qf[2], kf[2][2], vf[2][2];
#pragma unroll
    for (int a = 0; a < 2; ++a) {
      const u32x4 q = *(const LAS u32x4*)(kp + a * 48 + 16 * K1_QS + 384);
      qf[a] = __builtin_bit_cast(bf16x8, (u32x4){q[0] & mk0, q[1] & mk1, q[2] & mk1, q[3] & mk1});
      kf[a][0] = lds_b128(kp + a * 48); kf[a][1] = lds_b128(kp + a * 48 + 16 * K1_QS);
    }
#pragma unroll
    for (int a = 0; a < 2; ++a)
#pragma unroll
      for (int x = 0; x < 2; ++x) vf[a][x] = lds_tr2(va + a * 48 + x * 32, va + a * 48 + x * 32 + 16 * K1_QS);
    if (kq < 2) static_for<20, 25>([&](auto N) { loadB(N); });
    constexpr float SC = 0.23570226039551584f * 1.4426950408889634f;
    f32x4 sc[2][2], lg[2];
    float mx[2], inv[2];
#pragma unroll
    for (int a = 0; a < 2; ++a) { sc[a][0] = mfma16(kf[a][0], qf[a], bias[0]); sc[a][1] = mfma16(kf[a][1], qf[a], bias[1]); }
#pragma unroll
    for (int a = 0; a < 2; ++a) {
      lg[a] = (f32x4){mx2(sc[a][0][0], sc[a][0][1]), mx2(sc[a][0][2], sc[a][0][3]), mx2(sc[a][1][0], sc[a][1][1]), mx2(sc[a][1][2], sc[a][1][3])};
      mx[a] = mx2(mx2(mx2(lg[a][0], lg[a][1]), lg[a][2]), lg[a][3]);
    }
    xrow2<true>(mx[0], mx[1]);
#pragma unroll
    for (int a = 0; a < 2; ++a) {
      const f32x4 z = lg[a] * SC - mx[a] * SC;
#pragma unroll
      for (int r = 0; r < 4; ++r) lg[a][r] = __builtin_amdgcn_exp2f(z[r]);
      inv[a] = (lg[a][0] + lg[a][1]) + (lg[a][2] + lg[a][3]);
    }
    xrow2<false>(inv[0], inv[1]);
    u32x2 ow[2]; unsigned ox[2];
    f32x4 o0[2], o1[2];
#pragma unroll
    for (int a = 0; a < 2; ++a) {
      const float iv = __builtin_amdgcn_rcpf(inv[a]);
      const f32x2 i2 = {(m & 1) ? 0.f : iv, (m & 1) ? iv : 0.f};
      u32x4 pw;
#pragma unroll
      for (int r = 0; r < 4; ++r) { const f32x2 p = i2 * lg[a][r]; pw[r] = pk2(p.x, p.y); }
      const f32x4 z = {0, 0, 0, 0};
      o0[a] = mfma16(vf[a][0], __builtin_bit_cast(bf16x8, pw), z); o1[a] = mfma16(vf[a][1], __builtin_bit_cast(bf16x8, pw), z);
    }
#pragma unroll
    for (int a = 0; a < 2; ++a) {
      ow[a] = (u32x2){nonempty(pk2(o0[a][0], o0[a][1])), nonempty(pk2(o0[a][2], o0[a][3]))};
      ox[a] = nonempty(pk2(o1[a][0], o1[a][1]));
    }
    __builtin_amdgcn_raw_buffer_store_b128((u32x4){ow[0][0], ow[0][1], ow[1][0], ow[1][1]}, rso, ob0, 0, 16);
    if (g == 0) __builtin_amdgcn_raw_buffer_store_b64((u32x2){ox[0], ox[1]}, rso, ob1, 0, 16);
  }
  RSTAMP(6);
  u32x4 rv; unsigned rv1;
  asm volatile("global_load_dwordx4 %0, %2, %4 sc1\n\tglobal_load_ushort %1, %3, %4 sc1" : "=&v"(rv), "=&v"(rv1)
               : "v"(yo0), "v"(yo1), "s"(Lb + L_XIN) : "memory");
  {
    const int o4 = kq < 2 ? 4 * 2048 : 3 * 2048;
    f32x4 ac[2][5];
    unsigned spins = 0;
    WAIT_V(2);
#pragma unroll
    for (int t = 0; t < 5; ++t) asm volatile("" : "+v"(w2[t][0]), "+v"(w2[t][1]), "+v"(w2[t][2]), "+v"(w2[t][3]), "+v"(w2[t][4]));
    __builtin_amdgcn_s_sleep(8);
    RSTAMP(7);
    while (true) {
      asm volatile("" ::: "memory");
      u32x4 xf[5][2];
#pragma unroll
      for (int t = 0; t < 5; ++t)
#pragma unroll
        for (int i = 0; i < 2; ++i) {
          xf[t][i] = __builtin_amdgcn_raw_buffer_load_b128(rso, oa + i * 256, t < 4 ? t * 2048 : o4, 16);
          __builtin_amdgcn_sched_barrier(0);
        }
      unsigned mxw = 0;
#pragma unroll
      for (int t = 0; t < 5; ++t)
#pragma unroll
        for (int i = 0; i < 2; ++i) {
          mxw = max(max(mxw, max(xf[t][i][0], xf[t][i][1])), max(xf[t][i][2], xf[t][i][3]));
          asm volatile("" : "+v"(mxw));
          if (t < 4 || kq < 2) {
#pragma unroll
            for (int j = 0; j < 5; ++j)
              ac[i][j] = mfma16(__builtin_bit_cast(bf16x8, w2[t][j]), __builtin_bit_cast(bf16x8, xf[t][i]), t ? ac[i][j] : (f32x4){0, 0, 0, 0});
          }
          __builtin_amdgcn_sched_barrier(0);
        }
      if (__builtin_amdgcn_ballot_w64(mxw == EMPTY) == 0ull || ++spins > (1u << 18)) break;
    }
    asm volatile("" : "+v"(rv), "+v"(rv1));
    RSTAMP(8);
    RSTAMP(9);
    LAS float* rt = (LAS float*)(lds + K2_RT) + (kq * 32 + m) * K2_RS + cg * 80 + g * 4;
#pragma unroll
    for (int i = 0; i < 2; ++i)
#pragma unroll
      for (int j = 0; j < 5; ++j) *(LAS f32x4*)(rt + i * 16 * K2_RS + j * 16) = ac[i][j];
  }
  WAIT_L0(); BAR();
  RSTAMP(10);
  {
    const LAS float* rt = (const LAS float*)(lds + K2_RT) + prow * K2_RS;
    f32x2 dm = *(const LAS f32x2*)(rt + 144);
#pragma unroll
    for (int k = 1; k < 4; ++k) dm += *(const LAS f32x2*)(rt + k * 32 * K2_RS + 144);
    const float mean1 = rbf(((const LAS float*)(lds + K1_MEAN))[16 + th * 32 + prow] + (dm.x + dm.y));
    const LAS float* p0 = rt + sub * 8;
    const f32x4 a0 = (*(const LAS f32x4*)p0 + *(const LAS f32x4*)(p0 + 32 * K2_RS)) + (*(const LAS f32x4*)(p0 + 64 * K2_RS) + *(const LAS f32x4*)(p0 + 96 * K2_RS));
    const f32x4 a1 = (*(const LAS f32x4*)(p0 + 4) + *(const LAS f32x4*)(p0 + 32 * K2_RS + 4)) + (*(const LAS f32x4*)(p0 + 64 * K2_RS + 4) + *(const LAS f32x4*)(p0 + 96 * K2_RS + 4));
    const LAS float* p1 = rt + 128 + sub;
    const float a2 = (p1[0] + p1[32 * K2_RS]) + (p1[64 * K2_RS] + p1[96 * K2_RS]);
    const u32x4 y = {pk2(a0[0], a0[1]), pk2(a0[2], a0[3]), pk2(a1[0], a1[1]), pk2(a1[2], a1[3])};
    u32x4 xo, xc;
    const float x2 = rbf(bflo(rv1) + rbf(a2)), d2 = x2 - mean1;
    f32x2 s1 = {d2, 0.f}, s2 = {d2 * d2, 0.f};
#pragma unroll
    for (int e = 0; e < 4; ++e) {
      xo[e] = pk2(bflo(rv[e]) + bflo(y[e]), bfhi(rv[e]) + bfhi(y[e]));
      const f32x2 d = (f32x2){bflo(xo[e]), bfhi(xo[e])} - mean1;
      xc[e] = pk2(d.x, d.y);
      s1 += d; s2 += d * d;
    }
    const __amdgpu_buffer_rsrc_t ry = __builtin_amdgcn_make_buffer_rsrc((void*)(ws + O_X1), 0, 2 * XB_ + 65536, 0x00020000);
    __builtin_amdgcn_raw_buffer_store_b128(xc, ry, yc0, XB_, 16);
    __builtin_amdgcn_raw_buffer_store_b16((bf16_t)(pk2(d2, 0.f) & 0xffffu), ry, yc1, XB_, 16);
    __builtin_amdgcn_raw_buffer_store_b128(xo, ry, yo0, 0, 16);
    __builtin_amdgcn_raw_buffer_store_b16((bf16_t)(__float_as_uint(x2) >> 16), ry, yo1, 0, 16);
    float t1 = s1.x + s1.y, t2 = s2.x + s2.y;
    t1 = sum8(t1); t2 = sum8(t2); t1 += dpp<0x140>(t1); t2 += dpp<0x140>(t2);
    const f32x2 st = {mean1 + t1 * (1.0f / 144.0f), t2 - t1 * t1 * (1.0f / 144.0f)};
    if (sub == 0) __builtin_amdgcn_raw_buffer_store_b64(__builtin_bit_cast(u32x2, st), ry, (ct * S_ + tt * 64 + th * 32 + prow) * 8, 2 * XB_, 16);
  }
  RSTAMP(11);
  STAMPK(1, 1);
}

#define WGFLAG 1
#define EPIBAR 1
constexpr int WSL = 18432;
constexpr int K3_LDS = 8 * WSL + 256;
constexpr int FRS = 16384, HT_ = 73728;
constexpr int H8_ = 128 * FRS;
constexpr int PB_ = 32 * 64 * 9 * 1024;
constexpr int WR_ = 6;
#define HAUX 16
#define TS 2
#define TI 2
#define QA 9
#define TPK 0
#define ASIG 3
#define ELAG 3
constexpr int tail_n(int q) { return q < 16 ? (int)((0x7654765321043210ull >> (4 * q)) & 15) : q < 32 ? (q >> 3) * 4 + (q & 3) : 16 + (q & 1); }
constexpr int tail_s(int q) { return q < 16 ? (0xf1e0 >> q) & 1 : q < 32 ? (q >> 2) & 1 : (q >> 1) & 1; }
#define QB 63
constexpr int K4_P0 = 108, K4_NU = 18 - K4_P0 / 9;
constexpr int item_u(int q) { return q < K4_P0 ? q / 9 : K4_P0 / 9 + (q - K4_P0) % K4_NU; }
constexpr int item_j(int q) { return q < K4_P0 ? q % 9 : (q - K4_P0) / K4_NU; }
constexpr int slot4(int t) { return (int)((0x410237654ull >> (4 * t)) & 15); }

DEVI void gload2s(u32x4& a, u32x4& b, unsigned va, const char* sa) {
  asm volatile("global_load_dwordx4 %0, %2, %3 sc1\n\tglobal_load_dwordx4 %1, %2, %3 offset:1024 sc1" : "=&v"(a), "=&v"(b) : "v"(va), "s"(sa) : "memory");
}
#define HLD "sc1"
DEVI void gload2h(u32x4& a, u32x4& b, unsigned va, const char* sa) {
  asm volatile("global_load_dwordx4 %0, %2, %3 " HLD "\n\tglobal_load_dwordx4 %1, %2, %3 offset:1024 " HLD : "=&v"(a), "=&v"(b) : "v"(va), "s"(sa) : "memory");
}
DEVI void gload1s(u32x4& a, unsigned va, const char* sa) {
  asm volatile("global_load_dwordx4 %0, %1, %2 " HLD : "=&v"(a) : "v"(va), "s"(sa) : "memory");
}
DEVI void gload8s(f32x2& a, unsigned va, const char* sa) {
  asm volatile("global_load_dwordx2 %0, %1, %2 sc1" : "=&v"(a) : "v"(va), "s"(sa) : "memory");
}
template <int N> DEVI void vwait2(u32x4& a, u32x4& b) { asm volatile("s_waitcnt vmcnt(%2)" : "+v"(a), "+v"(b) : "n"(N) : "memory"); }
DEVI void vwait0(unsigned& a) { asm volatile("s_waitcnt vmcnt(0)" : "+v"(a) : : "memory"); }
template <int SEL> DEVI void glds_2qc(int sel, const char* sb, unsigned v0, unsigned v1, unsigned v2, unsigned la) {
  asm volatile("s_cmp_eq_u32 %5, %6\n\ts_cbranch_scc0 1f\n\ts_mov_b32 m0, %4\n\ts_nop 0\n\tglobal_load_lds_dwordx4 %0, %3\n\tglobal_load_lds_dwordx4 %1, %3 offset:1024\n\t"
               "s_mov_b64 exec, 0xffff\n\tglobal_load_lds_dwordx4 %2, %3 offset:2048\n\ts_mov_b64 exec, -1\n1:"
               ::"v"(v0), "v"(v1), "v"(v2), "s"(sb), "s"(la), "s"(sel), "n"(SEL) : "memory", "scc");
}
DEVI void mfma_ip(f32x4& c, bf16x8 a, u32x4 b) { asm volatile("v_mfma_f32_16x16x32_bf16 %0, %1, %2, %0" : "+v"(c) : "v"(a), "v"(b)); }
DEVI void mfma_z(f32x4& c, bf16x8 a, u32x4 b) { asm volatile("v_mfma_f32_16x16x32_bf16 %0, %1, %2, 0" : "=v"(c) : "v"(a), "v"(b)); }
DEVI void pin2(f32x2& a, f32x2& b) { asm volatile("" : "+v"(a), "+v"(b)); }
DEVI void pin(f32x4& a, f32x4& b) { asm volatile("" : "+v"(a), "+v"(b)); }
template <int SEL> DEVI void touch(int sel, const char* sb, unsigned v, unsigned la) {
  asm volatile("s_cmp_eq_u32 %3, %4\n\ts_cbranch_scc0 1f\n\ts_mov_b32 m0, %2\n\ts_nop 0\n\tglobal_load_lds_dword %0, %1\n1:"
               ::"v"(v), "s"(sb), "s"(la), "s"(sel), "n"(SEL) : "memory", "scc");
}
DEVI void gflag(unsigned& a, const char* sa, int wid) {
  asm volatile("s_cmp_eq_u32 %3, 1\n\ts_cbranch_scc0 1f\n\ts_mov_b64 exec, 1\n\tglobal_atomic_or %0, %1, %1, %2 sc0\n\ts_mov_b64 exec, -1\n1:" : "=&v"(a) : "v"(0), "s"(sa), "s"(wid) : "memory", "scc");
}

DEVI void k3_body(LAS char* lds, const char* X, const char* wsrc, const char* H, const unsigned (&woff)[3], unsigned wdst,
                  int lane, int wid, int pm, int pf, const char* Wd, GAS unsigned* __restrict__ cnt) {
  const int split = pf >> 2, ct = pf & 3, hi = wid >> 2;
  const int g = lane >> 4, m = lane & 15, q4 = (lane >> 2) & 3, p4 = lane & 3;
  const unsigned xoff = (unsigned)(wid * 2048 + lane * 16);
  const char* ST = X + (O_ST - O_X1C);
  X += pm * (18 * FRS);
  const int wa = (4 * g + q4) * 288 + p4 * 8;
  u32x4 xf[4][2];
  f32x2 st[2];
#pragma unroll
  for (int i = 0; i < 2; ++i) gload8s(st[i], (unsigned)((g * S_ + pm * 256 + wid * 32 + i * 16 + m) * 8), ST);
  static_for<0, 3>([&](auto S) { gload2s(xf[decltype(S)::value][0], xf[decltype(S)::value][1], xoff, X + decltype(S)::value * FRS); });
  unsigned w4off[3];
#pragma unroll
  for (int i = 0; i < 3; ++i) {
    int ci = i * 64 + lane; if (ci > 143) ci = 143;
    const int rr = ci / 18, c = ci - rr * 18;
    w4off[i] = (unsigned)((rr * D_ + ct * 144 + c * 8) * 2 + 2048 - i * 1024);
  }
  auto peer = [&](int t) { return split * 4 + ((ct + (t < 2 ? 0 : t < 5 ? t - 1 : t - 4)) & 3); };
  auto stage4 = [&](int t, int sl) {
    const int row = t < 8 ? peer(t) * 144 + (t == 1 || t >= 5 ? 64 : 0) + wid * 8 : (split * 4 + (wid >> 1)) * 144 + 128 + (wid & 1) * 8;
    glds_2q(Wd + row * (D_ * 2) - 2048, w4off[0], w4off[1], w4off[2], wdst + sl * WSL);
  };
  auto rdW = [&](const LAS char* p) -> bf16x8 {
    const bf16x4 a = __builtin_amdgcn_ds_read_tr16_b64_v4i16((LAS bf16x4*)p);
    __builtin_amdgcn_sched_barrier(0);
    const bf16x4 b = __builtin_amdgcn_ds_read_tr16_b64_v4i16((LAS bf16x4*)(p + 16 * 288));
    __builtin_amdgcn_sched_barrier(0);
    return __builtin_shufflevector(a, b, 0, 1, 2, 3, 4, 5, 6, 7);
  };
  u32x4 hx[5][2];
  GAS unsigned* hc = cnt + (512 + pm * 8 + split) * CS;
  {
  f32x4 acc[2][18];
#pragma unroll
  for (int n = 0; n < 18; ++n) { acc[0][n] = (f32x4){0, 0, 0, 0}; acc[1][n] = (f32x4){0, 0, 0, 0}; pin(acc[0][n], acc[1][n]); }
  bf16x8 wf[WR_];
  auto wp = [&](int s, int n) { return lds + (wa + (s & 3) * WSL + (n & 1) * 9216 + (n >> 1) * 32); };
  asm volatile("s_waitcnt vmcnt(4)" : "+v"(st[0]), "+v"(st[1]) : : "memory");
  BAR();
  STAMP(1);
#pragma unroll
  for (int n = 0; n < WR_ - 1; ++n) wf[n] = rdW(wp(0, n));
  const char* wn = wsrc + 3 * (32 * F_ * 2);
  const char* xn = X + 3 * FRS;
  const unsigned pfo = (unsigned)(((pf >> 3) * 10368 + ((pf & 7) * 4 + (pm & 3)) * 324 + min(wid * 64 + lane, 323)) * 128);
  auto step3 = [&](auto S, int it) {
    constexpr int s = decltype(S)::value;
    vwait2<s == 2 ? 8 : 5>(xf[s][0], xf[s][1]);
    BAR();
    __builtin_amdgcn_sched_barrier(0);
    static_for<0, 18>([&](auto N) {
      constexpr int n = decltype(N)::value, n2 = n + WR_ - 1;
      if constexpr (n == 0 || n == 9) glds_2qc<n / 9>(hi, wn, woff[0], woff[1], woff[2], wdst + ((s + 3) & 3) * WSL);
      if (n == 4) gload2s(xf[(s + 3) & 3][0], xf[(s + 3) & 3][1], xoff, xn);
      if (n == 15 && s == TS) touch<TI>(it, Wd, pfo, ldsa(lds) + 8 * WSL);
      if (n == 13) {
        if (s == 1) stage4(it, 4 + it);
        const bool more = s != 2 || it < 3;
        wn += more ? 32 * F_ * 2 : 0; xn += more ? FRS : 0;
      }
      const LAS char* p = wp(s + n2 / 18, n2 % 18);
      mfma_ip(acc[0][n], wf[n % WR_], xf[s][0]);
      const bf16x4 w0 = __builtin_amdgcn_ds_read_tr16_b64_v4i16((LAS bf16x4*)p);
      __builtin_amdgcn_sched_barrier(0);
      mfma_ip(acc[1][n], wf[n % WR_], xf[s][1]);
      const bf16x4 w1 = __builtin_amdgcn_ds_read_tr16_b64_v4i16((LAS bf16x4*)(p + 16 * 288));
      wf[n2 % WR_] = __builtin_shufflevector(w0, w1, 0, 1, 2, 3, 4, 5, 6, 7);
      __builtin_amdgcn_sched_barrier(0);
    });
  };
#pragma unroll 1
  for (int it = 0; it < 4; ++it) { step3(IC<0>{}, it); step3(IC<1>{}, it); step3(IC<2>{}, it); step3(IC<3>{}, it); }
  STAMP(2);
  vwait2<5>(xf[0][0], xf[0][1]); vwait2<5>(xf[1][0], xf[1][1]);
  BAR();
  f32x2 c1v[2], inv[2];
  const __amdgpu_buffer_rsrc_t rsh = __builtin_amdgcn_make_buffer_rsrc((void*)H, 0, S_ * F_ * 2, 0x00020000);
  auto epi = [&](auto SP) {
    constexpr int sp = decltype(SP)::value;
#pragma unroll
    for (int i = 0; i < 2; ++i) {
#pragma unroll
      for (int b = 0; b < (sp < 4 ? 2 : 1); ++b)
#pragma unroll
        for (int r = 0; r < 2; ++r) {
          const int n = 4 * sp + 2 * b;
          const f32x2 ag = {acc[i][n][2 * r], acc[i][n][2 * r + 1]}, au = {acc[i][n + 1][2 * r], acc[i][n + 1][2 * r + 1]};
          unsigned hw;
          if (sp < TPK) {
            float hv[2];
#pragma unroll
            for (int e = 0; e < 2; ++e) {
              hv[e] = (ag[e] * au[e]) * __builtin_amdgcn_rcpf(__builtin_fmaf(__builtin_amdgcn_exp2f(ag[e] * c1v[i].x), inv[i].x, inv[i].x));
              asm("" : "+v"(hv[e]));
            }
            hw = pk2(hv[0], hv[1]);
          } else {
            const f32x2 z = ag * c1v[i];
            const f32x2 e = {__builtin_amdgcn_exp2f(z.x), __builtin_amdgcn_exp2f(z.y)};
            const f32x2 d = e * inv[i] + inv[i];
            const f32x2 rc = {__builtin_amdgcn_rcpf(d.x), __builtin_amdgcn_rcpf(d.y)};
            const f32x2 hv = (ag * au) * rc;
            hw = pk2(hv.x, hv.y);
          }
          if (sp < 4) hx[sp][i][2 * b + r] = hw; else hx[4][0][2 * i + r] = hw;
        }
      if (sp < 4) __builtin_amdgcn_raw_buffer_store_b128(hx[sp][i], rsh, xoff + i * 1024, (pm * 32 + pf) * HT_ + sp * FRS, HAUX);
      else if (i) __builtin_amdgcn_raw_buffer_store_b128(hx[4][0], rsh, wid * 1024 + lane * 16, (pm * 32 + pf) * HT_ + 4 * FRS, HAUX);
    }
    __builtin_amdgcn_sched_barrier(0);
    if constexpr (sp == ASIG) {
      vwait2<2 * (ASIG - 1)>(xf[2][0], xf[2][1]);
      if (lane == 0) __hip_atomic_fetch_add(hc, 1u, __ATOMIC_RELAXED, __HIP_MEMORY_SCOPE_AGENT);
      __builtin_amdgcn_sched_barrier(0);
    }
  };
  static_for<0, 36>([&](auto Q) {
    constexpr int q = decltype(Q)::value, q2 = q + WR_ - 1, n = tail_n(q), s = tail_s(q);
    if constexpr (q == 0) stage4(4, slot4(4));
    if constexpr (q == 4) stage4(5, slot4(5));
    if constexpr (q == 2) {
      float mean[2] = {st[0].x, st[1].x};
      xrow2<false>(mean[0], mean[1]);
      float m2[2];
#pragma unroll
      for (int i = 0; i < 2; ++i) { const float d = st[i].x - mean[i] * 0.25f; m2[i] = st[i].y + 144.0f * (d * d); asm("" : "+v"(m2[i])); }
      xrow2<false>(m2[0], m2[1]);
#pragma unroll
      for (int i = 0; i < 2; ++i) {
        const float den = rbf(__builtin_amdgcn_sqrtf(rbf(rbf(m2[i] * (1.0f / D_)) + LN_EPS)));
        const float c1 = -1.4426950408889634f * __builtin_amdgcn_rcpf(den);
        c1v[i] = (f32x2){c1, c1}; inv[i] = (f32x2){den * den, den * den};
        pin2(c1v[i], inv[i]);
      }
    }
    if constexpr (q >= 8 + ELAG && q % 8 == ELAG % 8) epi(IC<(q - ELAG) / 8 - 1>{});
    __builtin_amdgcn_sched_barrier(0);
    bf16x4 w0, w1;
    mfma_ip(acc[0][n], wf[q % WR_], xf[s][0]);
    if constexpr (q2 < 36) w0 = __builtin_amdgcn_ds_read_tr16_b64_v4i16((LAS bf16x4*)wp(tail_s(q2), tail_n(q2)));
    __builtin_amdgcn_sched_barrier(0);
    mfma_ip(acc[1][n], wf[q % WR_], xf[s][1]);
    if constexpr (q2 < 36) {
      w1 = __builtin_amdgcn_ds_read_tr16_b64_v4i16((LAS bf16x4*)(wp(tail_s(q2), tail_n(q2)) + 16 * 288));
      wf[q2 % WR_] = __builtin_shufflevector(w0, w1, 0, 1, 2, 3, 4, 5, 6, 7);
    }
    __builtin_amdgcn_sched_barrier(0);
  });
  asm volatile("s_nop 15");
  __builtin_amdgcn_sched_barrier(0);
  static_for<(35 - ELAG) / 8, 5>([&](auto SP) { epi(SP); });
  STAMP(3);
  }
  auto poll = [&](unsigned f, unsigned sh, unsigned want) {
    unsigned spins = 0;
    while ((((unsigned)__builtin_amdgcn_readfirstlane(f) >> sh) & 0xffffu) < want) {
      __builtin_amdgcn_s_sleep(1);
      f = apoll(hc, lane == 0);
      if (++spins > (1u << 22)) break;
    }
  };
  unsigned fl;
  if (QA == 0 && wid == 1) { gflag(fl, (const char*)hc, wid); vwait0(fl); poll(fl, 0, 32); }
  BAR();
  STAMPK(4, 0);
  f32x4 acc[2][9];
  u32x4 xt[18][2];
#pragma unroll
  for (int q = 0; q < 4; ++q) { xt[q][0] = hx[q][0]; xt[q][1] = hx[q][1]; }
  auto loadX = [&](auto U) {
    constexpr int u = decltype(U)::value, t = u / 2;
    if constexpr (t < 8) gload2h(xt[u][0], xt[u][1], xoff, H + ((pm * 32 + peer(t)) * HT_ + ((t >= 5 ? 2 : 0) + (u & 1)) * FRS));
    else {
#pragma unroll
      for (int q = 0; q < 2; ++q) gload1s(xt[u][q], (unsigned)(wid * 1024 + lane * 16), H + ((pm * 32 + split * 4 + (u & 1) * 2 + q) * HT_ + 4 * FRS));
    }
  };
  const __amdgpu_buffer_rsrc_t rs = __builtin_amdgcn_make_buffer_rsrc((void*)(H + (size_t)S_ * F_ * 2), 0, wid == split ? 0 : PB_, 0x00020000);
  const int tile = pm * 4 + ct;
  const int pofs = ((tile * 8 + wid) * 8 + split) * 9 * 1024 + lane * 16;
  auto publish = [&](int j) {
    const u32x4 v = {pk2(acc[0][j][0], acc[0][j][1]), pk2(acc[0][j][2], acc[0][j][3]), pk2(acc[1][j][0], acc[1][j][1]), pk2(acc[1][j][2], acc[1][j][3])};
    __builtin_amdgcn_raw_buffer_store_b128(v, rs, pofs + j * 1024, 0, 16);
  };
  bf16x8 wf[WR_];
  auto wp = [&](int q) { return lds + (wa + slot4(item_u(q) >> 1) * WSL + (item_u(q) & 1) * 9216 + item_j(q) * 32); };
#pragma unroll
  for (int n = 0; n < WR_ - 1; ++n) wf[n] = rdW(wp(n));
  asm volatile("s_nop 3");
  static_for<0, 162>([&](auto Q) {
    constexpr int q = decltype(Q)::value, q2 = q + WR_ - 1, u = item_u(q), j = item_j(q);
    if constexpr (q == QA) {
      if (QA) {
        if (wid == 1) { gflag(fl, (const char*)hc, wid); vwait0(fl); poll(fl, 0, 32); }
        BAR();
      }
      loadX(IC<4>{}); loadX(IC<5>{});
      STAMPK(4, 1);
    }
    if constexpr (q == 36) {
      STAMPK(4, 2);
      vwait2<16>(xt[4][0], xt[4][1]);
      BAR();
      if (wid == 2 && lane == 0) __hip_atomic_fetch_add(hc, 65536u, __ATOMIC_RELAXED, __HIP_MEMORY_SCOPE_AGENT);
      STAMPK(4, 3);
    }
    if constexpr (q == 45) vwait2<17>(xt[5][0], xt[5][1]);
    if constexpr (q == 54) { vwait2<15>(xt[6][0], xt[6][1]); gflag(fl, (const char*)hc, wid); }
    if constexpr (q == QB) {
      vwait0(fl);
      static_for<7, 10>([&](auto U) { vwait2<0>(xt[decltype(U)::value][0], xt[decltype(U)::value][1]); });
      if (wid == 1) poll(fl, 16, 4);
      BAR();
    }
    if constexpr (q == 90) { vwait2<14>(xt[10][0], xt[10][1]); STAMPW(6, 0, wid == 0); }
    if constexpr (q == 99) vwait2<12>(xt[11][0], xt[11][1]);
    if constexpr (q == K4_P0) {
      static_for<12, 18>([&](auto U) { vwait2<0>(xt[decltype(U)::value][0], xt[decltype(U)::value][1]); });
#pragma unroll
      for (int k = 16; k < 18; ++k) {
        const u32x4 a = xt[k][0], b = xt[k][1];
        xt[k][0] = (u32x4){a[0], a[1], b[0], b[1]}; xt[k][1] = (u32x4){a[2], a[3], b[2], b[3]};
        vwait2<0>(xt[k][0], xt[k][1]);
      }
    }
    if constexpr (q >= QA + 3 && q <= QA + 12 && (q - QA) % 3 == 0) loadX(IC<5 + (q - QA) / 3>{});
    if constexpr (q == QA + 15) stage4(6, slot4(6));
    if constexpr (q == QA + 17) stage4(7, slot4(7));
    if constexpr (q == 38) stage4(8, slot4(8));
    if constexpr (q >= QB && q <= QB + 21 && (q - QB) % 3 == 0) loadX(IC<10 + (q - QB) / 3>{});
    if constexpr (q >= K4_P0 + K4_NU + 2 && (q - K4_P0 - 2) % K4_NU == 0) publish((q - K4_P0 - 2) / K4_NU - 1);
    __builtin_amdgcn_sched_barrier(0);
    bf16x4 w0, w1;
    if constexpr (u == 0) mfma_z(acc[0][j], wf[q % WR_], xt[u][0]); else mfma_ip(acc[0][j], wf[q % WR_], xt[u][0]);
    if constexpr (q2 < 162) w0 = __builtin_amdgcn_ds_read_tr16_b64_v4i16((LAS bf16x4*)wp(q2));
    __builtin_amdgcn_sched_barrier(0);
    if constexpr (u == 0) mfma_z(acc[1][j], wf[q % WR_], xt[u][1]); else mfma_ip(acc[1][j], wf[q % WR_], xt[u][1]);
    if constexpr (q2 < 162) {
      w1 = __builtin_amdgcn_ds_read_tr16_b64_v4i16((LAS bf16x4*)(wp(q2) + 16 * 288));
      wf[q2 % WR_] = __builtin_shufflevector(w0, w1, 0, 1, 2, 3, 4, 5, 6, 7);
    }
    __builtin_amdgcn_sched_barrier(0);
  });
  asm volatile("s_nop 15");
  __builtin_amdgcn_sched_barrier(0);
  publish(8);
  STAMPK(4, 4);
  STAMPW(6, 1, wid == 0); STAMPW(6, 2, wid == (split == 7 ? 6 : 7));
  if (wid != split) {
    WAIT_V(0);
    STAMPW(6, 3, wid == (split == 7 ? 6 : 7));
    if (lane == 0) __hip_atomic_fetch_add(cnt + (tile * 8 + wid) * CS, 1u, __ATOMIC_RELAXED, __HIP_MEMORY_SCOPE_AGENT);
  } else {
    LAS f32x4* own = (LAS f32x4*)(lds + slot4(1) * WSL);
#pragma unroll
    for (int i = 0; i < 2; ++i)
#pragma unroll
      for (int j = 0; j < 9; ++j) own[(i * 9 + j) * 64 + lane] = acc[i][j];
    unsigned spins = 0;
    while ((unsigned)__builtin_amdgcn_readfirstlane(apoll(cnt + (tile * 8 + split) * CS, lane == 0)) < 7u) {
      __builtin_amdgcn_s_sleep(1);
      if (++spins > (1u << 22)) break;
    }
    STAMPW(6, 4, true);
  }
}

typedef unsigned long long u64x4 __attribute__((ext_vector_type(4)));
template <int OFF> DEVI unsigned long long karg1() {
  unsigned long long v;
  asm volatile("s_load_dwordx2 %0, %1, %2\n\ts_waitcnt lgkmcnt(0)" : "=s"(v) : "s"(__builtin_amdgcn_kernarg_segment_ptr()), "n"(OFF) : "memory");
  return v;
}
__global__ __launch_bounds__(512, 2) void k_main(char* ws_, const bf16_t* Wg_, const bf16_t* Wu_, const bf16_t* Wd_, bf16_t* out_) {
  __shared__ __attribute__((aligned(16))) char lds_[K3_LDS];
  LAS char* lds = (LAS char*)lds_;
  const unsigned lbase = ldsa(lds);
#pragma unroll 1
  for (int layer = 0; layer < 2; ++layer) {
  int tid = threadIdx.x, bid = blockIdx.x;
  asm volatile("" : "+v"(tid), "+s"(bid));
  {
    const char* ws = (const char*)karg1<0>();
    k1_phase(lds, ws, ws + (O_L + layer * L_SZ), ws + (O_L + (1 - layer) * L_SZ), layer, tid, bid);
  }
  asm volatile("" : "+v"(tid), "+s"(bid));
  u64x4 ka;
  asm volatile("s_load_dwordx8 %0, %1, 0x0" : "=s"(ka) : "s"(__builtin_amdgcn_kernarg_segment_ptr()));
  const int lane = tid & 63, wid = __builtin_amdgcn_readfirstlane(tid >> 6);
  const int xcd = bid & 7, idx = bid >> 3;
  const int pm = (xcd >> 2) * 4 + (idx & 3), pf = (xcd & 3) * 8 + (idx >> 2);
  unsigned woff[3];
#pragma unroll
  for (int i = 0; i < 3; ++i) {
    int ci = i * 64 + lane; if (ci > 143) ci = 143;
    const int rr = ci / 18, cp = ci - rr * 18;
    const int rp = (wid & 3) * 8 + rr;
    woff[i] = (unsigned)(((8 * ((rp & 15) >> 2) + 4 * (rp >> 4) + (rp & 3)) * F_ + pf * 144 + cp * 8) * 2 - i * 1024);
  }
  const unsigned wdst = lbase + wid * 2304;
  asm volatile("" : "+v"(woff[0]), "+v"(woff[1]), "+v"(woff[2]));
  asm volatile("s_waitcnt lgkmcnt(0)" : "+s"(ka));
  const char* Lb = (const char*)ka[0] + (O_L + layer * L_SZ);
  const char* X = (const char*)ka[0] + O_X1C;
  const char* wsrc = (const char*)((wid >> 2) ? ka[2] : ka[1]);
  GAS unsigned* cnt = (GAS unsigned*)(Lb + L_CNT);
  WAIT_V(0); BAR();
  if (tid == 0) __hip_atomic_fetch_add(cnt + (320 + (MAP1 ? xcd : (xcd >> 1) * 2 + ((idx >> 2) & 1))) * CS, 1u, __ATOMIC_RELAXED, __HIP_MEMORY_SCOPE_AGENT);
  STAMP(0);
#pragma unroll
  for (int s = 0; s < 3; ++s) glds_2q(wsrc + s * (32 * F_ * 2), woff[0], woff[1], woff[2], wdst + s * WSL);
  if (wid == 7) {
    unsigned spins = 0;
    __builtin_amdgcn_s_sleep(12);
    while ((unsigned)__builtin_amdgcn_readfirstlane(__hip_atomic_load(cnt + (320 + pm) * CS, __ATOMIC_RELAXED, __HIP_MEMORY_SCOPE_AGENT)) < 32u) {
      __builtin_amdgcn_s_sleep(3);
      if (++spins > (1u << 22)) break;
    }
  }
  BAR();
  STAMPK(2, 0);
  k3_body(lds, X, wsrc, X + (O_H - O_X1C), woff, wdst, lane, wid, pm, pf, (const char*)ka[3], cnt);
  const int split = pf >> 2, ct = pf & 3, tile = pm * 4 + ct;
  const int row = tid >> 4, sub = tid & 15;
  const int grow = pm * 256 + split * 32 + row;
  const __amdgpu_buffer_rsrc_t rr = __builtin_amdgcn_make_buffer_rsrc((void*)(X - XB_), 0, XB_, 0x00020000);
  const int po0 = (ct * 2 + (sub >> 3)) * XPL + grow * 128 + (sub & 7) * 16, po1 = XPE + ct * (S_ * 32) + grow * 32 + sub * 2;
  const u32x4 rv = __builtin_amdgcn_raw_buffer_load_b128(rr, po0, 0, 16);
  const unsigned rv1 = __builtin_amdgcn_raw_buffer_load_b16(rr, po1, 0, 16);
  const unsigned long long outp = karg1<32>();
  const char* Lo = Lb + L_SZ;
  WAIT_L0(); BAR();
  STAMPK(4, 5);
  LAS f32x4* own = (LAS f32x4*)(lds + slot4(1) * WSL);
  LAS float* rt = (LAS float*)lds;
  {
    const __amdgpu_buffer_rsrc_t rs = __builtin_amdgcn_make_buffer_rsrc((void*)(X + (O_P - O_X1C)), 0, PB_, 0x00020000);
    const int pb = (tile * 8 + split) * 8 * 9 * 1024;
    const int s8 = lane >> 3, l8 = wid * 8 + (lane & 7);
    const u32x4 p8 = __builtin_amdgcn_raw_buffer_load_b128(rs, pb + ((s8 + (s8 >= split)) & 7) * 9216 + 8 * 1024 + l8 * 16, 0, 16);
    u32x4 pv[7];
#pragma unroll
    for (int sx = 0; sx < 7; ++sx) pv[sx] = __builtin_amdgcn_raw_buffer_load_b128(rs, pb + wid * 1024 + lane * 16, (sx + (sx >= split)) * 9216, 16);
    *(LAS u32x4*)(lds + 20480 + s8 * 1024 + l8 * 16) = p8;
#pragma unroll
    for (int i = 0; i < 2; ++i) {
      f32x4 v = own[(i * 9 + wid) * 64 + lane];
#pragma unroll
      for (int sx = 0; sx < 7; ++sx) v += (f32x4){bflo(pv[sx][2 * i]), bfhi(pv[sx][2 * i]), bflo(pv[sx][2 * i + 1]), bfhi(pv[sx][2 * i + 1])};
      *(LAS f32x4*)(rt + (i * 16 + (lane & 15)) * 148 + wid * 16 + (lane >> 4) * 4) = v;
    }
  }
  WAIT_L0(); BAR();
  {
    const LAS float* p0 = rt + row * 148;
    const f32x4 a0 = *(const LAS f32x4*)(p0 + sub * 8), a1 = *(const LAS f32x4*)(p0 + sub * 8 + 4);
    const int l8 = (sub >> 2) * 16 + (row & 15);
    float a2 = ((const LAS float*)own)[(((row >> 4) * 9 + 8) * 64 + l8) * 4 + (sub & 3)];
#pragma unroll
    for (int sx = 0; sx < 7; ++sx) a2 += bf2f(*(const LAS bf16_t*)(lds + 20480 + sx * 1024 + l8 * 16 + (row >> 4) * 8 + (sub & 3) * 2));
    const u32x4 y = {pk2(a0[0], a0[1]), pk2(a0[2], a0[3]), pk2(a1[0], a1[1]), pk2(a1[2], a1[3])};
    const float x2 = rbf(bflo(rv1) + rbf(a2));
    const float sh = dpp<0x150>(x2), d2 = x2 - sh;
    f32x2 s1 = {d2, 0.f}, s2 = {d2 * d2, 0.f};
    u32x4 xo;
#pragma unroll
    for (int e = 0; e < 4; ++e) {
      xo[e] = pk2(bflo(rv[e]) + bflo(y[e]), bfhi(rv[e]) + bfhi(y[e]));
      const f32x2 d = (f32x2){bflo(xo[e]), bfhi(xo[e])} - sh;
      s1 += d; s2 += d * d;
    }
    if (layer) {
      GAS bf16_t* Y = (GAS bf16_t*)outp + (size_t)grow * D_ + ct * 144;
      *(GAS u32x4*)(Y + sub * 8) = xo;
      Y[128 + sub] = (bf16_t)(__float_as_uint(x2) >> 16);
      break;
    }
    const __amdgpu_buffer_rsrc_t ry = __builtin_amdgcn_make_buffer_rsrc((void*)(Lo + L_XIN), 0, XB_ + 65536, 0x00020000);
    __builtin_amdgcn_raw_buffer_store_b128(xo, ry, po0, 0, 16);
    __builtin_amdgcn_raw_buffer_store_b16((bf16_t)(__float_as_uint(x2) >> 16), ry, po1, 0, 16);
    float t1 = s1.x + s1.y, t2 = s2.x + s2.y;
    t1 = sum8(t1); t2 = sum8(t2); t1 += dpp<0x140>(t1); t2 += dpp<0x140>(t2);
    const f32x2 st = {sh + t1 * (1.0f / 144.0f), t2 - t1 * t1 * (1.0f / 144.0f)};
    if (sub == 0) __builtin_amdgcn_raw_buffer_store_b64(__builtin_bit_cast(u32x2, st), ry, (ct * S_ + grow) * 8, XB_, 16);
  }
  WAIT_V(0); BAR();
  if (tid == 0) __hip_atomic_fetch_add((GAS unsigned*)(Lo + L_CNT) + (384 + (grow >> 5)) * CS, 1u, __ATOMIC_RELAXED, __HIP_MEMORY_SCOPE_AGENT);
  STAMPK(4, 6);
  }
  TOT1();
}

extern "C" __global__ void k9_end() {}

extern "C" void kernel_launch(void* const* d_in, const int* in_sizes, int n_in,
                              void* d_out, int out_size, void* d_ws, size_t ws_size,
                              hipStream_t stream) {
  const bf16_t* x0 = (const bf16_t*)d_in[0];
  const bf16_t* wqkv = (const bf16_t*)d_in[1];
  const bf16_t* wo = (const bf16_t*)d_in[2];
  const bf16_t* wg = (const bf16_t*)d_in[4];
  const bf16_t* wu = (const bf16_t*)d_in[5];
  const bf16_t* wd = (const bf16_t*)d_in[6];
  bf16_t* out = (bf16_t*)d_out;
  char* ws = (char*)d_ws;
  k0_prep<<<256, 512, 0, stream>>>(wqkv, ws + O_WP, x0, wo, (f32x2*)(ws + O_L + L_XST), (bf16_t*)(ws + O_WB), ws + O_L + L_OB, ws + O_WOP);
  k_main<<<256, 512, 0, stream>>>(ws, wg, wu, wd, out);
}
```
